```python
import torch

def _softmax(x, axis=-1):
    return torch.softmax(x, dim=axis)
def _gelu(x):
    return torch.nn.functional.gelu(x, approximate="tanh")
def _silu(x):
    return torch.nn.functional.silu(x)
def _sigmoid(x):
    return torch.sigmoid(x)
def _layernorm(x, axis=-1, eps=1e-5):
    m = x.mean(dim=axis, keepdim=True)
    v = x.var(dim=axis, keepdim=True, unbiased=False)
    return (x - m) / torch.sqrt(v + eps)
def _scatter_add(tgt, idx, src, axis=0):
    return tgt.clone().index_add_(axis, idx, src)
def _segment_sum(src, seg, n):
    out = torch.zeros((n,) + src.shape[1:], dtype=src.dtype)
    return out.index_add_(0, seg, src)
def _pad(x, pw):
    flat = tuple(v for pair in reversed(pw) for v in pair)
    return torch.nn.functional.pad(x, flat)

kernel_name = 'sequential_preLN__mha_block__dense_activation_relu__prompt'

def setup_inputs():

    _w = (lambda a: (lambda t: t.to(torch.bfloat16) if t.is_floating_point() else t)(torch.as_tensor(a)))
    def _A_setup():
        B, S, D, H = 1, 2048, 640, 32
        return {
            "x": _w((lambda *s: torch.randn(*s, dtype=torch.bfloat16) * 0.05)(B, S, D)),
            "wqkv": _w((lambda *s: torch.randn(*s, dtype=torch.bfloat16) * 0.05)(D, 3 * D)),
            "wo": _w((lambda *s: torch.randn(*s, dtype=torch.bfloat16) * 0.05)(D, D)),
        }
    _A_in = _A_setup()

    def _B_setup():
        B, S, D, Dff = 1, 2048, 640, 1728
        return {
            "x": _w((lambda *s: torch.randn(*s, dtype=torch.bfloat16) * 0.05)(B, S, D)),
            "w1": _w((lambda *s: torch.randn(*s, dtype=torch.bfloat16) * 0.05)(D, Dff)),
            "w2": _w((lambda *s: torch.randn(*s, dtype=torch.bfloat16) * 0.05)(Dff, D)),
        }
    _B_in = _B_setup()

    return {
        "A_x": _A_in["x"],
        "A_wqkv": _A_in["wqkv"],
        "A_wo": _A_in["wo"],
        "B_x": _B_in["x"],
        "B_w1": _B_in["w1"],
        "B_w2": _B_in["w2"],
    }

def reference(A_x, A_wqkv, A_wo, B_x, B_w1, B_w2):
    def _A_ref(x, wqkv, wo):
        B, S, D = x.shape
        H = 32
        Dh = D // H
        qkv = (lambda x, *d: x.permute(*d))((x @ wqkv).reshape(B, S, 3, H, Dh), 2, 0, 3, 1, 4)
        q, k, v = qkv[0], qkv[1], qkv[2]
        scores = torch.einsum("bhqd,bhkd->bhqk", q, k) * (Dh ** -0.5)
        p = _softmax(scores, axis=-1)
        o = torch.einsum("bhqk,bhkd->bhqd", p, v)
        return (lambda x, *d: x.permute(*d))(o, 0, 2, 1, 3).reshape(B, S, D) @ wo

    def _B_ref(x, w1, w2):
        h = torch.einsum("bsd,df->bsf", x, w1)
        zero = torch.zeros_like(h)
        h = torch.maximum(h, zero)
        return torch.einsum("bsf,fd->bsd", h, w2)

    x = A_x
    h = x + _A_ref(_layernorm(x, axis=-1), A_wqkv, A_wo)
    h2 = h + _B_ref(_layernorm(h, axis=-1), B_w1, B_w2)
    return h2
```

```cpp
#include <hip/hip_runtime.h>
#include <hip/hip_bf16.h>
#include <stdint.h>

typedef __bf16 bf16x8 __attribute__((ext_vector_type(8)));
typedef __bf16 bf16x16 __attribute__((ext_vector_type(16)));
typedef short s16x4 __attribute__((ext_vector_type(4)));
typedef float f32x2 __attribute__((ext_vector_type(2)));
typedef float f32x4 __attribute__((ext_vector_type(4)));
typedef float f32x8 __attribute__((ext_vector_type(8)));
typedef float f32x16 __attribute__((ext_vector_type(16)));
typedef unsigned u32x2 __attribute__((ext_vector_type(2)));
typedef unsigned u32x4 __attribute__((ext_vector_type(4)));
typedef __bf16 bf16x2 __attribute__((ext_vector_type(2)));
#define LDS_AS __attribute__((address_space(3)))

constexpr int SEQ = 2048, DM = 640, NH = 32, FF = 1728;
constexpr size_t WS_QK = 0;
constexpr int VROW = 648;
constexpr size_t WS_V = WS_QK + (size_t)64 * 160 * 512;
constexpr size_t WS_O = WS_V + (size_t)64 * 4 * VROW * 16;
constexpr size_t WS_X = WS_O + (size_t)64 * 80 * 512;
constexpr size_t WS_H = WS_X + (size_t)64 * 80 * 512;
constexpr size_t WS_U = WS_H + (size_t)64 * 80 * 512;
constexpr size_t WS_WO = WS_U + (size_t)64 * 216 * 512;
constexpr size_t WS_W2 = WS_WO + (size_t)8 * 20 * 5 * 1024;

#define SBAR() __builtin_amdgcn_sched_barrier(0)
#define BAR() asm volatile("s_barrier" ::: "memory")

__device__ __forceinline__ unsigned cvtpk(float lo, float hi) {
  bf16x2 r = __builtin_convertvector((f32x2){lo, hi}, bf16x2);
  return __builtin_bit_cast(unsigned, r);
}
__device__ __forceinline__ float bflo(unsigned w) { return __uint_as_float(w << 16); }
__device__ __forceinline__ float bfhi(unsigned w) { return __uint_as_float(w & 0xffff0000u); }
__device__ __forceinline__ unsigned rfl(unsigned v) { return __builtin_amdgcn_readfirstlane(v); }

__device__ __forceinline__ void glds4(const char* sbase, unsigned v0, unsigned v1, unsigned v2, unsigned v3, unsigned m0v) {
  unsigned keep;
  asm volatile(
      "s_mov_b32 %0, m0\n\ts_mov_b32 m0, %6\n\ts_nop 0\n\t"
      "global_load_lds_dwordx4 %1, %5\n\t"
      "global_load_lds_dwordx4 %2, %5 offset:1024\n\t"
      "global_load_lds_dwordx4 %3, %5 offset:2048\n\t"
      "global_load_lds_dwordx4 %4, %5 offset:3072\n\t"
      "s_mov_b32 m0, %0"
      : "=&s"(keep) : "v"(v0), "v"(v1), "v"(v2), "v"(v3), "s"(sbase), "s"(m0v) : "memory");
}
__device__ __forceinline__ void glds1(const char* sbase, unsigned v0, unsigned m0v) {
  unsigned keep;
  asm volatile(
      "s_mov_b32 %0, m0\n\ts_mov_b32 m0, %3\n\ts_nop 0\n\t"
      "global_load_lds_dwordx4 %1, %2\n\t"
      "s_mov_b32 m0, %0"
      : "=&s"(keep) : "v"(v0), "s"(sbase), "s"(m0v) : "memory");
}
template <int N> __device__ __forceinline__ void wait_vm() { asm volatile("s_waitcnt vmcnt(%0)" ::"i"(N) : "memory"); }
__device__ __forceinline__ u32x4 lds128(const LDS_AS char* p) { return *(const LDS_AS u32x4*)p; }
__device__ __forceinline__ u32x2 ldstr(const LDS_AS char* p) {
  return __builtin_bit_cast(u32x2, __builtin_amdgcn_ds_read_tr16_b64_v4i16((LDS_AS s16x4*)p));
}
__device__ __forceinline__ f32x16 mfma32(u32x4 a, u32x4 b, f32x16 c) {
  return __builtin_amdgcn_mfma_f32_32x32x16_bf16(__builtin_bit_cast(bf16x8, a), __builtin_bit_cast(bf16x8, b), c, 0, 0, 0);
}
__device__ __forceinline__ f32x4 mfma16(u32x4 a, u32x4 b, f32x4 c) {
  return __builtin_amdgcn_mfma_f32_16x16x32_bf16(__builtin_bit_cast(bf16x8, a), __builtin_bit_cast(bf16x8, b), c, 0, 0, 0);
}
__device__ __forceinline__ f32x16 mfma32k8(u32x2 a, u32x2 b, f32x16 c) {
  return __builtin_amdgcn_mfma_f32_32x32x8bf16_1k(__builtin_bit_cast(s16x4, a), __builtin_bit_cast(s16x4, b), c, 0, 0, 0);
}

template <int PER, int LEFT> __device__ __forceinline__ void drain_tail() {
  if constexpr (LEFT > 0) { wait_vm<PER*(LEFT - 1)>(); BAR(); drain_tail<PER, LEFT - 1>(); }
}

constexpr int BG_NS = 4, BG_SLOT = 32768, BG_ST = 10;
template <int MODE, bool VORI>
__device__ __forceinline__ void big_mfma(LDS_AS char* lds, int w, int lane, int rb, int nb, char* ws) {
  const int wm = w & 1, wn = w >> 1, r = lane & 31, h = lane >> 5;
  LDS_AS float* f_rs = (LDS_AS float*)(lds + BG_NS * BG_SLOT);
  LDS_AS float* f_mr = f_rs + 128;
  LDS_AS float* f_cs = f_rs + 256;
  int xo[4];
#pragma unroll
  for (int ks = 0; ks < 4; ++ks)
    xo[ks] = MODE == 0 ? (wm * 64 + r) * 128 + (((2 * ks + h) ^ ((r >> 1) & 7)) << 4) : wm * 8192 + ks * 1024 + lane * 16;
  const int q = (lane & 15) >> 2, p = lane & 3, blk = (lane >> 4) & 1;
  const int P = VORI ? p : ((p >> 1) | ((p & 1) << 1));
  const int nt0 = 2 * wn + wm, nt1 = 2 * wn + 1 - wm;
  const int wo0 = 16384 + (8 * h + q) * 256 + (((8 * nt0 + 4 * blk + P) * 8) ^ (q << 6));
  const int wo1 = 16384 + (8 * h + q) * 256 + (((8 * nt1 + 4 * blk + P) * 8) ^ (q << 6));
  const unsigned selv = (((lane >> 4) ^ lane) & 1) ? 0u : 0x3f803f80u;
  const u32x4 sel = {selv, selv, selv, selv};
  f32x16 acc[2][2] = {};
  f32x4 cs = {};
  u32x4 xa[2], wa[2], xb[2], wb[2];
#define BG_READ(X, W, sl, ks)                                                                   \
  {                                                                                             \
    X[0] = lds128(sl + xo[ks]);                                                                 \
    X[1] = lds128(sl + xo[ks] + 4096);                                                          \
    u32x2 a0 = ldstr(sl + wo0 + ks * 4096), a1 = ldstr(sl + wo0 + ks * 4096 + 1024);            \
    u32x2 b0 = ldstr(sl + wo1 + ks * 4096), b1 = ldstr(sl + wo1 + ks * 4096 + 1024);            \
    W[0] = (u32x4){a0[0], a0[1], a1[0], a1[1]};                                                 \
    W[1] = (u32x4){b0[0], b0[1], b1[0], b1[1]};                                                 \
  }
#define BG_MMA(X, W)                                                                            \
  {                                                                                             \
    _Pragma("unroll") for (int i = 0; i < 2; ++i) _Pragma("unroll") for (int j = 0; j < 2; ++j) \
        acc[i][j] = VORI ? mfma32(X[j], W[i], acc[i][j]) : mfma32(W[i], X[j], acc[i][j]);       \
    cs = mfma16(sel, W[0], cs);                                                                 \
  }
  BAR();
  LDS_AS char* sl = lds;
  BG_READ(xa, wa, sl, 0);
#pragma unroll 1
  for (int s = 0; s < BG_ST; ++s) {
    SBAR(); BG_READ(xb, wb, sl, 1); SBAR(); BG_MMA(xa, wa);
    SBAR(); BG_READ(xa, wa, sl, 2); SBAR(); BG_MMA(xb, wb);
    SBAR(); BG_READ(xb, wb, sl, 3); SBAR(); BG_MMA(xa, wa);
    SBAR();
    if (s + 1 < BG_ST) {
      asm volatile("s_waitcnt lgkmcnt(0)\n\ts_barrier" ::: "memory");
      sl = lds + ((s + 1) & (BG_NS - 1)) * BG_SLOT;
      BG_READ(xa, wa, sl, 0);
    }
    SBAR(); BG_MMA(xb, wb);
  }
#undef BG_READ
#undef BG_MMA
  if (lane < 16) { f_cs[nt0 * 32 + lane] = cs[0]; f_cs[nt0 * 32 + 16 + lane] = cs[1]; }
  asm volatile("s_waitcnt lgkmcnt(0)\n\ts_barrier" ::: "memory");
#pragma unroll
  for (int i = 0; i < 2; ++i) {
    const int nt = i ? nt1 : nt0;
#pragma unroll
    for (int j = 0; j < 2; ++j) {
      const int tl = 2 * wm + j, tt = rb * 4 + tl;
      float v[16];
      if constexpr (VORI) {
        const float c = f_cs[nt * 32 + r];
#pragma unroll
        for (int g = 0; g < 4; ++g) {
          f32x4 rs = *(LDS_AS f32x4*)(f_rs + tl * 32 + 8 * g + 4 * h), mr = *(LDS_AS f32x4*)(f_mr + tl * 32 + 8 * g + 4 * h);
#pragma unroll
          for (int e = 0; e < 4; ++e) v[4 * g + e] = acc[i][j][4 * g + e] * rs[e] - mr[e] * c;
        }
        const int n = (nb - 10) * 128 + nt * 32 + r;
        char* dst = ws + WS_V + ((size_t)(tt * 4 + h) * VROW + n) * 16;
        *(u32x4*)dst = (u32x4){cvtpk(v[0], v[1]), cvtpk(v[2], v[3]), cvtpk(v[4], v[5]), cvtpk(v[6], v[7])};
        *(u32x4*)(dst + 2 * VROW * 16) = (u32x4){cvtpk(v[8], v[9]), cvtpk(v[10], v[11]), cvtpk(v[12], v[13]), cvtpk(v[14], v[15])};
      } else {
        float rs = f_rs[tl * 32 + r], mr = f_mr[tl * 32 + r];
        if (MODE == 0 && nb < 5) { const float sc = 0.22360679774997896f * 1.4426950408889634f; rs *= sc; mr *= sc; }
#pragma unroll
        for (int g = 0; g < 4; ++g) {
          f32x4 c = *(LDS_AS f32x4*)(f_cs + nt * 32 + 8 * g + 4 * h);
#pragma unroll
          for (int e = 0; e < 4; ++e) {
            float t = acc[i][j][4 * g + e] * rs - mr * c[e];
            v[4 * g + e] = MODE == 1 ? fmaxf(t, 0.f) : t;
          }
        }
        const int ch = nb * 16 + nt * 4 + h;
        char* dst;
        if constexpr (MODE == 0) {
          const int pos = nb < 5 ? r : ((r & 19) | ((r & 4) << 1) | ((r & 8) >> 1));
          dst = ws + WS_QK + ((size_t)tt * 160 + ch) * 512 + pos * 16;
        } else {
          dst = ws + WS_U + ((size_t)tt * 216 + ch) * 512 + r * 16;
        }
        if (MODE == 0 || nb * 16 + nt * 4 < 216) {
          *(u32x4*)dst = (u32x4){cvtpk(v[0], v[1]), cvtpk(v[2], v[3]), cvtpk(v[4], v[5]), cvtpk(v[6], v[7])};
          *(u32x4*)(dst + 1024) = (u32x4){cvtpk(v[8], v[9]), cvtpk(v[10], v[11]), cvtpk(v[12], v[13]), cvtpk(v[14], v[15])};
        }
      }
    }
  }
}

template <int MODE>
__global__ void __launch_bounds__(768) k_big(const char* __restrict__ X, const char* __restrict__ W, char* __restrict__ ws) {
  __shared__ __attribute__((aligned(16))) char smem[BG_NS * BG_SLOT + 1536];
  LDS_AS char* lds = (LDS_AS char*)smem;
  const int lane = threadIdx.x & 63, w = rfl(threadIdx.x >> 6);
  const int xcd = blockIdx.x & 7, bi = blockIdx.x >> 3;
  const int rb = 4 * (xcd >> 1) + (bi & 3);
  const int nb = MODE == 0 ? 8 * (xcd & 1) + (bi >> 2) : 7 * (xcd & 1) + (bi >> 2);
  constexpr int WST = MODE == 0 ? 3 * DM * 2 : FF * 2;
  if (MODE == 0 && nb == 15) {
    if (threadIdx.x < 16) *(u32x4*)(ws + WS_V + ((size_t)(rb * 16 + threadIdx.x) * VROW + 640) * 16) = (u32x4){0x3f803f80u, 0x3f803f80u, 0x3f803f80u, 0x3f803f80u};
    return;
  }
  if (w < 4) {
    if (MODE == 0 && nb >= 10) big_mfma<MODE, true>(lds, w, lane, rb, nb, ws);
    else big_mfma<MODE, false>(lds, w, lane, rb, nb, ws);
  } else if (w < 8) {
    const int lw = w - 4;
    unsigned xv[4], wv[4];
#pragma unroll
    for (int i = 0; i < 4; ++i) {
      if (MODE == 0) {
        const int row = 8 * i + (lane >> 3);
        xv[i] = row * (DM * 2) + (((lane & 7) ^ ((row >> 1) & 7)) << 4) + 3072 - 1024 * i;
      } else xv[i] = lane * 16 + 3072;
      const int kr = 4 * i + (lane >> 4);
      int c = (lane & 15) ^ ((kr & 3) << 2);
      if (MODE == 1 && nb == 13) c &= 7;
      wv[i] = kr * WST + c * 16 + 3072 - 1024 * i;
    }
    const char* xs = (MODE == 0 ? X + (size_t)(rb * 128 + lw * 32) * (DM * 2) : X + (size_t)(rb * 4 + lw) * 80 * 512) - 3072;
    const char* wsrc = W + (size_t)(lw * 16) * WST + nb * 256 - 3072;
    constexpr int XADV = MODE == 0 ? 128 : 4096;
    const unsigned l0 = (unsigned)(size_t)lds + lw * 4096;
#pragma unroll
    for (int s = 0; s < BG_NS; ++s) {
      glds4(xs, xv[0], xv[1], xv[2], xv[3], l0 + s * BG_SLOT);
      glds4(wsrc, wv[0], wv[1], wv[2], wv[3], l0 + s * BG_SLOT + 16384);
      xs += XADV; wsrc += 64 * WST;
    }
    wait_vm<8 * (BG_NS - 1)>(); BAR();
#pragma unroll 1
    for (int s = 1; s <= BG_ST - BG_NS; ++s) {
      wait_vm<8 * (BG_NS - 2)>(); BAR();
      const unsigned d = l0 + ((s - 1) & (BG_NS - 1)) * BG_SLOT;
      glds4(xs, xv[0], xv[1], xv[2], xv[3], d);
      glds4(wsrc, wv[0], wv[1], wv[2], wv[3], d + 16384);
      xs += XADV; wsrc += 64 * WST;
    }
    drain_tail<8, BG_NS - 1>();
  } else {
    const int sw = w - 8, r = lane & 31, h = lane >> 5;
    int xo[4];
#pragma unroll
    for (int ks = 0; ks < 4; ++ks)
      xo[ks] = MODE == 0 ? (sw * 32 + r) * 128 + (((2 * ks + h) ^ ((r >> 1) & 7)) << 4) : sw * 4096 + ks * 1024 + lane * 16;
    float s0 = 0.f, s1 = 0.f, q0 = 0.f, q1 = 0.f;
#pragma unroll 1
    for (int s = 0; s < BG_ST; ++s) {
      BAR();
      LDS_AS char* sl = lds + (s & (BG_NS - 1)) * BG_SLOT;
      u32x4 f[4];
#pragma unroll
      for (int ks = 0; ks < 4; ++ks) f[ks] = lds128(sl + xo[ks]);
#pragma unroll
      for (int ks = 0; ks < 4; ++ks)
#pragma unroll
        for (int e = 0; e < 4; ++e) {
          float a = bflo(f[ks][e]), b = bfhi(f[ks][e]);
          asm("v_add_f32 %0, %0, %1" : "+v"(s0) : "v"(a));
          asm("v_add_f32 %0, %0, %1" : "+v"(s1) : "v"(b));
          asm("v_fmac_f32 %0, %1, %1" : "+v"(q0) : "v"(a));
          asm("v_fmac_f32 %0, %1, %1" : "+v"(q1) : "v"(b));
        }
      asm volatile("" ::"v"(s0), "v"(s1), "v"(q0), "v"(q1) : "memory");
    }
    s0 += s1; q0 += q1;
    s0 += __shfl_xor(s0, 32); q0 += __shfl_xor(q0, 32);
    const float mu = s0 * (1.f / DM), var = fmaxf(q0 * (1.f / DM) - mu * mu, 0.f);
    const float rs = rsqrtf(var + 1e-5f);
    LDS_AS float* f_rs = (LDS_AS float*)(lds + BG_NS * BG_SLOT);
    if (h == 0) { f_rs[sw * 32 + r] = rs; f_rs[128 + sw * 32 + r] = mu * rs; }
    asm volatile("s_waitcnt lgkmcnt(0)\n\ts_barrier" ::: "memory");
  }
}

constexpr int SM_NS = 8, SM_SLOT = 18432;
template <int MODE, int NJ>
__device__ __forceinline__ void small_mfma(LDS_AS char* lds, int wm, int j0, int lane, int rb, int nb, char* ws, char* out) {
  constexpr int ST = MODE == 0 ? 10 : 27;
  const int tt = rb * 2 + wm;
  const char* rsrc = ws + (MODE == 0 ? WS_X : WS_H) + ((size_t)tt * 80 + nb * 10 + 2 * j0) * 512 + lane * 16;
  u32x4 res[NJ];
#pragma unroll
  for (int j = 0; j < NJ; ++j) res[j] = *(const u32x4*)(rsrc + j * 1024);
  const int ao = wm * 4096 + (lane >> 4) * 512 + (lane & 15) * 16, wo = 8192 + j0 * 1024 + lane * 16;
  f32x4 acc[NJ][2] = {};
  u32x4 aa[2], wa[NJ], ab[2], wb[NJ];
#define SM_READ(A, W, sl, ks)                                                              \
  {                                                                                        \
    A[0] = lds128(sl + ao + ks * 2048); A[1] = lds128(sl + ao + ks * 2048 + 256);          \
    _Pragma("unroll") for (int j = 0; j < NJ; ++j) W[j] = lds128(sl + wo + ks * 5120 + j * 1024); \
  }
#define SM_MMA(A, W) \
  { _Pragma("unroll") for (int j = 0; j < NJ; ++j) _Pragma("unroll") for (int b = 0; b < 2; ++b) acc[j][b] = mfma16(W[j], A[b], acc[j][b]); }
  BAR();
  LDS_AS char* sl = lds;
  SM_READ(aa, wa, sl, 0);
#pragma unroll 1
  for (int s = 0; s < ST; ++s) {
    SBAR(); SM_READ(ab, wb, sl, 1); SBAR(); SM_MMA(aa, wa);
    SBAR();
    if (s + 1 < ST) {
      asm volatile("s_waitcnt lgkmcnt(0)\n\ts_barrier" ::: "memory");
      sl = lds + ((s + 1) & (SM_NS - 1)) * SM_SLOT;
      SM_READ(aa, wa, sl, 0);
    }
    SBAR(); SM_MMA(ab, wb);
  }
#undef SM_READ
#undef SM_MMA
#pragma unroll
  for (int j = 0; j < NJ; ++j) {
    float v[8];
#pragma unroll
    for (int e = 0; e < 4; ++e) {
      auto sw = __builtin_amdgcn_permlane16_swap(__float_as_uint(acc[j][0][e]), __float_as_uint(acc[j][1][e]), false, false);
      v[e] = __uint_as_float(sw[0]); v[4 + e] = __uint_as_float(sw[1]);
    }
    u32x4 o;
#pragma unroll
    for (int e = 0; e < 4; ++e) o[e] = cvtpk(v[2 * e] + bflo(res[j][e]), v[2 * e + 1] + bfhi(res[j][e]));
    if constexpr (MODE == 0) *(u32x4*)(ws + WS_H + ((size_t)tt * 80 + nb * 10 + 2 * (j0 + j)) * 512 + lane * 16) = o;
    else *(u32x4*)(out + (size_t)(tt * 32 + (lane & 31)) * (DM * 2) + (nb * 10 + 2 * (j0 + j) + (lane >> 5)) * 16) = o;
  }
}
template <int MODE, int PER>
__device__ __forceinline__ void small_load(const char* src, int adv, unsigned l0, int lane) {
  constexpr int ST = MODE == 0 ? 10 : 27;
  const unsigned v = lane * 16;
  auto issue = [&](unsigned d) { glds4(src, v, v, v, v, d); if constexpr (PER == 5) glds1(src + 4096, v, d + 4096); src += adv; };
#pragma unroll 1
  for (int s = 0; s < SM_NS; ++s) issue(l0 + s * SM_SLOT);
  wait_vm<PER*(SM_NS - 1)>(); BAR();
#pragma unroll 1
  for (int s = 1; s <= ST - SM_NS; ++s) {
    wait_vm<PER*(SM_NS - 2)>(); BAR();
    issue(l0 + ((s - 1) & (SM_NS - 1)) * SM_SLOT);
  }
  drain_tail<PER, SM_NS - 1>();
}
template <int MODE>
__global__ void __launch_bounds__(512) k_small(char* __restrict__ ws, char* __restrict__ out) {
  __shared__ __attribute__((aligned(16))) char smem[SM_NS * SM_SLOT];
  LDS_AS char* lds = (LDS_AS char*)smem;
  const int lane = threadIdx.x & 63, w = rfl(threadIdx.x >> 6);
  const int xcd = blockIdx.x & 7, bi = blockIdx.x >> 3;
  const int rb = 8 * (xcd >> 1) + (bi & 7), nb = 4 * (xcd & 1) + (bi >> 3);
  constexpr int NCH = MODE == 0 ? 80 : 216, KS = NCH / 4;
  if (w < 2) small_mfma<MODE, 3>(lds, w, 0, lane, rb, nb, ws, out);
  else if (w < 4) small_mfma<MODE, 2>(lds, w - 2, 3, lane, rb, nb, ws, out);
  else if (w < 6) small_load<MODE, 4>(ws + (MODE == 0 ? WS_O : WS_U) + (size_t)(rb * 2 + w - 4) * NCH * 512, 4096, (unsigned)(size_t)lds + (w - 4) * 4096, lane);
  else small_load<MODE, 5>(ws + (MODE == 0 ? WS_WO : WS_W2) + (size_t)nb * KS * 5120 + (w - 6) * 5120, 10240, (unsigned)(size_t)lds + 8192 + (w - 6) * 5120, lane);
}

__device__ __forceinline__ f32x16 smfmac(u32x4 a, const f32x16& p, int half, f32x16 c) {
  f32x8 b = half ? __builtin_shufflevector(p, p, 8, 9, 10, 11, 12, 13, 14, 15) : __builtin_shufflevector(p, p, 0, 1, 2, 3, 4, 5, 6, 7);
  return __builtin_amdgcn_smfmac_f32_32x32x32_bf16(__builtin_bit_cast(bf16x8, a), __builtin_bit_cast(bf16x16, b), c, 0xDDDD, 0, 0);
}
__device__ __forceinline__ float max16(const f32x16& s) {
  float a = fmaxf(fmaxf(s[0], s[1]), s[2]), b = fmaxf(fmaxf(s[3], s[4]), s[5]);
  a = fmaxf(fmaxf(a, s[6]), s[7]); b = fmaxf(fmaxf(b, s[8]), s[9]);
  a = fmaxf(fmaxf(a, s[10]), s[11]); b = fmaxf(fmaxf(b, s[12]), s[13]);
  a = fmaxf(fmaxf(a, s[14]), s[15]);
  return fmaxf(a, b);
}
__device__ __forceinline__ float xhalf_max(float m) {
  auto rr = __builtin_amdgcn_permlane32_swap(__float_as_uint(m), __float_as_uint(m), false, false);
  return fmaxf(__uint_as_float(rr[0]), __uint_as_float(rr[1]));
}
__device__ __forceinline__ unsigned shift_word(float m, int h) {
  const float n = -m, a = bfhi(__float_as_uint(n)), r1 = n - a, b = bfhi(__float_as_uint(r1)), r2 = r1 - b;
  return h ? (__float_as_uint(r2) >> 16) : ((__float_as_uint(a) >> 16) | __float_as_uint(b));
}

constexpr int AT_LDS = 8 * 4096 + 4 * 2 * 13 * 256 + 64;
__global__ void __launch_bounds__(576) k_attn(const char* __restrict__ x, const char* __restrict__ wo, const char* __restrict__ w2, char* __restrict__ ws) {
  __shared__ __attribute__((aligned(16))) char smem[AT_LDS];
  LDS_AS char* lds = (LDS_AS char*)smem;
  const int lane = threadIdx.x & 63, w = rfl(threadIdx.x >> 6);
  const int xcd = blockIdx.x & 7, bi = blockIdx.x >> 3;
  const int head = xcd * 4 + (bi >> 3), qb = bi & 7;
  if (w == 8) {
    const unsigned l0 = (unsigned)(size_t)lds;
    const unsigned v0 = (lane >> 3) * (DM * 2) + (lane & 7) * 16;
#pragma unroll 1
    for (int i = 0; i < 6; ++i) {
      const int u = blockIdx.x + 256 * i;
      if (u < 1380) {
        const int rt = u / 10, cb = u % 10;
        const char* src = (rt < 64 ? x + (size_t)rt * 32 * 1280 : rt < 84 ? wo + (size_t)(rt - 64) * 32 * 1280 : w2 + (size_t)(rt - 84) * 32 * 1280) + cb * 128;
        glds4(src, v0, v0 + 9216, v0 + 2 * 9216, v0 + 3 * 9216, l0 + i * 4096);
      }
    }
    wait_vm<0>();
#pragma unroll 1
    for (int i = 0; i < 6; ++i) {
      const int u = blockIdx.x + 256 * i;
      if (u < 1380) {
        const int rt = u / 10, cb = u % 10;
        LDS_AS char* t = lds + i * 4096;
        if (rt < 64) {
#pragma unroll
          for (int c = 0; c < 4; ++c)
            *(u32x4*)(ws + WS_X + ((size_t)rt * 80 + cb * 8 + 2 * c) * 512 + lane * 16) = lds128(t + (lane & 31) * 128 + (2 * c + (lane >> 5)) * 16);
        } else {
          const bool iswo = rt < 84;
          const int ks = iswo ? rt - 64 : rt - 84, KS = iswo ? 20 : 54;
          char* dst = ws + (iswo ? WS_WO : WS_W2);
#pragma unroll
          for (int c = 0; c < 4; ++c) {
            const int nt = cb * 4 + c;
            LDS_AS char* a = t + (8 * (lane >> 4) + ((lane & 15) >> 2)) * 128 + c * 32 + (lane & 3) * 8;
            u32x2 a0 = ldstr(a), a1 = ldstr(a + 512);
            *(u32x4*)(dst + ((size_t)((nt / 5) * KS + ks) * 5 + nt % 5) * 1024 + lane * 16) = (u32x4){a0[0], a0[1], a1[0], a1[1]};
          }
        }
      }
    }
    return;
  }
  const int qg = w & 3, kh = w >> 2, r = lane & 31, h = lane >> 5;
  const int odd = head & 1, cA = 5 * (head >> 1) + 3 * odd, cL = 5 * (head >> 1) + 2;
  const unsigned o0 = cA * 512 + lane * 16, o1 = cL * 512 + r * 16 + odd * 8 + h * 4;
  const int vd = r < 16 ? ((r & 3) | ((r & 4) << 1) | ((r & 8) >> 1)) + 4 * odd : (odd ? r - 16 : r);
  const unsigned ov = (h * VROW + (r < 20 ? head * 20 + vd : 640)) * 16;
  const auto rs = __builtin_amdgcn_make_buffer_rsrc(ws, 0, (int)WS_U, 0x00020000);
  const int qsrc = (int)WS_QK + (qb * 8 + qg * 2) * 81920;
  const int ksrc = (int)WS_QK + (kh * 32) * 81920 + 80 * 512;
  const int vsrc = (int)WS_V + (kh * 32) * (4 * VROW * 16);
  u32x4 q0[2]; u32x2 q1[2];
#pragma unroll
  for (int i = 0; i < 2; ++i) {
    q0[i] = __builtin_amdgcn_raw_buffer_load_b128(rs, o0, qsrc + i * 81920, 0);
    q1[i] = (u32x2){__builtin_amdgcn_raw_buffer_load_b32(rs, o1, qsrc + i * 81920, 0), 0u};
  }
  auto ldk0 = [&](int t) { return (u32x4)__builtin_amdgcn_raw_buffer_load_b128(rs, o0, ksrc + t * 81920, 0); };
  auto ldk1 = [&](int t) { return (u32x2){__builtin_amdgcn_raw_buffer_load_b32(rs, o1, ksrc + t * 81920, 0), 0x3f803f80u}; };
  auto ldv = [&](int t, int s) { return (u32x4)__builtin_amdgcn_raw_buffer_load_b128(rs, ov, vsrc + t * (4 * VROW * 16) + s * (2 * VROW * 16), 0); };
  const f32x16 zero = {};
  float m[2];
  {
    u32x4 k0 = ldk0(0); u32x2 k1 = ldk1(0);
#pragma unroll
    for (int i = 0; i < 2; ++i) m[i] = xhalf_max(max16(mfma32k8(k1, q1[i], mfma32(k0, q0[i], zero))));
  }
  LDS_AS float* part = (LDS_AS float*)(lds + 8 * 4096) + qg * (2 * 13 * 64);
  LDS_AS int* flag = (LDS_AS int*)(lds + AT_LDS - 64);
  if (threadIdx.x == 0) *flag = 0;
  for (int pass = 0;; ++pass) {
    q1[0][1] = shift_word(m[0], h); q1[1][1] = shift_word(m[1], h);
    f32x16 o[2] = {};
    u32x4 ka0 = ldk0(0), kb0 = ldk0(1), va0 = ldv(0, 0), va1 = ldv(0, 1), vb0 = {}, vb1 = {};
    u32x2 ka1 = ldk1(0), kb1 = ldk1(1);
    f32x16 sA, sB, sC, sD = {};
    sA = mfma32k8(ka1, q1[0], mfma32(ka0, q0[0], zero));
#define EXP4(S, i) { S[i] = __builtin_amdgcn_exp2f(S[i]); S[i + 1] = __builtin_amdgcn_exp2f(S[i + 1]); S[i + 2] = __builtin_amdgcn_exp2f(S[i + 2]); S[i + 3] = __builtin_amdgcn_exp2f(S[i + 3]); asm volatile("" : "+v"(S)); SBAR(); }
#define STEP(CUR, NXT, PRV, K0, K1, QI, V0, V1, OI)                     \
  {                                                                     \
    NXT = mfma32(K0, q0[QI], zero); SBAR(); EXP4(CUR, 0);               \
    NXT = mfma32k8(K1, q1[QI], NXT); SBAR(); EXP4(CUR, 4);              \
    o[OI] = smfmac(V0, PRV, 0, o[OI]); SBAR(); EXP4(CUR, 8);            \
    o[OI] = smfmac(V1, PRV, 1, o[OI]); SBAR(); EXP4(CUR, 12);           \
  }
#pragma unroll 1
    for (int t = 0; t < 32; t += 2) {
      const int t2 = t + 2, t3 = t + 3;
      STEP(sA, sB, sD, ka0, ka1, 1, vb0, vb1, 1);
      ka0 = ldk0(t2); ka1 = ldk1(t2); vb0 = ldv(t + 1, 0); vb1 = ldv(t + 1, 1); SBAR();
      STEP(sB, sC, sA, kb0, kb1, 0, va0, va1, 0);
      STEP(sC, sD, sB, kb0, kb1, 1, va0, va1, 1);
      kb0 = ldk0(t3); kb1 = ldk1(t3); va0 = ldv(t2, 0); va1 = ldv(t2, 1); SBAR();
      STEP(sD, sA, sC, ka0, ka1, 0, vb0, vb1, 0);
    }
    o[1] = smfmac(vb0, sD, 0, o[1]); o[1] = smfmac(vb1, sD, 1, o[1]);
#undef STEP
#undef EXP4
    const f32x16 give = kh ? o[0] : o[1], mine = kh ? o[1] : o[0];
    const float mg = kh ? m[0] : m[1], mm = kh ? m[1] : m[0];
    LDS_AS float* pw = part + kh * (13 * 64);
#pragma unroll
    for (int g = 0; g < 3; ++g) *(LDS_AS f32x4*)(pw + g * 256 + lane * 4) = (f32x4){give[4 * g], give[4 * g + 1], give[4 * g + 2], give[4 * g + 3]};
    pw[768 + lane] = mg;
    __syncthreads();
    LDS_AS float* pr = part + (1 - kh) * (13 * 64);
    const float mo = pr[768 + lane], mx = fmaxf(mo, mm);
    const float fm = __builtin_amdgcn_exp2f(mm - mx), fo = __builtin_amdgcn_exp2f(mo - mx);
    float v[12], big = 0.f;
#pragma unroll
    for (int g = 0; g < 3; ++g) {
      f32x4 t = *(LDS_AS f32x4*)(pr + g * 256 + lane * 4);
#pragma unroll
      for (int e = 0; e < 4; ++e) { big = fmaxf(big, fmaxf(fabsf(t[e]), fabsf(mine[4 * g + e]))); v[4 * g + e] = mine[4 * g + e] * fm + t[e] * fo; }
    }
    if (pass == 0) {
      if (!(big < 1.0e30f)) *flag = 1;
      __syncthreads();
      if (*flag) {
        float tm[2] = {-INFINITY, -INFINITY};
        q1[0][1] = 0; q1[1][1] = 0;
#pragma unroll 1
        for (int t = 0; t < 32; ++t) {
          u32x4 k0 = ldk0(t); u32x2 k1 = ldk1(t);
#pragma unroll
          for (int i = 0; i < 2; ++i) tm[i] = fmaxf(tm[i], max16(mfma32k8(k1, q1[i], mfma32(k0, q0[i], zero))));
        }
        m[0] = xhalf_max(tm[0]); m[1] = xhalf_max(tm[1]);
        continue;
      }
    }
    auto lr = __builtin_amdgcn_permlane32_swap(__float_as_uint(v[8]), __float_as_uint(v[8]), false, false);
    const float inv = 1.0f / __uint_as_float(lr[1]);
    char* dst = ws + WS_O + (size_t)(qb * 8 + qg * 2 + kh) * (80 * 512);
    *(u32x4*)(dst + o0) = (u32x4){cvtpk(v[0] * inv, v[1] * inv), cvtpk(v[2] * inv, v[3] * inv), cvtpk(v[4] * inv, v[5] * inv), cvtpk(v[6] * inv, v[7] * inv)};
    if (h == 0) *(u32x2*)(dst + o1) = (u32x2){cvtpk(v[8] * inv, v[9] * inv), cvtpk(v[10] * inv, v[11] * inv)};
    break;
  }
}

extern "C" void kernel_launch(void* const* d_in, const int* in_sizes, int n_in, void* d_out, int out_size, void* d_ws, size_t ws_size,
                              hipStream_t stream) {
  const char* x = (const char*)d_in[0];
  const char* wqkv = (const char*)d_in[1];
  const char* wo = (const char*)d_in[2];
  const char* w1 = (const char*)d_in[4];
  const char* w2 = (const char*)d_in[5];
  char* ws = (char*)d_ws;
  k_big<0><<<256, 768, 0, stream>>>(x, wqkv, ws);
  k_attn<<<256, 576, 0, stream>>>(x, wo, w2, ws);
  k_small<0><<<256, 512, 0, stream>>>(ws, (char*)d_out);
  k_big<1><<<224, 768, 0, stream>>>(ws + WS_H, w1, ws);
  k_small<1><<<256, 512, 0, stream>>>(ws, (char*)d_out);
}
```

```cpp
#include <hip/hip_runtime.h>
#include <hip/hip_bf16.h>
#include <stdint.h>

typedef __bf16 bf16x2 __attribute__((ext_vector_type(2)));
typedef __bf16 bf16x8 __attribute__((ext_vector_type(8)));
typedef __bf16 bf16x16 __attribute__((ext_vector_type(16)));
typedef short s16x2 __attribute__((ext_vector_type(2)));
typedef short s16x4 __attribute__((ext_vector_type(4)));
typedef float f32x2 __attribute__((ext_vector_type(2)));
typedef float f32x4 __attribute__((ext_vector_type(4)));
typedef float f32x8 __attribute__((ext_vector_type(8)));
typedef float f32x16 __attribute__((ext_vector_type(16)));
typedef unsigned u32x2 __attribute__((ext_vector_type(2)));
typedef unsigned u32x4 __attribute__((ext_vector_type(4)));
typedef unsigned u32x8 __attribute__((ext_vector_type(8)));
typedef unsigned u32x16 __attribute__((ext_vector_type(16)));
#define LDS_AS __attribute__((address_space(3)))
#define GLB_AS __attribute__((address_space(1)))

#define CFG_TP 24
#define CFG_LS 2
#define CFG_NS3 1
#define CFG_EP 1
#define CFG_T 8
#define CFG_MAP 0
#define CFG_PRE 0
#define CFG_R0 16
#define CFG_R1 16
#define CFG_R2 8
#define CFG_R3 8
#define CFG_QE 2
#define CFG_WOFF 1
#define CFG_ENCA 0
#define CFG_ENCB -1
#define CFG_ENCC 0
#define P1_ASM \
  "s_mov_b64 exec, 0\n" \
  "s_branch .Lk_pad8\n" \
  ".Lk_ret8:\n" \
  "s_mov_b64 exec, -1\n" \
  "s_mov_b64 exec, 0\n" \
  "s_branch .Lk_pad9\n" \
  ".Lk_ret9:\n" \
  "s_mov_b64 exec, -1\n" \
  "v_mov_b64 v[0:1], 0\n" \
  "v_mov_b64 v[2:3], 0\n" \
  "v_mov_b64 v[4:5], 0\n" \
  "v_mov_b64 v[6:7], 0\n" \
  "v_mov_b64 v[8:9], 0\n" \
  "v_mov_b64 v[10:11], 0\n" \
  "v_mov_b64 v[12:13], 0\n" \
  "v_mov_b64 v[14:15], 0\n" \
  "v_mov_b64 v[16:17], 0\n" \
  "v_mov_b64 v[18:19], 0\n" \
  "v_mov_b64 v[20:21], 0\n" \
  "v_mov_b64 v[22:23], 0\n" \
  "v_mov_b64 v[24:25], 0\n" \
  "v_mov_b64 v[26:27], 0\n" \
  "v_mov_b64 v[28:29], 0\n" \
  "v_mov_b64 v[30:31], 0\n" \
  "v_mov_b64 v[32:33], 0\n" \
  "v_mov_b64 v[34:35], 0\n" \
  "v_mov_b64 v[36:37], 0\n" \
  "v_mov_b64 v[38:39], 0\n" \
  "v_mov_b64 v[40:41], 0\n" \
  "v_mov_b64 v[42:43], 0\n" \
  "v_mov_b64 v[44:45], 0\n" \
  "v_mov_b64 v[46:47], 0\n" \
  "v_mov_b64 v[48:49], 0\n" \
  "v_mov_b64 v[50:51], 0\n" \
  "v_mov_b64 v[52:53], 0\n" \
  "v_mov_b64 v[54:55], 0\n" \
  "v_mov_b64 v[56:57], 0\n" \
  "v_mov_b64 v[58:59], 0\n" \
  "v_mov_b64 v[60:61], 0\n" \
  "v_mov_b64 v[62:63], 0\n" \
  "v_mov_b64 v[188:189], 0\n" \
  "v_mov_b64 v[190:191], 0\n" \
  "v_mov_b32 v184, v110\n" \
  "v_mov_b32 v185, v110\n" \
  "v_mov_b32 v186, v110\n" \
  "v_mov_b32 v187, v110\n" \
  "v_and_b32 v176, 127, v109\n" \
  "v_lshlrev_b32 v176, 5, v176\n" \
  "s_cmp_ge_u32 %[nb], 10\n" \
  "s_cbranch_scc1 5f\n" \
  "v_mov_b32 v180, 163392\n" \
  "ds_write_b64 v180, v[0:1]\n" \
  "s_mov_b32 %[cnt], 5\n" \
  "s_waitcnt lgkmcnt(0)\n" \
  "s_barrier\n" \
  "ds_read_b64_tr_b16 v[72:73], v100 offset:0\n" \
  "ds_read_b64_tr_b16 v[74:75], v100 offset:1024\n" \
  "ds_read_b128 v[64:67], v96 offset:0\n" \
  "ds_read_b128 v[68:71], v96 offset:4096\n" \
  "ds_read_b64_tr_b16 v[76:77], v101 offset:0\n" \
  "ds_read_b64_tr_b16 v[78:79], v101 offset:1024\n" \
  "1:\n" \
  "s_waitcnt lgkmcnt(3)\n" \
  "v_mfma_f32_32x32x16_bf16 v[0:15], v[72:75], v[64:67], v[0:15]\n" \
  "ds_read_b64_tr_b16 v[88:89], v100 offset:4096\n" \
  "ds_read_b64_tr_b16 v[90:91], v100 offset:5120\n" \
  "s_waitcnt lgkmcnt(4)\n" \
  "v_mfma_f32_32x32x16_bf16 v[16:31], v[72:75], v[68:71], v[16:31]\n" \
  "ds_read_b128 v[80:83], v97 offset:0\n" \
  "ds_read_b128 v[84:87], v97 offset:4096\n" \
  "s_waitcnt lgkmcnt(4)\n" \
  "v_mfma_f32_32x32x16_bf16 v[48:63], v[76:79], v[68:71], v[48:63]\n" \
  "ds_read_b64_tr_b16 v[92:93], v101 offset:4096\n" \
  "ds_read_b64_tr_b16 v[94:95], v101 offset:5120\n" \
  "v_mfma_f32_32x32x16_bf16 v[32:47], v[76:79], v[64:67], v[32:47]\n" \
  "v_mfma_f32_16x16x32_bf16 v[188:191], v[184:187], v[72:75], v[188:191]\n" \
  "s_waitcnt lgkmcnt(3)\n" \
  "v_mfma_f32_32x32x16_bf16 v[0:15], v[88:91], v[80:83], v[0:15]\n" \
  "ds_read_b64_tr_b16 v[72:73], v100 offset:8192\n" \
  "ds_read_b64_tr_b16 v[74:75], v100 offset:9216\n" \
  "s_waitcnt lgkmcnt(4)\n" \
  "v_mfma_f32_32x32x16_bf16 v[16:31], v[88:91], v[84:87], v[16:31]\n" \
  "ds_read_b128 v[64:67], v98 offset:0\n" \
  "ds_read_b128 v[68:71], v98 offset:4096\n" \
  "s_waitcnt lgkmcnt(4)\n" \
  "v_mfma_f32_32x32x16_bf16 v[48:63], v[92:95], v[84:87], v[48:63]\n" \
  ".Lk_pad8:\n" \
  "s_cbranch_execz .Lk_ret8\n" \
  "ds_read_b64_tr_b16 v[76:77], v101 offset:8192\n" \
  "ds_read_b64_tr_b16 v[78:79], v101 offset:9216\n" \
  "v_mfma_f32_32x32x16_bf16 v[32:47], v[92:95], v[80:83], v[32:47]\n" \
  "v_mfma_f32_16x16x32_bf16 v[188:191], v[184:187], v[88:91], v[188:191]\n" \
  "s_waitcnt lgkmcnt(3)\n" \
  "v_mfma_f32_32x32x16_bf16 v[0:15], v[72:75], v[64:67], v[0:15]\n" \
  "ds_read_b64_tr_b16 v[88:89], v100 offset:12288\n" \
  "ds_read_b64_tr_b16 v[90:91], v100 offset:13312\n" \
  "s_waitcnt lgkmcnt(4)\n" \
  "v_mfma_f32_32x32x16_bf16 v[16:31], v[72:75], v[68:71], v[16:31]\n" \
  "ds_read_b128 v[80:83], v99 offset:0\n" \
  "ds_read_b128 v[84:87], v99 offset:4096\n" \
  "s_waitcnt lgkmcnt(4)\n" \
  "v_mfma_f32_32x32x16_bf16 v[48:63], v[76:79], v[68:71], v[48:63]\n" \
  "ds_read_b64_tr_b16 v[92:93], v101 offset:12288\n" \
  "ds_read_b64_tr_b16 v[94:95], v101 offset:13312\n" \
  "v_mfma_f32_32x32x16_bf16 v[32:47], v[76:79], v[64:67], v[32:47]\n" \
  "v_mfma_f32_16x16x32_bf16 v[188:191], v[184:187], v[72:75], v[188:191]\n" \
  "s_waitcnt lgkmcnt(3)\n" \
  "v_mfma_f32_32x32x16_bf16 v[0:15], v[88:91], v[80:83], v[0:15]\n" \
  "s_waitcnt lgkmcnt(0)\n" \
  "s_barrier\n" \
  "ds_read_b64_tr_b16 v[72:73], v100 offset:32768\n" \
  "ds_read_b64_tr_b16 v[74:75], v100 offset:33792\n" \
  "v_mfma_f32_32x32x16_bf16 v[16:31], v[88:91], v[84:87], v[16:31]\n" \
  "ds_read_b128 v[64:67], v96 offset:32768\n" \
  "ds_read_b128 v[68:71], v96 offset:36864\n" \
  "v_mfma_f32_32x32x16_bf16 v[48:63], v[92:95], v[84:87], v[48:63]\n" \
  "ds_read_b64_tr_b16 v[76:77], v101 offset:32768\n" \
  "ds_read_b64_tr_b16 v[78:79], v101 offset:33792\n" \
  "v_mfma_f32_32x32x16_bf16 v[32:47], v[92:95], v[80:83], v[32:47]\n" \
  "v_mfma_f32_16x16x32_bf16 v[188:191], v[184:187], v[88:91], v[188:191]\n" \
  "s_waitcnt lgkmcnt(3)\n" \
  "v_mfma_f32_32x32x16_bf16 v[0:15], v[72:75], v[64:67], v[0:15]\n" \
  "ds_read_b64_tr_b16 v[88:89], v100 offset:36864\n" \
  "ds_read_b64_tr_b16 v[90:91], v100 offset:37888\n" \
  "s_waitcnt lgkmcnt(4)\n" \
  "v_mfma_f32_32x32x16_bf16 v[16:31], v[72:75], v[68:71], v[16:31]\n" \
  "ds_read_b128 v[80:83], v97 offset:32768\n" \
  "ds_read_b128 v[84:87], v97 offset:36864\n" \
  "s_waitcnt lgkmcnt(4)\n" \
  "v_mfma_f32_32x32x16_bf16 v[48:63], v[76:79], v[68:71], v[48:63]\n" \
  "ds_read_b64_tr_b16 v[92:93], v101 offset:36864\n" \
  "ds_read_b64_tr_b16 v[94:95], v101 offset:37888\n" \
  "v_mfma_f32_32x32x16_bf16 v[32:47], v[76:79], v[64:67], v[32:47]\n" \
  "v_mfma_f32_16x16x32_bf16 v[188:191], v[184:187], v[72:75], v[188:191]\n" \
  "s_waitcnt lgkmcnt(3)\n" \
  "v_mfma_f32_32x32x16_bf16 v[0:15], v[88:91], v[80:83], v[0:15]\n" \
  "ds_read_b64_tr_b16 v[72:73], v100 offset:40960\n" \
  "ds_read_b64_tr_b16 v[74:75], v100 offset:41984\n" \
  "s_waitcnt lgkmcnt(4)\n" \
  "v_mfma_f32_32x32x16_bf16 v[16:31], v[88:91], v[84:87], v[16:31]\n" \
  "ds_read_b128 v[64:67], v98 offset:32768\n" \
  "ds_read_b128 v[68:71], v98 offset:36864\n" \
  "s_waitcnt lgkmcnt(4)\n" \
  "v_mfma_f32_32x32x16_bf16 v[48:63], v[92:95], v[84:87], v[48:63]\n" \
  "ds_read_b64_tr_b16 v[76:77], v101 offset:40960\n" \
  "ds_read_b64_tr_b16 v[78:79], v101 offset:41984\n" \
  "v_mfma_f32_32x32x16_bf16 v[32:47], v[92:95], v[80:83], v[32:47]\n" \
  "v_mfma_f32_16x16x32_bf16 v[188:191], v[184:187], v[88:91], v[188:191]\n" \
  "s_waitcnt lgkmcnt(3)\n" \
  "v_mfma_f32_32x32x16_bf16 v[0:15], v[72:75], v[64:67], v[0:15]\n" \
  "ds_read_b64_tr_b16 v[88:89], v100 offset:45056\n" \
  "ds_read_b64_tr_b16 v[90:91], v100 offset:46080\n" \
  "s_waitcnt lgkmcnt(4)\n" \
  "v_mfma_f32_32x32x16_bf16 v[16:31], v[72:75], v[68:71], v[16:31]\n" \
  "ds_read_b128 v[80:83], v99 offset:32768\n" \
  "ds_read_b128 v[84:87], v99 offset:36864\n" \
  "s_waitcnt lgkmcnt(4)\n" \
  "v_mfma_f32_32x32x16_bf16 v[48:63], v[76:79], v[68:71], v[48:63]\n" \
  "ds_read_b64_tr_b16 v[92:93], v101 offset:45056\n" \
  "ds_read_b64_tr_b16 v[94:95], v101 offset:46080\n" \
  "v_mfma_f32_32x32x16_bf16 v[32:47], v[76:79], v[64:67], v[32:47]\n" \
  "s_sub_u32 %[cnt], %[cnt], 1\n" \
  "s_cmp_lg_u32 %[cnt], 0\n" \
  "v_mfma_f32_16x16x32_bf16 v[188:191], v[184:187], v[72:75], v[188:191]\n" \
  "s_cbranch_scc0 2f\n" \
  "s_waitcnt lgkmcnt(3)\n" \
  "v_mfma_f32_32x32x16_bf16 v[0:15], v[88:91], v[80:83], v[0:15]\n" \
  "s_waitcnt lgkmcnt(0)\n" \
  "s_barrier\n" \
  "v_xor_b32 v100, 0x10000, v100\n" \
  "v_xor_b32 v96, 0x10000, v96\n" \
  "v_xor_b32 v101, 0x10000, v101\n" \
  "ds_read_b64_tr_b16 v[72:73], v100 offset:0\n" \
  "ds_read_b64_tr_b16 v[74:75], v100 offset:1024\n" \
  "v_mfma_f32_32x32x16_bf16 v[16:31], v[88:91], v[84:87], v[16:31]\n" \
  "ds_read_b128 v[64:67], v96 offset:0\n" \
  "ds_read_b128 v[68:71], v96 offset:4096\n" \
  "v_mfma_f32_32x32x16_bf16 v[48:63], v[92:95], v[84:87], v[48:63]\n" \
  "ds_read_b64_tr_b16 v[76:77], v101 offset:0\n" \
  "ds_read_b64_tr_b16 v[78:79], v101 offset:1024\n" \
  "v_mfma_f32_32x32x16_bf16 v[32:47], v[92:95], v[80:83], v[32:47]\n" \
  "v_xor_b32 v97, 0x10000, v97\n" \
  "v_xor_b32 v98, 0x10000, v98\n" \
  "v_xor_b32 v99, 0x10000, v99\n" \
  "v_mfma_f32_16x16x32_bf16 v[188:191], v[184:187], v[88:91], v[188:191]\n" \
  "s_branch 1b\n" \
  "2:\n" \
  "s_waitcnt lgkmcnt(3)\n" \
  "v_mfma_f32_16x16x32_bf16 v[188:191], v[184:187], v[88:91], v[188:191]\n" \
  "v_mfma_f32_32x32x16_bf16 v[0:15], v[88:91], v[80:83], v[0:15]\n" \
  "s_waitcnt lgkmcnt(0)\n" \
  "v_mfma_f32_32x32x16_bf16 v[32:47], v[92:95], v[80:83], v[32:47]\n" \
  "s_mov_b64 exec, 0xffff\n" \
  "s_nop 3\n" \
  "ds_write2_b32 v109, v188, v189 offset1:16\n" \
  "s_mov_b64 exec, -1\n" \
  "s_waitcnt lgkmcnt(0)\n" \
  "s_barrier\n" \
  "v_mfma_f32_32x32x16_bf16 v[16:31], v[88:91], v[84:87], v[16:31]\n" \
  "ds_read2st64_b32 v[180:181], v103 offset1:2\n" \
  "ds_read_b128 v[112:115], v102 offset:0\n" \
  "ds_read_b128 v[116:119], v102 offset:32\n" \
  "ds_read_b128 v[120:123], v102 offset:64\n" \
  "ds_read_b128 v[124:127], v102 offset:96\n" \
  "v_mfma_f32_32x32x16_bf16 v[48:63], v[92:95], v[84:87], v[48:63]\n" \
  "ds_read_b128 v[128:131], v108 offset:0\n" \
  "ds_read_b128 v[132:135], v108 offset:32\n" \
  "ds_read_b128 v[136:139], v108 offset:64\n" \
  "ds_read_b128 v[140:143], v108 offset:96\n" \
  "ds_read2_b32 v[182:183], v103 offset0:32 offset1:160\n" \
  "s_waitcnt lgkmcnt(5)\n" \
  "v_pk_fma_f32 v[0:1], v[112:113], v[180:181], v[0:1] op_sel:[0,1,0] op_sel_hi:[1,1,1]\n" \
  "v_pk_fma_f32 v[2:3], v[114:115], v[180:181], v[2:3] op_sel:[0,1,0] op_sel_hi:[1,1,1]\n" \
  "v_pk_fma_f32 v[4:5], v[116:117], v[180:181], v[4:5] op_sel:[0,1,0] op_sel_hi:[1,1,1]\n" \
  "v_pk_fma_f32 v[6:7], v[118:119], v[180:181], v[6:7] op_sel:[0,1,0] op_sel_hi:[1,1,1]\n" \
  "v_pk_fma_f32 v[8:9], v[120:121], v[180:181], v[8:9] op_sel:[0,1,0] op_sel_hi:[1,1,1]\n" \
  "v_pk_fma_f32 v[10:11], v[122:123], v[180:181], v[10:11] op_sel:[0,1,0] op_sel_hi:[1,1,1]\n" \
  "v_pk_fma_f32 v[12:13], v[124:125], v[180:181], v[12:13] op_sel:[0,1,0] op_sel_hi:[1,1,1]\n" \
  "v_pk_fma_f32 v[14:15], v[126:127], v[180:181], v[14:15] op_sel:[0,1,0] op_sel_hi:[1,1,1]\n" \
  "v_pk_mul_f32 v[0:1], v[0:1], v[180:181] op_sel_hi:[1,0]\n" \
  "v_pk_mul_f32 v[2:3], v[2:3], v[180:181] op_sel_hi:[1,0]\n" \
  "v_pk_mul_f32 v[4:5], v[4:5], v[180:181] op_sel_hi:[1,0]\n" \
  "v_pk_mul_f32 v[6:7], v[6:7], v[180:181] op_sel_hi:[1,0]\n" \
  "v_cvt_pk_bf16_f32 v64, v0, v1\n" \
  "v_cvt_pk_bf16_f32 v65, v2, v3\n" \
  "v_cvt_pk_bf16_f32 v66, v4, v5\n" \
  "v_cvt_pk_bf16_f32 v67, v6, v7\n" \
  "s_cmp_lg_u32 %[lw], 0\n" \
  "s_cbranch_scc1 3f\n" \
  "buffer_store_dwordx4 v[64:67], v104, %[srd], %[so0] offen sc1\n" \
  "3:\n" \
  "v_pk_mul_f32 v[8:9], v[8:9], v[180:181] op_sel_hi:[1,0]\n" \
  "v_pk_mul_f32 v[10:11], v[10:11], v[180:181] op_sel_hi:[1,0]\n" \
  "v_pk_mul_f32 v[12:13], v[12:13], v[180:181] op_sel_hi:[1,0]\n" \
  "v_pk_mul_f32 v[14:15], v[14:15], v[180:181] op_sel_hi:[1,0]\n" \
  "v_cvt_pk_bf16_f32 v68, v8, v9\n" \
  "v_cvt_pk_bf16_f32 v69, v10, v11\n" \
  "v_cvt_pk_bf16_f32 v70, v12, v13\n" \
  "v_cvt_pk_bf16_f32 v71, v14, v15\n" \
  "s_cmp_lg_u32 %[lw], 0\n" \
  "s_cbranch_scc1 3f\n" \
  "buffer_store_dwordx4 v[68:71], v105, %[srd], %[so0] offen sc1\n" \
  "3:\n" \
  "s_waitcnt lgkmcnt(1)\n" \
  "v_pk_fma_f32 v[32:33], v[128:129], v[180:181], v[32:33] op_sel:[0,1,0] op_sel_hi:[1,1,1]\n" \
  "v_pk_fma_f32 v[34:35], v[130:131], v[180:181], v[34:35] op_sel:[0,1,0] op_sel_hi:[1,1,1]\n" \
  "v_pk_fma_f32 v[36:37], v[132:133], v[180:181], v[36:37] op_sel:[0,1,0] op_sel_hi:[1,1,1]\n" \
  "v_pk_fma_f32 v[38:39], v[134:135], v[180:181], v[38:39] op_sel:[0,1,0] op_sel_hi:[1,1,1]\n" \
  "v_pk_fma_f32 v[40:41], v[136:137], v[180:181], v[40:41] op_sel:[0,1,0] op_sel_hi:[1,1,1]\n" \
  "v_pk_fma_f32 v[42:43], v[138:139], v[180:181], v[42:43] op_sel:[0,1,0] op_sel_hi:[1,1,1]\n" \
  "v_pk_fma_f32 v[44:45], v[140:141], v[180:181], v[44:45] op_sel:[0,1,0] op_sel_hi:[1,1,1]\n" \
  "v_pk_fma_f32 v[46:47], v[142:143], v[180:181], v[46:47] op_sel:[0,1,0] op_sel_hi:[1,1,1]\n" \
  "v_pk_mul_f32 v[32:33], v[32:33], v[180:181] op_sel_hi:[1,0]\n" \
  "v_pk_mul_f32 v[34:35], v[34:35], v[180:181] op_sel_hi:[1,0]\n" \
  "v_pk_mul_f32 v[36:37], v[36:37], v[180:181] op_sel_hi:[1,0]\n" \
  "v_pk_mul_f32 v[38:39], v[38:39], v[180:181] op_sel_hi:[1,0]\n" \
  "v_cvt_pk_bf16_f32 v72, v32, v33\n" \
  "v_cvt_pk_bf16_f32 v73, v34, v35\n" \
  "v_cvt_pk_bf16_f32 v74, v36, v37\n" \
  "v_cvt_pk_bf16_f32 v75, v38, v39\n" \
  "s_cmp_lg_u32 %[lw], 0\n" \
  "s_cbranch_scc1 3f\n" \
  "buffer_store_dwordx4 v[72:75], v106, %[srd], %[so0] offen sc1\n" \
  "3:\n" \
  "v_pk_mul_f32 v[40:41], v[40:41], v[180:181] op_sel_hi:[1,0]\n" \
  "v_pk_mul_f32 v[42:43], v[42:43], v[180:181] op_sel_hi:[1,0]\n" \
  "v_pk_mul_f32 v[44:45], v[44:45], v[180:181] op_sel_hi:[1,0]\n" \
  "v_pk_mul_f32 v[46:47], v[46:47], v[180:181] op_sel_hi:[1,0]\n" \
  "v_cvt_pk_bf16_f32 v76, v40, v41\n" \
  "v_cvt_pk_bf16_f32 v77, v42, v43\n" \
  "v_cvt_pk_bf16_f32 v78, v44, v45\n" \
  "v_cvt_pk_bf16_f32 v79, v46, v47\n" \
  "s_cmp_lg_u32 %[lw], 0\n" \
  "s_cbranch_scc1 3f\n" \
  "buffer_store_dwordx4 v[76:79], v107, %[srd], %[so0] offen sc1\n" \
  "3:\n" \
  "s_waitcnt lgkmcnt(0)\n" \
  "v_pk_fma_f32 v[16:17], v[112:113], v[182:183], v[16:17] op_sel:[0,1,0] op_sel_hi:[1,1,1]\n" \
  "v_pk_fma_f32 v[18:19], v[114:115], v[182:183], v[18:19] op_sel:[0,1,0] op_sel_hi:[1,1,1]\n" \
  "v_pk_fma_f32 v[20:21], v[116:117], v[182:183], v[20:21] op_sel:[0,1,0] op_sel_hi:[1,1,1]\n" \
  "v_pk_fma_f32 v[22:23], v[118:119], v[182:183], v[22:23] op_sel:[0,1,0] op_sel_hi:[1,1,1]\n" \
  "v_pk_fma_f32 v[24:25], v[120:121], v[182:183], v[24:25] op_sel:[0,1,0] op_sel_hi:[1,1,1]\n" \
  "v_pk_fma_f32 v[26:27], v[122:123], v[182:183], v[26:27] op_sel:[0,1,0] op_sel_hi:[1,1,1]\n" \
  "v_pk_fma_f32 v[28:29], v[124:125], v[182:183], v[28:29] op_sel:[0,1,0] op_sel_hi:[1,1,1]\n" \
  "v_pk_fma_f32 v[30:31], v[126:127], v[182:183], v[30:31] op_sel:[0,1,0] op_sel_hi:[1,1,1]\n" \
  "v_pk_mul_f32 v[16:17], v[16:17], v[182:183] op_sel_hi:[1,0]\n" \
  "v_pk_mul_f32 v[18:19], v[18:19], v[182:183] op_sel_hi:[1,0]\n" \
  "v_pk_mul_f32 v[20:21], v[20:21], v[182:183] op_sel_hi:[1,0]\n" \
  "v_pk_mul_f32 v[22:23], v[22:23], v[182:183] op_sel_hi:[1,0]\n" \
  "v_cvt_pk_bf16_f32 v80, v16, v17\n" \
  "v_cvt_pk_bf16_f32 v81, v18, v19\n" \
  "v_cvt_pk_bf16_f32 v82, v20, v21\n" \
  "v_cvt_pk_bf16_f32 v83, v22, v23\n" \
  "s_cmp_ge_u32 %[nb], 5\n" \
  "s_cbranch_scc1 3f\n" \
  "buffer_store_dwordx4 v[80:83], v104, %[srd], %[so1] offen sc1\n" \
  "3:\n" \
  "v_pk_mul_f32 v[24:25], v[24:25], v[182:183] op_sel_hi:[1,0]\n" \
  "v_pk_mul_f32 v[26:27], v[26:27], v[182:183] op_sel_hi:[1,0]\n" \
  "v_pk_mul_f32 v[28:29], v[28:29], v[182:183] op_sel_hi:[1,0]\n" \
  "v_pk_mul_f32 v[30:31], v[30:31], v[182:183] op_sel_hi:[1,0]\n" \
  "v_cvt_pk_bf16_f32 v84, v24, v25\n" \
  "v_cvt_pk_bf16_f32 v85, v26, v27\n" \
  "v_cvt_pk_bf16_f32 v86, v28, v29\n" \
  "v_cvt_pk_bf16_f32 v87, v30, v31\n" \
  "s_cmp_ge_u32 %[nb], 5\n" \
  "s_cbranch_scc1 3f\n" \
  "buffer_store_dwordx4 v[84:87], v105, %[srd], %[so1] offen sc1\n" \
  "3:\n" \
  "v_pk_fma_f32 v[48:49], v[128:129], v[182:183], v[48:49] op_sel:[0,1,0] op_sel_hi:[1,1,1]\n" \
  "v_pk_fma_f32 v[50:51], v[130:131], v[182:183], v[50:51] op_sel:[0,1,0] op_sel_hi:[1,1,1]\n" \
  "v_pk_fma_f32 v[52:53], v[132:133], v[182:183], v[52:53] op_sel:[0,1,0] op_sel_hi:[1,1,1]\n" \
  "v_pk_fma_f32 v[54:55], v[134:135], v[182:183], v[54:55] op_sel:[0,1,0] op_sel_hi:[1,1,1]\n" \
  "v_pk_fma_f32 v[56:57], v[136:137], v[182:183], v[56:57] op_sel:[0,1,0] op_sel_hi:[1,1,1]\n" \
  "v_pk_fma_f32 v[58:59], v[138:139], v[182:183], v[58:59] op_sel:[0,1,0] op_sel_hi:[1,1,1]\n" \
  "v_pk_fma_f32 v[60:61], v[140:141], v[182:183], v[60:61] op_sel:[0,1,0] op_sel_hi:[1,1,1]\n" \
  "v_pk_fma_f32 v[62:63], v[142:143], v[182:183], v[62:63] op_sel:[0,1,0] op_sel_hi:[1,1,1]\n" \
  "v_pk_mul_f32 v[48:49], v[48:49], v[182:183] op_sel_hi:[1,0]\n" \
  "v_pk_mul_f32 v[50:51], v[50:51], v[182:183] op_sel_hi:[1,0]\n" \
  "v_pk_mul_f32 v[52:53], v[52:53], v[182:183] op_sel_hi:[1,0]\n" \
  "v_pk_mul_f32 v[54:55], v[54:55], v[182:183] op_sel_hi:[1,0]\n" \
  "v_cvt_pk_bf16_f32 v88, v48, v49\n" \
  "v_cvt_pk_bf16_f32 v89, v50, v51\n" \
  "v_cvt_pk_bf16_f32 v90, v52, v53\n" \
  "v_cvt_pk_bf16_f32 v91, v54, v55\n" \
  "s_cmp_ge_u32 %[nb], 5\n" \
  "s_cbranch_scc1 3f\n" \
  "buffer_store_dwordx4 v[88:91], v106, %[srd], %[so1] offen sc1\n" \
  "3:\n" \
  "v_pk_mul_f32 v[56:57], v[56:57], v[182:183] op_sel_hi:[1,0]\n" \
  "v_pk_mul_f32 v[58:59], v[58:59], v[182:183] op_sel_hi:[1,0]\n" \
  "v_pk_mul_f32 v[60:61], v[60:61], v[182:183] op_sel_hi:[1,0]\n" \
  "v_pk_mul_f32 v[62:63], v[62:63], v[182:183] op_sel_hi:[1,0]\n" \
  "v_cvt_pk_bf16_f32 v92, v56, v57\n" \
  "v_cvt_pk_bf16_f32 v93, v58, v59\n" \
  "v_cvt_pk_bf16_f32 v94, v60, v61\n" \
  "v_cvt_pk_bf16_f32 v95, v62, v63\n" \
  "s_cmp_ge_u32 %[nb], 5\n" \
  "s_cbranch_scc1 3f\n" \
  "buffer_store_dwordx4 v[92:95], v107, %[srd], %[so1] offen sc1\n" \
  "3:\n" \
  "v_mov_b32 v180, 1\n" \
  "v_mov_b32 v181, 163392\n" \
  "s_mov_b64 exec, 1\n" \
  "s_waitcnt vmcnt(0)\n" \
  "ds_add_rtn_u32 v182, v181, v180 offset:0\n" \
  "s_waitcnt lgkmcnt(0)\n" \
  "v_readfirstlane_b32 %[cnt], v182\n" \
  "s_mov_b64 exec, %[act]\n" \
  "s_cmp_lg_u32 %[cnt], 3\n" \
  "s_cbranch_scc1 4f\n" \
  "buffer_atomic_add v180, v111, %[srd], 0 offen sc1\n" \
  "4:\n" \
  "s_mov_b64 exec, -1\n" \
  "s_cmp_lt_u32 %[nb], 5\n" \
  "s_cbranch_scc1 3f\n" \
  "s_cmp_eq_u32 %[lw], 0\n" \
  "s_cbranch_scc1 4f\n" \
  "s_sleep 4\n" \
  "buffer_store_dwordx4 v[64:67], v104, %[srd], %[so0] offen sc1\n" \
  "buffer_store_dwordx4 v[68:71], v105, %[srd], %[so0] offen sc1\n" \
  "buffer_store_dwordx4 v[72:75], v106, %[srd], %[so0] offen sc1\n" \
  "buffer_store_dwordx4 v[76:79], v107, %[srd], %[so0] offen sc1\n" \
  "4:\n" \
  "s_sleep 10\n" \
  "buffer_store_dwordx4 v[80:83], v104, %[srd], %[so1] offen sc1\n" \
  "buffer_store_dwordx4 v[84:87], v105, %[srd], %[so1] offen sc1\n" \
  "buffer_store_dwordx4 v[88:91], v106, %[srd], %[so1] offen sc1\n" \
  "buffer_store_dwordx4 v[92:95], v107, %[srd], %[so1] offen sc1\n" \
  "s_nop 0\n" \
  "3:\n" \
  "s_branch 7f\n" \
  "5:\n" \
  "s_cmp_ge_u32 %[nb], 15\n" \
  "s_cbranch_scc1 6f\n" \
  "v_mov_b32 v180, 163392\n" \
  "ds_write_b64 v180, v[0:1]\n" \
  "s_mov_b32 %[cnt], 5\n" \
  "s_waitcnt lgkmcnt(0)\n" \
  "s_barrier\n" \
  "ds_read_b64_tr_b16 v[72:73], v100 offset:0\n" \
  "ds_read_b64_tr_b16 v[74:75], v100 offset:1024\n" \
  "ds_read_b128 v[64:67], v96 offset:0\n" \
  "ds_read_b128 v[68:71], v96 offset:4096\n" \
  "ds_read_b64_tr_b16 v[76:77], v101 offset:0\n" \
  "ds_read_b64_tr_b16 v[78:79], v101 offset:1024\n" \
  "1:\n" \
  "s_waitcnt lgkmcnt(3)\n" \
  "v_mfma_f32_32x32x16_bf16 v[0:15], v[64:67], v[72:75], v[0:15]\n" \
  "ds_read_b64_tr_b16 v[88:89], v100 offset:4096\n" \
  "ds_read_b64_tr_b16 v[90:91], v100 offset:5120\n" \
  "s_waitcnt lgkmcnt(4)\n" \
  "v_mfma_f32_32x32x16_bf16 v[16:31], v[68:71], v[72:75], v[16:31]\n" \
  "ds_read_b128 v[80:83], v97 offset:0\n" \
  "ds_read_b128 v[84:87], v97 offset:4096\n" \
  "s_waitcnt lgkmcnt(4)\n" \
  "v_mfma_f32_32x32x16_bf16 v[48:63], v[68:71], v[76:79], v[48:63]\n" \
  "ds_read_b64_tr_b16 v[92:93], v101 offset:4096\n" \
  "ds_read_b64_tr_b16 v[94:95], v101 offset:5120\n" \
  "v_mfma_f32_32x32x16_bf16 v[32:47], v[64:67], v[76:79], v[32:47]\n" \
  "v_mfma_f32_16x16x32_bf16 v[188:191], v[184:187], v[72:75], v[188:191]\n" \
  "s_waitcnt lgkmcnt(3)\n" \
  "v_mfma_f32_32x32x16_bf16 v[0:15], v[80:83], v[88:91], v[0:15]\n" \
  "ds_read_b64_tr_b16 v[72:73], v100 offset:8192\n" \
  "ds_read_b64_tr_b16 v[74:75], v100 offset:9216\n" \
  "s_waitcnt lgkmcnt(4)\n" \
  "v_mfma_f32_32x32x16_bf16 v[16:31], v[84:87], v[88:91], v[16:31]\n" \
  "ds_read_b128 v[64:67], v98 offset:0\n" \
  "ds_read_b128 v[68:71], v98 offset:4096\n" \
  "s_waitcnt lgkmcnt(4)\n" \
  "v_mfma_f32_32x32x16_bf16 v[48:63], v[84:87], v[92:95], v[48:63]\n" \
  "ds_read_b64_tr_b16 v[76:77], v101 offset:8192\n" \
  "ds_read_b64_tr_b16 v[78:79], v101 offset:9216\n" \
  "v_mfma_f32_32x32x16_bf16 v[32:47], v[80:83], v[92:95], v[32:47]\n" \
  "v_mfma_f32_16x16x32_bf16 v[188:191], v[184:187], v[88:91], v[188:191]\n" \
  "s_waitcnt lgkmcnt(3)\n" \
  "v_mfma_f32_32x32x16_bf16 v[0:15], v[64:67], v[72:75], v[0:15]\n" \
  "ds_read_b64_tr_b16 v[88:89], v100 offset:12288\n" \
  "ds_read_b64_tr_b16 v[90:91], v100 offset:13312\n" \
  "s_waitcnt lgkmcnt(4)\n" \
  "v_mfma_f32_32x32x16_bf16 v[16:31], v[68:71], v[72:75], v[16:31]\n" \
  "ds_read_b128 v[80:83], v99 offset:0\n" \
  "ds_read_b128 v[84:87], v99 offset:4096\n" \
  "s_waitcnt lgkmcnt(4)\n" \
  "v_mfma_f32_32x32x16_bf16 v[48:63], v[68:71], v[76:79], v[48:63]\n" \
  "ds_read_b64_tr_b16 v[92:93], v101 offset:12288\n" \
  "ds_read_b64_tr_b16 v[94:95], v101 offset:13312\n" \
  "v_mfma_f32_32x32x16_bf16 v[32:47], v[64:67], v[76:79], v[32:47]\n" \
  "v_mfma_f32_16x16x32_bf16 v[188:191], v[184:187], v[72:75], v[188:191]\n" \
  "s_waitcnt lgkmcnt(3)\n" \
  "v_mfma_f32_32x32x16_bf16 v[0:15], v[80:83], v[88:91], v[0:15]\n" \
  "s_waitcnt lgkmcnt(0)\n" \
  "s_barrier\n" \
  "ds_read_b64_tr_b16 v[72:73], v100 offset:32768\n" \
  "ds_read_b64_tr_b16 v[74:75], v100 offset:33792\n" \
  "v_mfma_f32_32x32x16_bf16 v[16:31], v[84:87], v[88:91], v[16:31]\n" \
  "ds_read_b128 v[64:67], v96 offset:32768\n" \
  "ds_read_b128 v[68:71], v96 offset:36864\n" \
  "v_mfma_f32_32x32x16_bf16 v[48:63], v[84:87], v[92:95], v[48:63]\n" \
  "ds_read_b64_tr_b16 v[76:77], v101 offset:32768\n" \
  "ds_read_b64_tr_b16 v[78:79], v101 offset:33792\n" \
  "v_mfma_f32_32x32x16_bf16 v[32:47], v[80:83], v[92:95], v[32:47]\n" \
  "v_mfma_f32_16x16x32_bf16 v[188:191], v[184:187], v[88:91], v[188:191]\n" \
  "s_waitcnt lgkmcnt(3)\n" \
  "v_mfma_f32_32x32x16_bf16 v[0:15], v[64:67], v[72:75], v[0:15]\n" \
  "ds_read_b64_tr_b16 v[88:89], v100 offset:36864\n" \
  "ds_read_b64_tr_b16 v[90:91], v100 offset:37888\n" \
  "s_waitcnt lgkmcnt(4)\n" \
  "v_mfma_f32_32x32x16_bf16 v[16:31], v[68:71], v[72:75], v[16:31]\n" \
  "ds_read_b128 v[80:83], v97 offset:32768\n" \
  "ds_read_b128 v[84:87], v97 offset:36864\n" \
  "s_waitcnt lgkmcnt(4)\n" \
  "v_mfma_f32_32x32x16_bf16 v[48:63], v[68:71], v[76:79], v[48:63]\n" \
  "ds_read_b64_tr_b16 v[92:93], v101 offset:36864\n" \
  "ds_read_b64_tr_b16 v[94:95], v101 offset:37888\n" \
  "v_mfma_f32_32x32x16_bf16 v[32:47], v[64:67], v[76:79], v[32:47]\n" \
  "v_mfma_f32_16x16x32_bf16 v[188:191], v[184:187], v[72:75], v[188:191]\n" \
  "s_waitcnt lgkmcnt(3)\n" \
  "v_mfma_f32_32x32x16_bf16 v[0:15], v[80:83], v[88:91], v[0:15]\n" \
  "ds_read_b64_tr_b16 v[72:73], v100 offset:40960\n" \
  "ds_read_b64_tr_b16 v[74:75], v100 offset:41984\n" \
  "s_waitcnt lgkmcnt(4)\n" \
  "v_mfma_f32_32x32x16_bf16 v[16:31], v[84:87], v[88:91], v[16:31]\n" \
  "ds_read_b128 v[64:67], v98 offset:32768\n" \
  "ds_read_b128 v[68:71], v98 offset:36864\n" \
  "s_waitcnt lgkmcnt(4)\n" \
  "v_mfma_f32_32x32x16_bf16 v[48:63], v[84:87], v[92:95], v[48:63]\n" \
  "ds_read_b64_tr_b16 v[76:77], v101 offset:40960\n" \
  "ds_read_b64_tr_b16 v[78:79], v101 offset:41984\n" \
  "v_mfma_f32_32x32x16_bf16 v[32:47], v[80:83], v[92:95], v[32:47]\n" \
  "v_mfma_f32_16x16x32_bf16 v[188:191], v[184:187], v[88:91], v[188:191]\n" \
  "s_waitcnt lgkmcnt(3)\n" \
  "v_mfma_f32_32x32x16_bf16 v[0:15], v[64:67], v[72:75], v[0:15]\n" \
  "ds_read_b64_tr_b16 v[88:89], v100 offset:45056\n" \
  "ds_read_b64_tr_b16 v[90:91], v100 offset:46080\n" \
  "s_waitcnt lgkmcnt(4)\n" \
  "v_mfma_f32_32x32x16_bf16 v[16:31], v[68:71], v[72:75], v[16:31]\n" \
  "ds_read_b128 v[80:83], v99 offset:32768\n" \
  "ds_read_b128 v[84:87], v99 offset:36864\n" \
  "s_waitcnt lgkmcnt(4)\n" \
  "v_mfma_f32_32x32x16_bf16 v[48:63], v[68:71], v[76:79], v[48:63]\n" \
  "ds_read_b64_tr_b16 v[92:93], v101 offset:45056\n" \
  "ds_read_b64_tr_b16 v[94:95], v101 offset:46080\n" \
  "v_mfma_f32_32x32x16_bf16 v[32:47], v[64:67], v[76:79], v[32:47]\n" \
  "s_sub_u32 %[cnt], %[cnt], 1\n" \
  "s_cmp_lg_u32 %[cnt], 0\n" \
  "v_mfma_f32_16x16x32_bf16 v[188:191], v[184:187], v[72:75], v[188:191]\n" \
  "s_cbranch_scc0 2f\n" \
  "s_waitcnt lgkmcnt(3)\n" \
  "v_mfma_f32_32x32x16_bf16 v[0:15], v[80:83], v[88:91], v[0:15]\n" \
  "s_waitcnt lgkmcnt(0)\n" \
  "s_barrier\n" \
  "v_xor_b32 v100, 0x10000, v100\n" \
  "v_xor_b32 v96, 0x10000, v96\n" \
  "v_xor_b32 v101, 0x10000, v101\n" \
  "ds_read_b64_tr_b16 v[72:73], v100 offset:0\n" \
  "ds_read_b64_tr_b16 v[74:75], v100 offset:1024\n" \
  "v_mfma_f32_32x32x16_bf16 v[16:31], v[84:87], v[88:91], v[16:31]\n" \
  "ds_read_b128 v[64:67], v96 offset:0\n" \
  "ds_read_b128 v[68:71], v96 offset:4096\n" \
  "v_mfma_f32_32x32x16_bf16 v[48:63], v[84:87], v[92:95], v[48:63]\n" \
  "ds_read_b64_tr_b16 v[76:77], v101 offset:0\n" \
  "ds_read_b64_tr_b16 v[78:79], v101 offset:1024\n" \
  "v_mfma_f32_32x32x16_bf16 v[32:47], v[80:83], v[92:95], v[32:47]\n" \
  "v_xor_b32 v97, 0x10000, v97\n" \
  "v_xor_b32 v98, 0x10000, v98\n" \
  "v_xor_b32 v99, 0x10000, v99\n" \
  "v_mfma_f32_16x16x32_bf16 v[188:191], v[184:187], v[88:91], v[188:191]\n" \
  "s_branch 1b\n" \
  "2:\n" \
  "s_waitcnt lgkmcnt(3)\n" \
  "v_mfma_f32_16x16x32_bf16 v[188:191], v[184:187], v[88:91], v[188:191]\n" \
  "v_mfma_f32_32x32x16_bf16 v[0:15], v[80:83], v[88:91], v[0:15]\n" \
  "s_waitcnt lgkmcnt(0)\n" \
  "v_mfma_f32_32x32x16_bf16 v[32:47], v[80:83], v[92:95], v[32:47]\n" \
  "s_mov_b64 exec, 0xffff\n" \
  "s_nop 3\n" \
  "ds_write2_b32 v109, v188, v189 offset1:16\n" \
  "s_mov_b64 exec, -1\n" \
  "s_waitcnt lgkmcnt(0)\n" \
  "s_barrier\n" \
  "v_mfma_f32_32x32x16_bf16 v[16:31], v[84:87], v[88:91], v[16:31]\n" \
  "ds_read_b32 v180, v103\n" \
  "ds_read_b32 v181, v108\n" \
  "ds_read_b128 v[128:131], v102 offset:512\n" \
  "ds_read_b128 v[132:135], v102 offset:544\n" \
  "ds_read_b128 v[136:139], v102 offset:576\n" \
  "ds_read_b128 v[140:143], v102 offset:608\n" \
  "ds_read_b128 v[112:115], v102 offset:0\n" \
  "ds_read_b128 v[116:119], v102 offset:32\n" \
  "ds_read_b128 v[120:123], v102 offset:64\n" \
  "v_mfma_f32_32x32x16_bf16 v[48:63], v[84:87], v[92:95], v[48:63]\n" \
  "ds_read_b128 v[124:127], v102 offset:96\n" \
  "ds_read_b128 v[160:163], v102 offset:640\n" \
  "ds_read_b128 v[164:167], v102 offset:672\n" \
  "ds_read_b128 v[168:171], v102 offset:704\n" \
  "ds_read_b128 v[172:175], v102 offset:736\n" \
  "ds_read_b128 v[144:147], v102 offset:128\n" \
  "ds_read_b128 v[148:151], v102 offset:160\n" \
  "ds_read_b128 v[152:155], v102 offset:192\n" \
  "ds_read_b128 v[156:159], v102 offset:224\n" \
  "s_waitcnt lgkmcnt(12)\n" \
  "v_pk_fma_f32 v[0:1], v[128:129], v[180:181], v[0:1] op_sel:[0,0,0] op_sel_hi:[1,0,1]\n" \
  "v_pk_fma_f32 v[2:3], v[130:131], v[180:181], v[2:3] op_sel:[0,0,0] op_sel_hi:[1,0,1]\n" \
  "v_pk_fma_f32 v[4:5], v[132:133], v[180:181], v[4:5] op_sel:[0,0,0] op_sel_hi:[1,0,1]\n" \
  "v_pk_fma_f32 v[6:7], v[134:135], v[180:181], v[6:7] op_sel:[0,0,0] op_sel_hi:[1,0,1]\n" \
  "v_pk_fma_f32 v[8:9], v[136:137], v[180:181], v[8:9] op_sel:[0,0,0] op_sel_hi:[1,0,1]\n" \
  "v_pk_fma_f32 v[10:11], v[138:139], v[180:181], v[10:11] op_sel:[0,0,0] op_sel_hi:[1,0,1]\n" \
  "v_pk_fma_f32 v[12:13], v[140:141], v[180:181], v[12:13] op_sel:[0,0,0] op_sel_hi:[1,0,1]\n" \
  "v_pk_fma_f32 v[14:15], v[142:143], v[180:181], v[14:15] op_sel:[0,0,0] op_sel_hi:[1,0,1]\n" \
  "s_waitcnt lgkmcnt(8)\n" \
  "v_pk_mul_f32 v[0:1], v[0:1], v[112:113]\n" \
  "v_pk_mul_f32 v[2:3], v[2:3], v[114:115]\n" \
  "v_pk_mul_f32 v[4:5], v[4:5], v[116:117]\n" \
  "v_pk_mul_f32 v[6:7], v[6:7], v[118:119]\n" \
  "v_cvt_pk_bf16_f32 v64, v0, v1\n" \
  "v_cvt_pk_bf16_f32 v65, v2, v3\n" \
  "v_cvt_pk_bf16_f32 v66, v4, v5\n" \
  "v_cvt_pk_bf16_f32 v67, v6, v7\n" \
  "s_cmp_lg_u32 %[lw], 0\n" \
  "s_cbranch_scc1 3f\n" \
  "buffer_store_dwordx4 v[64:67], v104, %[srd], %[so0] offen sc1\n" \
  "3:\n" \
  "v_pk_mul_f32 v[8:9], v[8:9], v[120:121]\n" \
  "v_pk_mul_f32 v[10:11], v[10:11], v[122:123]\n" \
  "v_pk_mul_f32 v[12:13], v[12:13], v[124:125]\n" \
  "v_pk_mul_f32 v[14:15], v[14:15], v[126:127]\n" \
  "v_cvt_pk_bf16_f32 v68, v8, v9\n" \
  "v_cvt_pk_bf16_f32 v69, v10, v11\n" \
  "v_cvt_pk_bf16_f32 v70, v12, v13\n" \
  "v_cvt_pk_bf16_f32 v71, v14, v15\n" \
  "s_cmp_lg_u32 %[lw], 0\n" \
  "s_cbranch_scc1 3f\n" \
  "buffer_store_dwordx4 v[68:71], v105, %[srd], %[so0] offen sc1\n" \
  "3:\n" \
  "v_pk_fma_f32 v[32:33], v[128:129], v[180:181], v[32:33] op_sel:[0,1,0] op_sel_hi:[1,1,1]\n" \
  "v_pk_fma_f32 v[34:35], v[130:131], v[180:181], v[34:35] op_sel:[0,1,0] op_sel_hi:[1,1,1]\n" \
  "v_pk_fma_f32 v[36:37], v[132:133], v[180:181], v[36:37] op_sel:[0,1,0] op_sel_hi:[1,1,1]\n" \
  "v_pk_fma_f32 v[38:39], v[134:135], v[180:181], v[38:39] op_sel:[0,1,0] op_sel_hi:[1,1,1]\n" \
  "v_pk_fma_f32 v[40:41], v[136:137], v[180:181], v[40:41] op_sel:[0,1,0] op_sel_hi:[1,1,1]\n" \
  "v_pk_fma_f32 v[42:43], v[138:139], v[180:181], v[42:43] op_sel:[0,1,0] op_sel_hi:[1,1,1]\n" \
  "v_pk_fma_f32 v[44:45], v[140:141], v[180:181], v[44:45] op_sel:[0,1,0] op_sel_hi:[1,1,1]\n" \
  "v_pk_fma_f32 v[46:47], v[142:143], v[180:181], v[46:47] op_sel:[0,1,0] op_sel_hi:[1,1,1]\n" \
  "v_pk_mul_f32 v[32:33], v[32:33], v[112:113]\n" \
  "v_pk_mul_f32 v[34:35], v[34:35], v[114:115]\n" \
  "v_pk_mul_f32 v[36:37], v[36:37], v[116:117]\n" \
  "v_pk_mul_f32 v[38:39], v[38:39], v[118:119]\n" \
  "v_cvt_pk_bf16_f32 v72, v32, v33\n" \
  "v_cvt_pk_bf16_f32 v73, v34, v35\n" \
  "v_cvt_pk_bf16_f32 v74, v36, v37\n" \
  "v_cvt_pk_bf16_f32 v75, v38, v39\n" \
  "s_cmp_lg_u32 %[lw], 0\n" \
  "s_cbranch_scc1 3f\n" \
  "buffer_store_dwordx4 v[72:75], v106, %[srd], %[so0] offen sc1\n" \
  "3:\n" \
  "v_pk_mul_f32 v[40:41], v[40:41], v[120:121]\n" \
  "v_pk_mul_f32 v[42:43], v[42:43], v[122:123]\n" \
  "v_pk_mul_f32 v[44:45], v[44:45], v[124:125]\n" \
  "v_pk_mul_f32 v[46:47], v[46:47], v[126:127]\n" \
  "v_cvt_pk_bf16_f32 v76, v40, v41\n" \
  "v_cvt_pk_bf16_f32 v77, v42, v43\n" \
  "v_cvt_pk_bf16_f32 v78, v44, v45\n" \
  "v_cvt_pk_bf16_f32 v79, v46, v47\n" \
  "s_cmp_lg_u32 %[lw], 0\n" \
  "s_cbranch_scc1 3f\n" \
  "buffer_store_dwordx4 v[76:79], v107, %[srd], %[so0] offen sc1\n" \
  "3:\n" \
  "s_waitcnt lgkmcnt(4)\n" \
  "v_pk_fma_f32 v[16:17], v[160:161], v[180:181], v[16:17] op_sel:[0,0,0] op_sel_hi:[1,0,1]\n" \
  "v_pk_fma_f32 v[18:19], v[162:163], v[180:181], v[18:19] op_sel:[0,0,0] op_sel_hi:[1,0,1]\n" \
  "v_pk_fma_f32 v[20:21], v[164:165], v[180:181], v[20:21] op_sel:[0,0,0] op_sel_hi:[1,0,1]\n" \
  "v_pk_fma_f32 v[22:23], v[166:167], v[180:181], v[22:23] op_sel:[0,0,0] op_sel_hi:[1,0,1]\n" \
  "v_pk_fma_f32 v[24:25], v[168:169], v[180:181], v[24:25] op_sel:[0,0,0] op_sel_hi:[1,0,1]\n" \
  "v_pk_fma_f32 v[26:27], v[170:171], v[180:181], v[26:27] op_sel:[0,0,0] op_sel_hi:[1,0,1]\n" \
  "v_pk_fma_f32 v[28:29], v[172:173], v[180:181], v[28:29] op_sel:[0,0,0] op_sel_hi:[1,0,1]\n" \
  "v_pk_fma_f32 v[30:31], v[174:175], v[180:181], v[30:31] op_sel:[0,0,0] op_sel_hi:[1,0,1]\n" \
  "s_waitcnt lgkmcnt(0)\n" \
  "v_pk_mul_f32 v[16:17], v[16:17], v[144:145]\n" \
  "v_pk_mul_f32 v[18:19], v[18:19], v[146:147]\n" \
  "v_pk_mul_f32 v[20:21], v[20:21], v[148:149]\n" \
  "v_pk_mul_f32 v[22:23], v[22:23], v[150:151]\n" \
  "v_cvt_pk_bf16_f32 v80, v16, v17\n" \
  "v_cvt_pk_bf16_f32 v81, v18, v19\n" \
  "v_cvt_pk_bf16_f32 v82, v20, v21\n" \
  "v_cvt_pk_bf16_f32 v83, v22, v23\n" \
  "v_pk_mul_f32 v[24:25], v[24:25], v[152:153]\n" \
  "v_pk_mul_f32 v[26:27], v[26:27], v[154:155]\n" \
  "v_pk_mul_f32 v[28:29], v[28:29], v[156:157]\n" \
  "v_pk_mul_f32 v[30:31], v[30:31], v[158:159]\n" \
  "v_cvt_pk_bf16_f32 v84, v24, v25\n" \
  "v_cvt_pk_bf16_f32 v85, v26, v27\n" \
  "v_cvt_pk_bf16_f32 v86, v28, v29\n" \
  "v_cvt_pk_bf16_f32 v87, v30, v31\n" \
  "v_pk_fma_f32 v[48:49], v[160:161], v[180:181], v[48:49] op_sel:[0,1,0] op_sel_hi:[1,1,1]\n" \
  "v_pk_fma_f32 v[50:51], v[162:163], v[180:181], v[50:51] op_sel:[0,1,0] op_sel_hi:[1,1,1]\n" \
  "v_pk_fma_f32 v[52:53], v[164:165], v[180:181], v[52:53] op_sel:[0,1,0] op_sel_hi:[1,1,1]\n" \
  "v_pk_fma_f32 v[54:55], v[166:167], v[180:181], v[54:55] op_sel:[0,1,0] op_sel_hi:[1,1,1]\n" \
  "v_pk_fma_f32 v[56:57], v[168:169], v[180:181], v[56:57] op_sel:[0,1,0] op_sel_hi:[1,1,1]\n" \
  "v_pk_fma_f32 v[58:59], v[170:171], v[180:181], v[58:59] op_sel:[0,1,0] op_sel_hi:[1,1,1]\n" \
  "v_pk_fma_f32 v[60:61], v[172:173], v[180:181], v[60:61] op_sel:[0,1,0] op_sel_hi:[1,1,1]\n" \
  "v_pk_fma_f32 v[62:63], v[174:175], v[180:181], v[62:63] op_sel:[0,1,0] op_sel_hi:[1,1,1]\n" \
  "v_pk_mul_f32 v[48:49], v[48:49], v[144:145]\n" \
  "v_pk_mul_f32 v[50:51], v[50:51], v[146:147]\n" \
  "v_pk_mul_f32 v[52:53], v[52:53], v[148:149]\n" \
  "v_pk_mul_f32 v[54:55], v[54:55], v[150:151]\n" \
  "v_cvt_pk_bf16_f32 v88, v48, v49\n" \
  "v_cvt_pk_bf16_f32 v89, v50, v51\n" \
  "v_cvt_pk_bf16_f32 v90, v52, v53\n" \
  "v_cvt_pk_bf16_f32 v91, v54, v55\n" \
  "v_pk_mul_f32 v[56:57], v[56:57], v[152:153]\n" \
  "v_pk_mul_f32 v[58:59], v[58:59], v[154:155]\n" \
  "v_pk_mul_f32 v[60:61], v[60:61], v[156:157]\n" \
  "v_pk_mul_f32 v[62:63], v[62:63], v[158:159]\n" \
  "v_cvt_pk_bf16_f32 v92, v56, v57\n" \
  "v_cvt_pk_bf16_f32 v93, v58, v59\n" \
  "v_cvt_pk_bf16_f32 v94, v60, v61\n" \
  "v_cvt_pk_bf16_f32 v95, v62, v63\n" \
  "v_mov_b32 v180, 1\n" \
  "v_mov_b32 v181, 163392\n" \
  "s_mov_b64 exec, 1\n" \
  "s_waitcnt vmcnt(0)\n" \
  "ds_add_rtn_u32 v182, v181, v180 offset:0\n" \
  "s_waitcnt lgkmcnt(0)\n" \
  "v_readfirstlane_b32 %[cnt], v182\n" \
  "s_mov_b64 exec, %[act]\n" \
  "s_cmp_lg_u32 %[cnt], 3\n" \
  "s_cbranch_scc1 4f\n" \
  "buffer_atomic_add v180, v111, %[srd], 0 offen sc1\n" \
  "4:\n" \
  "s_mov_b64 exec, -1\n" \
  "s_cmp_eq_u32 %[lw], 0\n" \
  "s_cbranch_scc1 4f\n" \
  "s_sleep 4\n" \
  "buffer_store_dwordx4 v[64:67], v104, %[srd], %[so0] offen sc1\n" \
  "buffer_store_dwordx4 v[68:71], v105, %[srd], %[so0] offen sc1\n" \
  "buffer_store_dwordx4 v[72:75], v106, %[srd], %[so0] offen sc1\n" \
  "buffer_store_dwordx4 v[76:79], v107, %[srd], %[so0] offen sc1\n" \
  "4:\n" \
  "s_sleep 10\n" \
  "buffer_store_dwordx4 v[80:83], v104, %[srd], %[so1] offen sc1\n" \
  "buffer_store_dwordx4 v[84:87], v105, %[srd], %[so1] offen sc1\n" \
  "buffer_store_dwordx4 v[88:91], v106, %[srd], %[so1] offen sc1\n" \
  "buffer_store_dwordx4 v[92:95], v107, %[srd], %[so1] offen sc1\n" \
  "s_nop 0\n" \
  "3:\n" \
  "s_branch 7f\n" \
  "6:\n" \
  "s_mov_b32 %[cnt], 0\n" \
  ".Lk_pad9:\n" \
  "s_cbranch_execz .Lk_ret9\n" \
  "buffer_load_dword v3, off, %[srd], 0 sc1\n" \
  "buffer_load_dwordx4 v[16:19], v96, %[csrd], %[cnt] offen offset:0\n" \
  "buffer_load_dwordx4 v[20:23], v96, %[csrd], %[cnt] offen offset:256\n" \
  "buffer_load_dwordx4 v[24:27], v96, %[csrd], %[cnt] offen offset:512\n" \
  "buffer_load_dwordx4 v[28:31], v96, %[csrd], %[cnt] offen offset:768\n" \
  "buffer_load_dwordx4 v[32:35], v96, %[csrd], %[cnt] offen offset:1024\n" \
  "s_add_u32 %[cnt], %[cnt], 5120\n" \
  "buffer_load_dwordx4 v[36:39], v96, %[csrd], %[cnt] offen offset:0\n" \
  "buffer_load_dwordx4 v[40:43], v96, %[csrd], %[cnt] offen offset:256\n" \
  "buffer_load_dwordx4 v[44:47], v96, %[csrd], %[cnt] offen offset:512\n" \
  "buffer_load_dwordx4 v[48:51], v96, %[csrd], %[cnt] offen offset:768\n" \
  "buffer_load_dwordx4 v[52:55], v96, %[csrd], %[cnt] offen offset:1024\n" \
  "s_add_u32 %[cnt], %[cnt], 5120\n" \
  "v_mov_b32 v4, 0x3f803f80\n" \
  "s_mov_b32 vcc_lo, 0x20002\n" \
  "s_mov_b32 vcc_hi, 0x20002\n" \
  "s_waitcnt vmcnt(10)\n" \
  "v_add_u32 v3, 1, v3\n" \
  "1:\n" \
  "s_waitcnt vmcnt(9)\n" \
  "v_dot2c_f32_bf16 v0, v4, v16\n" \
  "v_dot2c_f32_bf16 v1, v16, v16\n" \
  "v_dot2c_f32_bf16 v0, v4, v17\n" \
  "v_dot2c_f32_bf16 v1, v17, v17\n" \
  "v_dot2c_f32_bf16 v0, v4, v18\n" \
  "v_dot2c_f32_bf16 v1, v18, v18\n" \
  "v_dot2c_f32_bf16 v0, v4, v19\n" \
  "v_dot2c_f32_bf16 v1, v19, v19\n" \
  "buffer_load_dwordx4 v[16:19], v96, %[csrd], %[cnt] offen offset:0\n" \
  "s_waitcnt vmcnt(9)\n" \
  "v_dot2c_f32_bf16 v0, v4, v20\n" \
  "v_dot2c_f32_bf16 v1, v20, v20\n" \
  "v_dot2c_f32_bf16 v0, v4, v21\n" \
  "v_dot2c_f32_bf16 v1, v21, v21\n" \
  "v_dot2c_f32_bf16 v0, v4, v22\n" \
  "v_dot2c_f32_bf16 v1, v22, v22\n" \
  "v_dot2c_f32_bf16 v0, v4, v23\n" \
  "v_dot2c_f32_bf16 v1, v23, v23\n" \
  "buffer_load_dwordx4 v[20:23], v96, %[csrd], %[cnt] offen offset:256\n" \
  "s_waitcnt vmcnt(9)\n" \
  "v_dot2c_f32_bf16 v0, v4, v24\n" \
  "v_dot2c_f32_bf16 v1, v24, v24\n" \
  "v_dot2c_f32_bf16 v0, v4, v25\n" \
  "v_dot2c_f32_bf16 v1, v25, v25\n" \
  "v_dot2c_f32_bf16 v0, v4, v26\n" \
  "v_dot2c_f32_bf16 v1, v26, v26\n" \
  "v_dot2c_f32_bf16 v0, v4, v27\n" \
  "v_dot2c_f32_bf16 v1, v27, v27\n" \
  "buffer_load_dwordx4 v[24:27], v96, %[csrd], %[cnt] offen offset:512\n" \
  "s_waitcnt vmcnt(9)\n" \
  "v_dot2c_f32_bf16 v0, v4, v28\n" \
  "v_dot2c_f32_bf16 v1, v28, v28\n" \
  "v_dot2c_f32_bf16 v0, v4, v29\n" \
  "v_dot2c_f32_bf16 v1, v29, v29\n" \
  "v_dot2c_f32_bf16 v0, v4, v30\n" \
  "v_dot2c_f32_bf16 v1, v30, v30\n" \
  "v_dot2c_f32_bf16 v0, v4, v31\n" \
  "v_dot2c_f32_bf16 v1, v31, v31\n" \
  "buffer_load_dwordx4 v[28:31], v96, %[csrd], %[cnt] offen offset:768\n" \
  "s_waitcnt vmcnt(9)\n" \
  "v_dot2c_f32_bf16 v0, v4, v32\n" \
  "v_dot2c_f32_bf16 v1, v32, v32\n" \
  "v_dot2c_f32_bf16 v0, v4, v33\n" \
  "v_dot2c_f32_bf16 v1, v33, v33\n" \
  "v_dot2c_f32_bf16 v0, v4, v34\n" \
  "v_dot2c_f32_bf16 v1, v34, v34\n" \
  "v_dot2c_f32_bf16 v0, v4, v35\n" \
  "v_dot2c_f32_bf16 v1, v35, v35\n" \
  "buffer_load_dwordx4 v[32:35], v96, %[csrd], %[cnt] offen offset:1024\n" \
  "s_add_u32 %[cnt], %[cnt], 5120\n" \
  "s_nop 1\n" \
  "v_add_f32_dpp v0, v0, v0 row_ror:8 row_mask:0xf bank_mask:0xf\n" \
  "v_add_f32_dpp v1, v1, v1 row_ror:8 row_mask:0xf bank_mask:0xf\n" \
  "s_nop 0\n" \
  "v_add_f32_dpp v0, v0, v0 row_ror:4 row_mask:0xf bank_mask:0xf\n" \
  "v_add_f32_dpp v1, v1, v1 row_ror:4 row_mask:0xf bank_mask:0xf\n" \
  "s_nop 0\n" \
  "v_add_f32_dpp v0, v0, v0 row_ror:2 row_mask:0xf bank_mask:0xf\n" \
  "v_add_f32_dpp v1, v1, v1 row_ror:2 row_mask:0xf bank_mask:0xf\n" \
  "s_nop 0\n" \
  "v_add_f32_dpp v0, v0, v0 row_ror:1 row_mask:0xf bank_mask:0xf\n" \
  "v_add_f32_dpp v1, v1, v1 row_ror:1 row_mask:0xf bank_mask:0xf\n" \
  "s_nop 0\n" \
  "v_mul_f32 v0, 0xbacccccd, v0\n" \
  "v_mul_f32 v1, 0x3acccccd, v1\n" \
  "v_fma_f32 v1, -v0, v0, v1\n" \
  "v_max_f32 v1, 0, v1\n" \
  "v_add_f32 v1, 0x3727c5ac, v1\n" \
  "v_rsq_f32 v1, v1\n" \
  "s_mov_b32 exec_lo, 0x30003\n" \
  "s_mov_b32 exec_hi, 0x30003\n" \
  "v_cndmask_b32 v2, v1, v0, vcc\n" \
  "buffer_store_dwordx2 v[2:3], v97, %[srd], 0 offen offset:0 sc1\n" \
  "s_mov_b64 exec, -1\n" \
  "v_mov_b64 v[0:1], 0\n" \
  "s_waitcnt vmcnt(9)\n" \
  "v_dot2c_f32_bf16 v0, v4, v36\n" \
  "v_dot2c_f32_bf16 v1, v36, v36\n" \
  "v_dot2c_f32_bf16 v0, v4, v37\n" \
  "v_dot2c_f32_bf16 v1, v37, v37\n" \
  "v_dot2c_f32_bf16 v0, v4, v38\n" \
  "v_dot2c_f32_bf16 v1, v38, v38\n" \
  "v_dot2c_f32_bf16 v0, v4, v39\n" \
  "v_dot2c_f32_bf16 v1, v39, v39\n" \
  "buffer_load_dwordx4 v[36:39], v96, %[csrd], %[cnt] offen offset:0\n" \
  "s_waitcnt vmcnt(9)\n" \
  "v_dot2c_f32_bf16 v0, v4, v40\n" \
  "v_dot2c_f32_bf16 v1, v40, v40\n" \
  "v_dot2c_f32_bf16 v0, v4, v41\n" \
  "v_dot2c_f32_bf16 v1, v41, v41\n" \
  "v_dot2c_f32_bf16 v0, v4, v42\n" \
  "v_dot2c_f32_bf16 v1, v42, v42\n" \
  "v_dot2c_f32_bf16 v0, v4, v43\n" \
  "v_dot2c_f32_bf16 v1, v43, v43\n" \
  "buffer_load_dwordx4 v[40:43], v96, %[csrd], %[cnt] offen offset:256\n" \
  "s_waitcnt vmcnt(9)\n" \
  "v_dot2c_f32_bf16 v0, v4, v44\n" \
  "v_dot2c_f32_bf16 v1, v44, v44\n" \
  "v_dot2c_f32_bf16 v0, v4, v45\n" \
  "v_dot2c_f32_bf16 v1, v45, v45\n" \
  "v_dot2c_f32_bf16 v0, v4, v46\n" \
  "v_dot2c_f32_bf16 v1, v46, v46\n" \
  "v_dot2c_f32_bf16 v0, v4, v47\n" \
  "v_dot2c_f32_bf16 v1, v47, v47\n" \
  "buffer_load_dwordx4 v[44:47], v96, %[csrd], %[cnt] offen offset:512\n" \
  "s_waitcnt vmcnt(9)\n" \
  "v_dot2c_f32_bf16 v0, v4, v48\n" \
  "v_dot2c_f32_bf16 v1, v48, v48\n" \
  "v_dot2c_f32_bf16 v0, v4, v49\n" \
  "v_dot2c_f32_bf16 v1, v49, v49\n" \
  "v_dot2c_f32_bf16 v0, v4, v50\n" \
  "v_dot2c_f32_bf16 v1, v50, v50\n" \
  "v_dot2c_f32_bf16 v0, v4, v51\n" \
  "v_dot2c_f32_bf16 v1, v51, v51\n" \
  "buffer_load_dwordx4 v[48:51], v96, %[csrd], %[cnt] offen offset:768\n" \
  "s_waitcnt vmcnt(9)\n" \
  "v_dot2c_f32_bf16 v0, v4, v52\n" \
  "v_dot2c_f32_bf16 v1, v52, v52\n" \
  "v_dot2c_f32_bf16 v0, v4, v53\n" \
  "v_dot2c_f32_bf16 v1, v53, v53\n" \
  "v_dot2c_f32_bf16 v0, v4, v54\n" \
  "v_dot2c_f32_bf16 v1, v54, v54\n" \
  "v_dot2c_f32_bf16 v0, v4, v55\n" \
  "v_dot2c_f32_bf16 v1, v55, v55\n" \
  "buffer_load_dwordx4 v[52:55], v96, %[csrd], %[cnt] offen offset:1024\n" \
  "s_add_u32 %[cnt], %[cnt], 5120\n" \
  "s_nop 1\n" \
  "v_add_f32_dpp v0, v0, v0 row_ror:8 row_mask:0xf bank_mask:0xf\n" \
  "v_add_f32_dpp v1, v1, v1 row_ror:8 row_mask:0xf bank_mask:0xf\n" \
  "s_nop 0\n" \
  "v_add_f32_dpp v0, v0, v0 row_ror:4 row_mask:0xf bank_mask:0xf\n" \
  "v_add_f32_dpp v1, v1, v1 row_ror:4 row_mask:0xf bank_mask:0xf\n" \
  "s_nop 0\n" \
  "v_add_f32_dpp v0, v0, v0 row_ror:2 row_mask:0xf bank_mask:0xf\n" \
  "v_add_f32_dpp v1, v1, v1 row_ror:2 row_mask:0xf bank_mask:0xf\n" \
  "s_nop 0\n" \
  "v_add_f32_dpp v0, v0, v0 row_ror:1 row_mask:0xf bank_mask:0xf\n" \
  "v_add_f32_dpp v1, v1, v1 row_ror:1 row_mask:0xf bank_mask:0xf\n" \
  "s_nop 0\n" \
  "v_mul_f32 v0, 0xbacccccd, v0\n" \
  "v_mul_f32 v1, 0x3acccccd, v1\n" \
  "v_fma_f32 v1, -v0, v0, v1\n" \
  "v_max_f32 v1, 0, v1\n" \
  "v_add_f32 v1, 0x3727c5ac, v1\n" \
  "v_rsq_f32 v1, v1\n" \
  "s_mov_b32 exec_lo, 0x30003\n" \
  "s_mov_b32 exec_hi, 0x30003\n" \
  "v_cndmask_b32 v2, v1, v0, vcc\n" \
  "buffer_store_dwordx2 v[2:3], v97, %[srd], 0 offen offset:64 sc1\n" \
  "s_mov_b64 exec, -1\n" \
  "v_mov_b64 v[0:1], 0\n" \
  "v_add_u32 v97, 128, v97\n" \
  "s_cmp_lt_u32 %[cnt], 51200\n" \
  "s_cbranch_scc1 1b\n" \
  "s_waitcnt vmcnt(0)\n" \
  "7:\n"
#define P1_CLOB "v0", "v1", "v2", "v3", "v4", "v5", "v6", "v7", "v8", "v9", "v10", "v11", "v12", "v13", "v14", "v15", "v16", "v17", "v18", "v19", "v20", "v21", "v22", "v23", "v24", "v25", "v26", "v27", "v28", "v29", "v30", "v31", "v32", "v33", "v34", "v35", "v36", "v37", "v38", "v39", "v40", "v41", "v42", "v43", "v44", "v45", "v46", "v47", "v48", "v49", "v50", "v51", "v52", "v53", "v54", "v55", "v56", "v57", "v58", "v59", "v60", "v61", "v62", "v63", "v64", "v65", "v66", "v67", "v68", "v69", "v70", "v71", "v72", "v73", "v74", "v75", "v76", "v77", "v78", "v79", "v80", "v81", "v82", "v83", "v84", "v85", "v86", "v87", "v88", "v89", "v90", "v91", "v92", "v93", "v94", "v95", "v112", "v113", "v114", "v115", "v116", "v117", "v118", "v119", "v120", "v121", "v122", "v123", "v124", "v125", "v126", "v127", "v128", "v129", "v130", "v131", "v132", "v133", "v134", "v135", "v136", "v137", "v138", "v139", "v140", "v141", "v142", "v143", "v144", "v145", "v146", "v147", "v148", "v149", "v150", "v151", "v152", "v153", "v154", "v155", "v156", "v157", "v158", "v159", "v160", "v161", "v162", "v163", "v164", "v165", "v166", "v167", "v168", "v169", "v170", "v171", "v172", "v173", "v174", "v175", "v176", "v177", "v178", "v179", "v180", "v181", "v182", "v183", "v184", "v185", "v186", "v187", "v188", "v189", "v190", "v191"
#define P2_ASM \
  "v_mov_b64 v[0:1], 0\n" \
  "v_mov_b64 v[2:3], 0\n" \
  "v_mov_b64 v[4:5], 0\n" \
  "v_mov_b64 v[6:7], 0\n" \
  "v_mov_b64 v[8:9], 0\n" \
  "v_mov_b64 v[10:11], 0\n" \
  "v_mov_b64 v[12:13], 0\n" \
  "v_mov_b64 v[14:15], 0\n" \
  "v_mov_b64 v[16:17], 0\n" \
  "v_mov_b64 v[18:19], 0\n" \
  "v_mov_b64 v[20:21], 0\n" \
  "v_mov_b64 v[22:23], 0\n" \
  "v_mov_b64 v[24:25], 0\n" \
  "v_mov_b64 v[26:27], 0\n" \
  "v_mov_b64 v[28:29], 0\n" \
  "v_mov_b64 v[30:31], 0\n" \
  "v_mov_b64 v[80:81], 0\n" \
  "v_mov_b64 v[82:83], 0\n" \
  "v_mov_b64 v[84:85], 0\n" \
  "v_mov_b64 v[86:87], 0\n" \
  "v_mov_b64 v[88:89], 0\n" \
  "v_mov_b64 v[90:91], 0\n" \
  "v_mov_b64 v[92:93], 0\n" \
  "v_mov_b64 v[94:95], 0\n" \
  "v_mov_b64 v[128:129], 0\n" \
  "v_mov_b64 v[130:131], 0\n" \
  "v_mov_b64 v[132:133], 0\n" \
  "v_mov_b64 v[134:135], 0\n" \
  "v_mov_b64 v[104:105], 0\n" \
  "v_mov_b64 v[106:107], 0\n" \
  "v_mov_b32 v144, 0xdddd\n" \
  "s_mov_b32 %[cnt], 3\n" \
  "v_add_u32 v146, %[ql], v136\n" \
  "v_add_u32 v147, %[ql], v137\n" \
  "s_cmp_eq_u64 %[lact], 0\n" \
  "s_cbranch_scc1 2f\n" \
  "v_mov_b32 v148, 1\n" \
  "v_mov_b32 v149, 163392\n" \
  "s_mov_b64 exec, 1\n" \
  "s_waitcnt vmcnt(0)\n" \
  "ds_add_rtn_u32 v150, v149, v148 offset:4\n" \
  "s_waitcnt lgkmcnt(0)\n" \
  "v_readfirstlane_b32 %[t0], v150\n" \
  "s_mov_b64 exec, %[lact]\n" \
  "s_cmp_lg_u32 %[t0], 3\n" \
  "s_cbranch_scc1 4f\n" \
  "buffer_atomic_add v148, v142, %[srd], 0 offen sc1\n" \
  "4:\n" \
  "s_mov_b64 exec, -1\n" \
  "2:\n" \
  "s_barrier\n" \
  "ds_read_b128 v[108:111], v136 offset:0\n" \
  "ds_read_b128 v[96:99], v146\n" \
  "ds_read_b128 v[100:103], v146 offset:1536\n" \
  "ds_read_b64 v[112:113], v137 offset:0\n" \
  "s_mov_b32 exec_hi, 0\n" \
  "ds_read_b64 v[104:105], v147\n" \
  "ds_read_b64 v[106:107], v147 offset:1536\n" \
  "s_mov_b32 exec_hi, -1\n" \
  "ds_read_b128 v[122:125], v136 offset:2880\n" \
  "ds_read_b64 v[126:127], v137 offset:2880\n" \
  "ds_read_b128 v[114:117], v138 offset:0\n" \
  "s_waitcnt lgkmcnt(3)\n" \
  "v_mfma_f32_32x32x16_bf16 v[32:47], v[108:111], v[96:99], 0\n" \
  "v_mfma_f32_32x32x16_bf16 v[64:79], v[108:111], v[100:103], 0\n" \
  "s_nop 7\n" \
  "v_mfma_f32_32x32x8_bf16 v[48:63], v[112:113], v[104:105], v[32:47]\n" \
  "v_mfma_f32_32x32x8_bf16 v[64:79], v[112:113], v[106:107], v[64:79]\n" \
  "s_nop 10\n" \
  "v_max3_f32 v146, v48, v49, v50\n" \
  "v_max3_f32 v147, v51, v52, v53\n" \
  "v_max3_f32 v146, v146, v54, v55\n" \
  "v_max3_f32 v147, v147, v56, v57\n" \
  "v_max3_f32 v146, v146, v58, v59\n" \
  "v_max3_f32 v147, v147, v60, v61\n" \
  "v_max3_f32 v146, v146, v62, v63\n" \
  "v_max3_f32 v148, v64, v65, v66\n" \
  "v_max3_f32 v149, v67, v68, v69\n" \
  "v_max3_f32 v148, v148, v70, v71\n" \
  "v_max3_f32 v149, v149, v72, v73\n" \
  "v_max3_f32 v148, v148, v74, v75\n" \
  "v_max3_f32 v149, v149, v76, v77\n" \
  "v_max3_f32 v148, v148, v78, v79\n" \
  "v_max_f32 v146, v146, v147\n" \
  "v_max_f32 v148, v148, v149\n" \
  "v_mov_b32 v147, v146\n" \
  "v_mov_b32 v149, v148\n" \
  "s_nop 0\n" \
  "v_permlane32_swap_b32 v146, v147\n" \
  "v_permlane32_swap_b32 v148, v149\n" \
  "s_mov_b32 exec_lo, 0\n" \
  "s_mov_b32 %[t0], 0x07060302\n" \
  "v_min_f32 v146, -v146, -v147\n" \
  "v_min_f32 v148, -v148, -v149\n" \
  "v_subrev_f32 v146, 0x42080000, v146\n" \
  "v_subrev_f32 v148, 0x42080000, v148\n" \
  "v_and_b32 v147, 0xffff0000, v146\n" \
  "v_and_b32 v149, 0xffff0000, v148\n" \
  "v_sub_f32 v147, v146, v147\n" \
  "v_sub_f32 v149, v148, v149\n" \
  "v_perm_b32 v104, v147, v146, %[t0]\n" \
  "v_perm_b32 v106, v149, v148, %[t0]\n" \
  "v_and_b32 v146, 0xffff0000, v147\n" \
  "v_and_b32 v148, 0xffff0000, v149\n" \
  "v_sub_f32 v146, v147, v146\n" \
  "v_sub_f32 v148, v149, v148\n" \
  "v_lshrrev_b32 v105, 16, v146\n" \
  "v_lshrrev_b32 v107, 16, v148\n" \
  "s_mov_b32 exec_lo, -1\n" \
  "s_nop 0\n" \
  "v_mfma_f32_32x32x8_bf16 v[32:47], v[112:113], v[104:105], v[32:47]\n" \
  "s_nop 10\n" \
  "1:\n" \
  "v_mfma_f32_32x32x8_bf16 v[48:63], v[112:113], v[106:107], 0\n" \
  "v_exp_f32 v32, v32\n" \
  "v_exp_f32 v33, v33\n" \
  "v_exp_f32 v34, v34\n" \
  "v_exp_f32 v35, v35\n" \
  "v_mfma_f32_32x32x16_bf16 v[48:63], v[108:111], v[100:103], v[48:63]\n" \
  "ds_read_b128 v[118:121], v138 offset:672\n" \
  "v_exp_f32 v36, v36\n" \
  "v_exp_f32 v37, v37\n" \
  "v_exp_f32 v38, v38\n" \
  "v_exp_f32 v39, v39\n" \
  "v_smfmac_f32_32x32x32_bf16 v[16:31], v[128:131], v[80:87], v144\n" \
  "v_exp_f32 v40, v40\n" \
  "v_exp_f32 v41, v41\n" \
  "v_exp_f32 v42, v42\n" \
  "v_exp_f32 v43, v43\n" \
  "v_smfmac_f32_32x32x32_bf16 v[16:31], v[132:135], v[88:95], v144\n" \
  "s_waitcnt lgkmcnt(0)\n" \
  "v_exp_f32 v44, v44\n" \
  "v_exp_f32 v45, v45\n" \
  "v_exp_f32 v46, v46\n" \
  "v_exp_f32 v47, v47\n" \
  "v_mfma_f32_32x32x8_bf16 v[64:79], v[126:127], v[104:105], 0\n" \
  "v_exp_f32 v48, v48\n" \
  "v_exp_f32 v49, v49\n" \
  "v_exp_f32 v50, v50\n" \
  "v_exp_f32 v51, v51\n" \
  "v_mfma_f32_32x32x16_bf16 v[64:79], v[122:125], v[96:99], v[64:79]\n" \
  "ds_read_b128 v[108:111], v136 offset:5760\n" \
  "v_exp_f32 v52, v52\n" \
  "v_exp_f32 v53, v53\n" \
  "v_exp_f32 v54, v54\n" \
  "v_exp_f32 v55, v55\n" \
  "v_smfmac_f32_32x32x32_bf16 v[0:15], v[114:117], v[32:39], v144\n" \
  "ds_read_b64 v[112:113], v137 offset:5760\n" \
  "v_exp_f32 v56, v56\n" \
  "v_exp_f32 v57, v57\n" \
  "v_exp_f32 v58, v58\n" \
  "v_exp_f32 v59, v59\n" \
  "v_smfmac_f32_32x32x32_bf16 v[0:15], v[118:121], v[40:47], v144\n" \
  "ds_read_b128 v[128:131], v138 offset:2880\n" \
  "v_exp_f32 v60, v60\n" \
  "v_exp_f32 v61, v61\n" \
  "v_exp_f32 v62, v62\n" \
  "v_exp_f32 v63, v63\n" \
  "v_mfma_f32_32x32x8_bf16 v[80:95], v[126:127], v[106:107], 0\n" \
  "v_exp_f32 v64, v64\n" \
  "v_exp_f32 v65, v65\n" \
  "v_exp_f32 v66, v66\n" \
  "v_exp_f32 v67, v67\n" \
  "v_mfma_f32_32x32x16_bf16 v[80:95], v[122:125], v[100:103], v[80:95]\n" \
  "ds_read_b128 v[132:135], v138 offset:3552\n" \
  "v_exp_f32 v68, v68\n" \
  "v_exp_f32 v69, v69\n" \
  "v_exp_f32 v70, v70\n" \
  "v_exp_f32 v71, v71\n" \
  "v_smfmac_f32_32x32x32_bf16 v[16:31], v[114:117], v[48:55], v144\n" \
  "v_exp_f32 v72, v72\n" \
  "v_exp_f32 v73, v73\n" \
  "v_exp_f32 v74, v74\n" \
  "v_exp_f32 v75, v75\n" \
  "v_smfmac_f32_32x32x32_bf16 v[16:31], v[118:121], v[56:63], v144\n" \
  "s_waitcnt lgkmcnt(0)\n" \
  "v_exp_f32 v76, v76\n" \
  "v_exp_f32 v77, v77\n" \
  "v_exp_f32 v78, v78\n" \
  "v_exp_f32 v79, v79\n" \
  "v_mfma_f32_32x32x8_bf16 v[32:47], v[112:113], v[104:105], 0\n" \
  "v_exp_f32 v80, v80\n" \
  "v_exp_f32 v81, v81\n" \
  "v_exp_f32 v82, v82\n" \
  "v_exp_f32 v83, v83\n" \
  "v_mfma_f32_32x32x16_bf16 v[32:47], v[108:111], v[96:99], v[32:47]\n" \
  "ds_read_b128 v[122:125], v136 offset:8640\n" \
  "v_exp_f32 v84, v84\n" \
  "v_exp_f32 v85, v85\n" \
  "v_exp_f32 v86, v86\n" \
  "v_exp_f32 v87, v87\n" \
  "v_smfmac_f32_32x32x32_bf16 v[0:15], v[128:131], v[64:71], v144\n" \
  "ds_read_b64 v[126:127], v137 offset:8640\n" \
  "v_exp_f32 v88, v88\n" \
  "v_exp_f32 v89, v89\n" \
  "v_exp_f32 v90, v90\n" \
  "v_exp_f32 v91, v91\n" \
  "v_smfmac_f32_32x32x32_bf16 v[0:15], v[132:135], v[72:79], v144\n" \
  "ds_read_b128 v[114:117], v138 offset:5760\n" \
  "v_exp_f32 v92, v92\n" \
  "v_exp_f32 v93, v93\n" \
  "v_exp_f32 v94, v94\n" \
  "v_exp_f32 v95, v95\n" \
  "v_mfma_f32_32x32x8_bf16 v[48:63], v[112:113], v[106:107], 0\n" \
  "v_exp_f32 v32, v32\n" \
  "v_exp_f32 v33, v33\n" \
  "v_exp_f32 v34, v34\n" \
  "v_exp_f32 v35, v35\n" \
  "v_mfma_f32_32x32x16_bf16 v[48:63], v[108:111], v[100:103], v[48:63]\n" \
  "ds_read_b128 v[118:121], v138 offset:6432\n" \
  "v_exp_f32 v36, v36\n" \
  "v_exp_f32 v37, v37\n" \
  "v_exp_f32 v38, v38\n" \
  "v_exp_f32 v39, v39\n" \
  "v_smfmac_f32_32x32x32_bf16 v[16:31], v[128:131], v[80:87], v144\n" \
  "v_exp_f32 v40, v40\n" \
  "v_exp_f32 v41, v41\n" \
  "v_exp_f32 v42, v42\n" \
  "v_exp_f32 v43, v43\n" \
  "v_smfmac_f32_32x32x32_bf16 v[16:31], v[132:135], v[88:95], v144\n" \
  "s_waitcnt lgkmcnt(0)\n" \
  "s_barrier\n" \
  "v_exp_f32 v44, v44\n" \
  "v_exp_f32 v45, v45\n" \
  "v_exp_f32 v46, v46\n" \
  "v_exp_f32 v47, v47\n" \
  "v_mfma_f32_32x32x8_bf16 v[64:79], v[126:127], v[104:105], 0\n" \
  "v_exp_f32 v48, v48\n" \
  "v_exp_f32 v49, v49\n" \
  "v_exp_f32 v50, v50\n" \
  "v_exp_f32 v51, v51\n" \
  "v_mfma_f32_32x32x16_bf16 v[64:79], v[122:125], v[96:99], v[64:79]\n" \
  "ds_read_b128 v[108:111], v136 offset:11520\n" \
  "v_exp_f32 v52, v52\n" \
  "v_exp_f32 v53, v53\n" \
  "v_exp_f32 v54, v54\n" \
  "v_exp_f32 v55, v55\n" \
  "v_smfmac_f32_32x32x32_bf16 v[0:15], v[114:117], v[32:39], v144\n" \
  "ds_read_b64 v[112:113], v137 offset:11520\n" \
  "v_exp_f32 v56, v56\n" \
  "v_exp_f32 v57, v57\n" \
  "v_exp_f32 v58, v58\n" \
  "v_exp_f32 v59, v59\n" \
  "v_smfmac_f32_32x32x32_bf16 v[0:15], v[118:121], v[40:47], v144\n" \
  "ds_read_b128 v[128:131], v138 offset:8640\n" \
  "v_exp_f32 v60, v60\n" \
  "v_exp_f32 v61, v61\n" \
  "v_exp_f32 v62, v62\n" \
  "v_exp_f32 v63, v63\n" \
  "v_mfma_f32_32x32x8_bf16 v[80:95], v[126:127], v[106:107], 0\n" \
  "v_exp_f32 v64, v64\n" \
  "v_exp_f32 v65, v65\n" \
  "v_exp_f32 v66, v66\n" \
  "v_exp_f32 v67, v67\n" \
  "v_mfma_f32_32x32x16_bf16 v[80:95], v[122:125], v[100:103], v[80:95]\n" \
  "ds_read_b128 v[132:135], v138 offset:9312\n" \
  "v_exp_f32 v68, v68\n" \
  "v_exp_f32 v69, v69\n" \
  "v_exp_f32 v70, v70\n" \
  "v_exp_f32 v71, v71\n" \
  "v_smfmac_f32_32x32x32_bf16 v[16:31], v[114:117], v[48:55], v144\n" \
  "v_exp_f32 v72, v72\n" \
  "v_exp_f32 v73, v73\n" \
  "v_exp_f32 v74, v74\n" \
  "v_exp_f32 v75, v75\n" \
  "v_smfmac_f32_32x32x32_bf16 v[16:31], v[118:121], v[56:63], v144\n" \
  "s_waitcnt lgkmcnt(0)\n" \
  "v_exp_f32 v76, v76\n" \
  "v_exp_f32 v77, v77\n" \
  "v_exp_f32 v78, v78\n" \
  "v_exp_f32 v79, v79\n" \
  "v_mfma_f32_32x32x8_bf16 v[32:47], v[112:113], v[104:105], 0\n" \
  "v_exp_f32 v80, v80\n" \
  "v_exp_f32 v81, v81\n" \
  "v_exp_f32 v82, v82\n" \
  "v_exp_f32 v83, v83\n" \
  "v_mfma_f32_32x32x16_bf16 v[32:47], v[108:111], v[96:99], v[32:47]\n" \
  "ds_read_b128 v[122:125], v136 offset:14400\n" \
  "v_exp_f32 v84, v84\n" \
  "v_exp_f32 v85, v85\n" \
  "v_exp_f32 v86, v86\n" \
  "v_exp_f32 v87, v87\n" \
  "v_smfmac_f32_32x32x32_bf16 v[0:15], v[128:131], v[64:71], v144\n" \
  "ds_read_b64 v[126:127], v137 offset:14400\n" \
  "v_exp_f32 v88, v88\n" \
  "v_exp_f32 v89, v89\n" \
  "v_exp_f32 v90, v90\n" \
  "v_exp_f32 v91, v91\n" \
  "v_smfmac_f32_32x32x32_bf16 v[0:15], v[132:135], v[72:79], v144\n" \
  "ds_read_b128 v[114:117], v138 offset:11520\n" \
  "v_exp_f32 v92, v92\n" \
  "v_exp_f32 v93, v93\n" \
  "v_exp_f32 v94, v94\n" \
  "v_exp_f32 v95, v95\n" \
  "v_mfma_f32_32x32x8_bf16 v[48:63], v[112:113], v[106:107], 0\n" \
  "v_exp_f32 v32, v32\n" \
  "v_exp_f32 v33, v33\n" \
  "v_exp_f32 v34, v34\n" \
  "v_exp_f32 v35, v35\n" \
  "v_mfma_f32_32x32x16_bf16 v[48:63], v[108:111], v[100:103], v[48:63]\n" \
  "ds_read_b128 v[118:121], v138 offset:12192\n" \
  "v_exp_f32 v36, v36\n" \
  "v_exp_f32 v37, v37\n" \
  "v_exp_f32 v38, v38\n" \
  "v_exp_f32 v39, v39\n" \
  "v_smfmac_f32_32x32x32_bf16 v[16:31], v[128:131], v[80:87], v144\n" \
  "v_exp_f32 v40, v40\n" \
  "v_exp_f32 v41, v41\n" \
  "v_exp_f32 v42, v42\n" \
  "v_exp_f32 v43, v43\n" \
  "v_smfmac_f32_32x32x32_bf16 v[16:31], v[132:135], v[88:95], v144\n" \
  "s_waitcnt lgkmcnt(0)\n" \
  "v_exp_f32 v44, v44\n" \
  "v_exp_f32 v45, v45\n" \
  "v_exp_f32 v46, v46\n" \
  "v_exp_f32 v47, v47\n" \
  "v_mfma_f32_32x32x8_bf16 v[64:79], v[126:127], v[104:105], 0\n" \
  "v_exp_f32 v48, v48\n" \
  "v_exp_f32 v49, v49\n" \
  "v_exp_f32 v50, v50\n" \
  "v_exp_f32 v51, v51\n" \
  "v_mfma_f32_32x32x16_bf16 v[64:79], v[122:125], v[96:99], v[64:79]\n" \
  "ds_read_b128 v[108:111], v136 offset:17280\n" \
  "v_exp_f32 v52, v52\n" \
  "v_exp_f32 v53, v53\n" \
  "v_exp_f32 v54, v54\n" \
  "v_exp_f32 v55, v55\n" \
  "v_smfmac_f32_32x32x32_bf16 v[0:15], v[114:117], v[32:39], v144\n" \
  "ds_read_b64 v[112:113], v137 offset:17280\n" \
  "v_exp_f32 v56, v56\n" \
  "v_exp_f32 v57, v57\n" \
  "v_exp_f32 v58, v58\n" \
  "v_exp_f32 v59, v59\n" \
  "v_smfmac_f32_32x32x32_bf16 v[0:15], v[118:121], v[40:47], v144\n" \
  "ds_read_b128 v[128:131], v138 offset:14400\n" \
  "v_exp_f32 v60, v60\n" \
  "v_exp_f32 v61, v61\n" \
  "v_exp_f32 v62, v62\n" \
  "v_exp_f32 v63, v63\n" \
  "v_mfma_f32_32x32x8_bf16 v[80:95], v[126:127], v[106:107], 0\n" \
  "v_exp_f32 v64, v64\n" \
  "v_exp_f32 v65, v65\n" \
  "v_exp_f32 v66, v66\n" \
  "v_exp_f32 v67, v67\n" \
  "v_mfma_f32_32x32x16_bf16 v[80:95], v[122:125], v[100:103], v[80:95]\n" \
  "ds_read_b128 v[132:135], v138 offset:15072\n" \
  "v_exp_f32 v68, v68\n" \
  "v_exp_f32 v69, v69\n" \
  "v_exp_f32 v70, v70\n" \
  "v_exp_f32 v71, v71\n" \
  "v_smfmac_f32_32x32x32_bf16 v[16:31], v[114:117], v[48:55], v144\n" \
  "v_exp_f32 v72, v72\n" \
  "v_exp_f32 v73, v73\n" \
  "v_exp_f32 v74, v74\n" \
  "v_exp_f32 v75, v75\n" \
  "v_smfmac_f32_32x32x32_bf16 v[16:31], v[118:121], v[56:63], v144\n" \
  "s_waitcnt lgkmcnt(0)\n" \
  "v_exp_f32 v76, v76\n" \
  "v_exp_f32 v77, v77\n" \
  "v_exp_f32 v78, v78\n" \
  "v_exp_f32 v79, v79\n" \
  "v_mfma_f32_32x32x8_bf16 v[32:47], v[112:113], v[104:105], 0\n" \
  "v_exp_f32 v80, v80\n" \
  "v_exp_f32 v81, v81\n" \
  "v_exp_f32 v82, v82\n" \
  "v_exp_f32 v83, v83\n" \
  "v_mfma_f32_32x32x16_bf16 v[32:47], v[108:111], v[96:99], v[32:47]\n" \
  "ds_read_b128 v[122:125], v136 offset:20160\n" \
  "v_exp_f32 v84, v84\n" \
  "v_exp_f32 v85, v85\n" \
  "v_exp_f32 v86, v86\n" \
  "v_exp_f32 v87, v87\n" \
  "v_smfmac_f32_32x32x32_bf16 v[0:15], v[128:131], v[64:71], v144\n" \
  "ds_read_b64 v[126:127], v137 offset:20160\n" \
  "v_exp_f32 v88, v88\n" \
  "v_exp_f32 v89, v89\n" \
  "v_exp_f32 v90, v90\n" \
  "v_exp_f32 v91, v91\n" \
  "v_smfmac_f32_32x32x32_bf16 v[0:15], v[132:135], v[72:79], v144\n" \
  "ds_read_b128 v[114:117], v138 offset:17280\n" \
  "v_exp_f32 v92, v92\n" \
  "v_exp_f32 v93, v93\n" \
  "v_exp_f32 v94, v94\n" \
  "v_exp_f32 v95, v95\n" \
  "v_mfma_f32_32x32x8_bf16 v[48:63], v[112:113], v[106:107], 0\n" \
  "v_exp_f32 v32, v32\n" \
  "v_exp_f32 v33, v33\n" \
  "v_exp_f32 v34, v34\n" \
  "v_exp_f32 v35, v35\n" \
  "v_mfma_f32_32x32x16_bf16 v[48:63], v[108:111], v[100:103], v[48:63]\n" \
  "ds_read_b128 v[118:121], v138 offset:17952\n" \
  "v_exp_f32 v36, v36\n" \
  "v_exp_f32 v37, v37\n" \
  "v_exp_f32 v38, v38\n" \
  "v_exp_f32 v39, v39\n" \
  "v_smfmac_f32_32x32x32_bf16 v[16:31], v[128:131], v[80:87], v144\n" \
  "v_exp_f32 v40, v40\n" \
  "v_exp_f32 v41, v41\n" \
  "v_exp_f32 v42, v42\n" \
  "v_exp_f32 v43, v43\n" \
  "v_smfmac_f32_32x32x32_bf16 v[16:31], v[132:135], v[88:95], v144\n" \
  "s_waitcnt lgkmcnt(0)\n" \
  "s_barrier\n" \
  "v_exp_f32 v44, v44\n" \
  "v_exp_f32 v45, v45\n" \
  "v_exp_f32 v46, v46\n" \
  "v_exp_f32 v47, v47\n" \
  "v_mfma_f32_32x32x8_bf16 v[64:79], v[126:127], v[104:105], 0\n" \
  "v_exp_f32 v48, v48\n" \
  "v_exp_f32 v49, v49\n" \
  "v_exp_f32 v50, v50\n" \
  "v_exp_f32 v51, v51\n" \
  "v_mfma_f32_32x32x16_bf16 v[64:79], v[122:125], v[96:99], v[64:79]\n" \
  "ds_read_b128 v[108:111], v136 offset:23040\n" \
  "v_exp_f32 v52, v52\n" \
  "v_exp_f32 v53, v53\n" \
  "v_exp_f32 v54, v54\n" \
  "v_exp_f32 v55, v55\n" \
  "v_smfmac_f32_32x32x32_bf16 v[0:15], v[114:117], v[32:39], v144\n" \
  "ds_read_b64 v[112:113], v137 offset:23040\n" \
  "v_exp_f32 v56, v56\n" \
  "v_exp_f32 v57, v57\n" \
  "v_exp_f32 v58, v58\n" \
  "v_exp_f32 v59, v59\n" \
  "v_smfmac_f32_32x32x32_bf16 v[0:15], v[118:121], v[40:47], v144\n" \
  "ds_read_b128 v[128:131], v138 offset:20160\n" \
  "v_exp_f32 v60, v60\n" \
  "v_exp_f32 v61, v61\n" \
  "v_exp_f32 v62, v62\n" \
  "v_exp_f32 v63, v63\n" \
  "v_mfma_f32_32x32x8_bf16 v[80:95], v[126:127], v[106:107], 0\n" \
  "v_exp_f32 v64, v64\n" \
  "v_exp_f32 v65, v65\n" \
  "v_exp_f32 v66, v66\n" \
  "v_exp_f32 v67, v67\n" \
  "v_mfma_f32_32x32x16_bf16 v[80:95], v[122:125], v[100:103], v[80:95]\n" \
  "ds_read_b128 v[132:135], v138 offset:20832\n" \
  "v_exp_f32 v68, v68\n" \
  "v_exp_f32 v69, v69\n" \
  "v_exp_f32 v70, v70\n" \
  "v_exp_f32 v71, v71\n" \
  "v_smfmac_f32_32x32x32_bf16 v[16:31], v[114:117], v[48:55], v144\n" \
  "v_exp_f32 v72, v72\n" \
  "v_exp_f32 v73, v73\n" \
  "v_exp_f32 v74, v74\n" \
  "v_exp_f32 v75, v75\n" \
  "v_smfmac_f32_32x32x32_bf16 v[16:31], v[118:121], v[56:63], v144\n" \
  "s_waitcnt lgkmcnt(0)\n" \
  "v_exp_f32 v76, v76\n" \
  "v_exp_f32 v77, v77\n" \
  "v_exp_f32 v78, v78\n" \
  "v_exp_f32 v79, v79\n" \
  "v_mfma_f32_32x32x8_bf16 v[32:47], v[112:113], v[104:105], 0\n" \
  "v_exp_f32 v80, v80\n" \
  "v_exp_f32 v81, v81\n" \
  "v_exp_f32 v82, v82\n" \
  "v_exp_f32 v83, v83\n" \
  "v_mfma_f32_32x32x16_bf16 v[32:47], v[108:111], v[96:99], v[32:47]\n" \
  "ds_read_b128 v[122:125], v136 offset:25920\n" \
  "v_exp_f32 v84, v84\n" \
  "v_exp_f32 v85, v85\n" \
  "v_exp_f32 v86, v86\n" \
  "v_exp_f32 v87, v87\n" \
  "v_smfmac_f32_32x32x32_bf16 v[0:15], v[128:131], v[64:71], v144\n" \
  "ds_read_b64 v[126:127], v137 offset:25920\n" \
  "v_exp_f32 v88, v88\n" \
  "v_exp_f32 v89, v89\n" \
  "v_exp_f32 v90, v90\n" \
  "v_exp_f32 v91, v91\n" \
  "v_smfmac_f32_32x32x32_bf16 v[0:15], v[132:135], v[72:79], v144\n" \
  "ds_read_b128 v[114:117], v138 offset:23040\n" \
  "v_exp_f32 v92, v92\n" \
  "v_exp_f32 v93, v93\n" \
  "v_exp_f32 v94, v94\n" \
  "v_exp_f32 v95, v95\n" \
  "v_mfma_f32_32x32x8_bf16 v[48:63], v[112:113], v[106:107], 0\n" \
  "v_exp_f32 v32, v32\n" \
  "v_exp_f32 v33, v33\n" \
  "v_exp_f32 v34, v34\n" \
  "v_exp_f32 v35, v35\n" \
  "v_mfma_f32_32x32x16_bf16 v[48:63], v[108:111], v[100:103], v[48:63]\n" \
  "ds_read_b128 v[118:121], v138 offset:23712\n" \
  "v_exp_f32 v36, v36\n" \
  "v_exp_f32 v37, v37\n" \
  "v_exp_f32 v38, v38\n" \
  "v_exp_f32 v39, v39\n" \
  "v_smfmac_f32_32x32x32_bf16 v[16:31], v[128:131], v[80:87], v144\n" \
  "v_exp_f32 v40, v40\n" \
  "v_exp_f32 v41, v41\n" \
  "v_exp_f32 v42, v42\n" \
  "v_exp_f32 v43, v43\n" \
  "v_smfmac_f32_32x32x32_bf16 v[16:31], v[132:135], v[88:95], v144\n" \
  "s_waitcnt lgkmcnt(0)\n" \
  "v_exp_f32 v44, v44\n" \
  "v_exp_f32 v45, v45\n" \
  "v_exp_f32 v46, v46\n" \
  "v_exp_f32 v47, v47\n" \
  "v_mfma_f32_32x32x8_bf16 v[64:79], v[126:127], v[104:105], 0\n" \
  "v_exp_f32 v48, v48\n" \
  "v_exp_f32 v49, v49\n" \
  "v_exp_f32 v50, v50\n" \
  "v_exp_f32 v51, v51\n" \
  "v_mfma_f32_32x32x16_bf16 v[64:79], v[122:125], v[96:99], v[64:79]\n" \
  "ds_read_b128 v[108:111], v136 offset:28800\n" \
  "v_exp_f32 v52, v52\n" \
  "v_exp_f32 v53, v53\n" \
  "v_exp_f32 v54, v54\n" \
  "v_exp_f32 v55, v55\n" \
  "v_smfmac_f32_32x32x32_bf16 v[0:15], v[114:117], v[32:39], v144\n" \
  "ds_read_b64 v[112:113], v137 offset:28800\n" \
  "v_exp_f32 v56, v56\n" \
  "v_exp_f32 v57, v57\n" \
  "v_exp_f32 v58, v58\n" \
  "v_exp_f32 v59, v59\n" \
  "v_smfmac_f32_32x32x32_bf16 v[0:15], v[118:121], v[40:47], v144\n" \
  "ds_read_b128 v[128:131], v138 offset:25920\n" \
  "v_exp_f32 v60, v60\n" \
  "v_exp_f32 v61, v61\n" \
  "v_exp_f32 v62, v62\n" \
  "v_exp_f32 v63, v63\n" \
  "v_mfma_f32_32x32x8_bf16 v[80:95], v[126:127], v[106:107], 0\n" \
  "v_exp_f32 v64, v64\n" \
  "v_exp_f32 v65, v65\n" \
  "v_exp_f32 v66, v66\n" \
  "v_exp_f32 v67, v67\n" \
  "v_mfma_f32_32x32x16_bf16 v[80:95], v[122:125], v[100:103], v[80:95]\n" \
  "ds_read_b128 v[132:135], v138 offset:26592\n" \
  "v_exp_f32 v68, v68\n" \
  "v_exp_f32 v69, v69\n" \
  "v_exp_f32 v70, v70\n" \
  "v_exp_f32 v71, v71\n" \
  "v_smfmac_f32_32x32x32_bf16 v[16:31], v[114:117], v[48:55], v144\n" \
  ".Lk_pad0:\n" \
  "s_cbranch_execz .Lk_ret0\n" \
  "v_exp_f32 v72, v72\n" \
  "v_exp_f32 v73, v73\n" \
  "v_exp_f32 v74, v74\n" \
  "v_exp_f32 v75, v75\n" \
  "v_smfmac_f32_32x32x32_bf16 v[16:31], v[118:121], v[56:63], v144\n" \
  "s_waitcnt lgkmcnt(0)\n" \
  "v_exp_f32 v76, v76\n" \
  "v_exp_f32 v77, v77\n" \
  "v_exp_f32 v78, v78\n" \
  "v_exp_f32 v79, v79\n" \
  "v_mfma_f32_32x32x8_bf16 v[32:47], v[112:113], v[104:105], 0\n" \
  "v_exp_f32 v80, v80\n" \
  "v_exp_f32 v81, v81\n" \
  "v_exp_f32 v82, v82\n" \
  "v_exp_f32 v83, v83\n" \
  "v_mfma_f32_32x32x16_bf16 v[32:47], v[108:111], v[96:99], v[32:47]\n" \
  "ds_read_b128 v[122:125], v136 offset:31680\n" \
  "v_exp_f32 v84, v84\n" \
  "v_exp_f32 v85, v85\n" \
  "v_exp_f32 v86, v86\n" \
  "v_exp_f32 v87, v87\n" \
  "v_smfmac_f32_32x32x32_bf16 v[0:15], v[128:131], v[64:71], v144\n" \
  "ds_read_b64 v[126:127], v137 offset:31680\n" \
  "v_exp_f32 v88, v88\n" \
  "v_exp_f32 v89, v89\n" \
  "v_exp_f32 v90, v90\n" \
  "v_exp_f32 v91, v91\n" \
  "v_smfmac_f32_32x32x32_bf16 v[0:15], v[132:135], v[72:79], v144\n" \
  "ds_read_b128 v[114:117], v138 offset:28800\n" \
  "v_exp_f32 v92, v92\n" \
  "v_exp_f32 v93, v93\n" \
  "v_exp_f32 v94, v94\n" \
  "v_exp_f32 v95, v95\n" \
  "v_mfma_f32_32x32x8_bf16 v[48:63], v[112:113], v[106:107], 0\n" \
  "v_exp_f32 v32, v32\n" \
  "v_exp_f32 v33, v33\n" \
  "v_exp_f32 v34, v34\n" \
  "v_exp_f32 v35, v35\n" \
  "v_mfma_f32_32x32x16_bf16 v[48:63], v[108:111], v[100:103], v[48:63]\n" \
  "ds_read_b128 v[118:121], v138 offset:29472\n" \
  "v_exp_f32 v36, v36\n" \
  "v_exp_f32 v37, v37\n" \
  "v_exp_f32 v38, v38\n" \
  "v_exp_f32 v39, v39\n" \
  "v_smfmac_f32_32x32x32_bf16 v[16:31], v[128:131], v[80:87], v144\n" \
  "v_exp_f32 v40, v40\n" \
  "v_exp_f32 v41, v41\n" \
  "v_exp_f32 v42, v42\n" \
  "v_exp_f32 v43, v43\n" \
  "v_smfmac_f32_32x32x32_bf16 v[16:31], v[132:135], v[88:95], v144\n" \
  "s_waitcnt lgkmcnt(0)\n" \
  "s_barrier\n" \
  "v_exp_f32 v44, v44\n" \
  "v_exp_f32 v45, v45\n" \
  "v_exp_f32 v46, v46\n" \
  "v_exp_f32 v47, v47\n" \
  "v_mfma_f32_32x32x8_bf16 v[64:79], v[126:127], v[104:105], 0\n" \
  "v_exp_f32 v48, v48\n" \
  "v_exp_f32 v49, v49\n" \
  "v_exp_f32 v50, v50\n" \
  "v_exp_f32 v51, v51\n" \
  "v_mfma_f32_32x32x16_bf16 v[64:79], v[122:125], v[96:99], v[64:79]\n" \
  "ds_read_b128 v[108:111], v136 offset:34560\n" \
  "v_exp_f32 v52, v52\n" \
  "v_exp_f32 v53, v53\n" \
  "v_exp_f32 v54, v54\n" \
  "v_exp_f32 v55, v55\n" \
  "v_smfmac_f32_32x32x32_bf16 v[0:15], v[114:117], v[32:39], v144\n" \
  "ds_read_b64 v[112:113], v137 offset:34560\n" \
  "v_exp_f32 v56, v56\n" \
  "v_exp_f32 v57, v57\n" \
  "v_exp_f32 v58, v58\n" \
  "v_exp_f32 v59, v59\n" \
  "v_smfmac_f32_32x32x32_bf16 v[0:15], v[118:121], v[40:47], v144\n" \
  "ds_read_b128 v[128:131], v138 offset:31680\n" \
  "v_exp_f32 v60, v60\n" \
  "v_exp_f32 v61, v61\n" \
  "v_exp_f32 v62, v62\n" \
  "v_exp_f32 v63, v63\n" \
  "v_mfma_f32_32x32x8_bf16 v[80:95], v[126:127], v[106:107], 0\n" \
  "v_exp_f32 v64, v64\n" \
  "v_exp_f32 v65, v65\n" \
  "v_exp_f32 v66, v66\n" \
  "v_exp_f32 v67, v67\n" \
  "v_mfma_f32_32x32x16_bf16 v[80:95], v[122:125], v[100:103], v[80:95]\n" \
  "ds_read_b128 v[132:135], v138 offset:32352\n" \
  "v_exp_f32 v68, v68\n" \
  "v_exp_f32 v69, v69\n" \
  "v_exp_f32 v70, v70\n" \
  "v_exp_f32 v71, v71\n" \
  "v_smfmac_f32_32x32x32_bf16 v[16:31], v[114:117], v[48:55], v144\n" \
  "v_exp_f32 v72, v72\n" \
  "v_exp_f32 v73, v73\n" \
  "v_exp_f32 v74, v74\n" \
  "v_exp_f32 v75, v75\n" \
  "v_smfmac_f32_32x32x32_bf16 v[16:31], v[118:121], v[56:63], v144\n" \
  "s_waitcnt lgkmcnt(0)\n" \
  "v_exp_f32 v76, v76\n" \
  "v_exp_f32 v77, v77\n" \
  "v_exp_f32 v78, v78\n" \
  "v_exp_f32 v79, v79\n" \
  "v_mfma_f32_32x32x8_bf16 v[32:47], v[112:113], v[104:105], 0\n" \
  "v_exp_f32 v80, v80\n" \
  "v_exp_f32 v81, v81\n" \
  "v_exp_f32 v82, v82\n" \
  "v_exp_f32 v83, v83\n" \
  "v_mfma_f32_32x32x16_bf16 v[32:47], v[108:111], v[96:99], v[32:47]\n" \
  "ds_read_b128 v[122:125], v136 offset:37440\n" \
  "v_exp_f32 v84, v84\n" \
  "v_exp_f32 v85, v85\n" \
  "v_exp_f32 v86, v86\n" \
  "v_exp_f32 v87, v87\n" \
  "v_smfmac_f32_32x32x32_bf16 v[0:15], v[128:131], v[64:71], v144\n" \
  "ds_read_b64 v[126:127], v137 offset:37440\n" \
  "v_exp_f32 v88, v88\n" \
  "v_exp_f32 v89, v89\n" \
  "v_exp_f32 v90, v90\n" \
  "v_exp_f32 v91, v91\n" \
  "v_smfmac_f32_32x32x32_bf16 v[0:15], v[132:135], v[72:79], v144\n" \
  "ds_read_b128 v[114:117], v138 offset:34560\n" \
  "v_exp_f32 v92, v92\n" \
  "v_exp_f32 v93, v93\n" \
  "v_exp_f32 v94, v94\n" \
  "v_exp_f32 v95, v95\n" \
  "v_mfma_f32_32x32x8_bf16 v[48:63], v[112:113], v[106:107], 0\n" \
  "v_exp_f32 v32, v32\n" \
  "v_exp_f32 v33, v33\n" \
  "v_exp_f32 v34, v34\n" \
  "v_exp_f32 v35, v35\n" \
  "v_mfma_f32_32x32x16_bf16 v[48:63], v[108:111], v[100:103], v[48:63]\n" \
  "ds_read_b128 v[118:121], v138 offset:35232\n" \
  "v_exp_f32 v36, v36\n" \
  "v_exp_f32 v37, v37\n" \
  "v_exp_f32 v38, v38\n" \
  "v_exp_f32 v39, v39\n" \
  "v_smfmac_f32_32x32x32_bf16 v[16:31], v[128:131], v[80:87], v144\n" \
  "v_exp_f32 v40, v40\n" \
  "v_exp_f32 v41, v41\n" \
  "v_exp_f32 v42, v42\n" \
  "v_exp_f32 v43, v43\n" \
  "v_smfmac_f32_32x32x32_bf16 v[16:31], v[132:135], v[88:95], v144\n" \
  "s_waitcnt lgkmcnt(0)\n" \
  "v_exp_f32 v44, v44\n" \
  "v_exp_f32 v45, v45\n" \
  "v_exp_f32 v46, v46\n" \
  "v_exp_f32 v47, v47\n" \
  "v_mfma_f32_32x32x8_bf16 v[64:79], v[126:127], v[104:105], 0\n" \
  "v_exp_f32 v48, v48\n" \
  "v_exp_f32 v49, v49\n" \
  "v_exp_f32 v50, v50\n" \
  "v_exp_f32 v51, v51\n" \
  "v_mfma_f32_32x32x16_bf16 v[64:79], v[122:125], v[96:99], v[64:79]\n" \
  "ds_read_b128 v[108:111], v136 offset:40320\n" \
  "v_exp_f32 v52, v52\n" \
  "v_exp_f32 v53, v53\n" \
  "v_exp_f32 v54, v54\n" \
  "v_exp_f32 v55, v55\n" \
  "v_smfmac_f32_32x32x32_bf16 v[0:15], v[114:117], v[32:39], v144\n" \
  "ds_read_b64 v[112:113], v137 offset:40320\n" \
  "v_exp_f32 v56, v56\n" \
  "v_exp_f32 v57, v57\n" \
  "v_exp_f32 v58, v58\n" \
  "v_exp_f32 v59, v59\n" \
  "v_smfmac_f32_32x32x32_bf16 v[0:15], v[118:121], v[40:47], v144\n" \
  "ds_read_b128 v[128:131], v138 offset:37440\n" \
  "v_exp_f32 v60, v60\n" \
  "v_exp_f32 v61, v61\n" \
  "v_exp_f32 v62, v62\n" \
  "v_exp_f32 v63, v63\n" \
  "v_mfma_f32_32x32x8_bf16 v[80:95], v[126:127], v[106:107], 0\n" \
  "v_exp_f32 v64, v64\n" \
  "v_exp_f32 v65, v65\n" \
  "v_exp_f32 v66, v66\n" \
  "v_exp_f32 v67, v67\n" \
  "v_mfma_f32_32x32x16_bf16 v[80:95], v[122:125], v[100:103], v[80:95]\n" \
  "ds_read_b128 v[132:135], v138 offset:38112\n" \
  "v_exp_f32 v68, v68\n" \
  "v_exp_f32 v69, v69\n" \
  "v_exp_f32 v70, v70\n" \
  "v_exp_f32 v71, v71\n" \
  "v_smfmac_f32_32x32x32_bf16 v[16:31], v[114:117], v[48:55], v144\n" \
  "v_exp_f32 v72, v72\n" \
  "v_exp_f32 v73, v73\n" \
  "v_exp_f32 v74, v74\n" \
  "v_exp_f32 v75, v75\n" \
  "v_smfmac_f32_32x32x32_bf16 v[16:31], v[118:121], v[56:63], v144\n" \
  "s_waitcnt lgkmcnt(0)\n" \
  "v_exp_f32 v76, v76\n" \
  "v_exp_f32 v77, v77\n" \
  "v_exp_f32 v78, v78\n" \
  "v_exp_f32 v79, v79\n" \
  "v_mfma_f32_32x32x8_bf16 v[32:47], v[112:113], v[104:105], 0\n" \
  "v_exp_f32 v80, v80\n" \
  "v_exp_f32 v81, v81\n" \
  "v_exp_f32 v82, v82\n" \
  "v_exp_f32 v83, v83\n" \
  "v_mfma_f32_32x32x16_bf16 v[32:47], v[108:111], v[96:99], v[32:47]\n" \
  "ds_read_b128 v[122:125], v136 offset:43200\n" \
  "v_exp_f32 v84, v84\n" \
  "v_exp_f32 v85, v85\n" \
  "v_exp_f32 v86, v86\n" \
  "v_exp_f32 v87, v87\n" \
  "v_smfmac_f32_32x32x32_bf16 v[0:15], v[128:131], v[64:71], v144\n" \
  "ds_read_b64 v[126:127], v137 offset:43200\n" \
  "v_exp_f32 v88, v88\n" \
  "v_exp_f32 v89, v89\n" \
  "v_exp_f32 v90, v90\n" \
  "v_exp_f32 v91, v91\n" \
  "v_smfmac_f32_32x32x32_bf16 v[0:15], v[132:135], v[72:79], v144\n" \
  "ds_read_b128 v[114:117], v138 offset:40320\n" \
  "v_exp_f32 v92, v92\n" \
  "v_exp_f32 v93, v93\n" \
  "v_exp_f32 v94, v94\n" \
  "v_exp_f32 v95, v95\n" \
  "v_mfma_f32_32x32x8_bf16 v[48:63], v[112:113], v[106:107], 0\n" \
  "v_exp_f32 v32, v32\n" \
  "v_exp_f32 v33, v33\n" \
  "v_exp_f32 v34, v34\n" \
  "v_exp_f32 v35, v35\n" \
  "v_mfma_f32_32x32x16_bf16 v[48:63], v[108:111], v[100:103], v[48:63]\n" \
  "ds_read_b128 v[118:121], v138 offset:40992\n" \
  "v_exp_f32 v36, v36\n" \
  "v_exp_f32 v37, v37\n" \
  "v_exp_f32 v38, v38\n" \
  "v_exp_f32 v39, v39\n" \
  "v_smfmac_f32_32x32x32_bf16 v[16:31], v[128:131], v[80:87], v144\n" \
  "s_sub_u32 %[cnt], %[cnt], 1\n" \
  "v_exp_f32 v40, v40\n" \
  "v_exp_f32 v41, v41\n" \
  "v_exp_f32 v42, v42\n" \
  "v_exp_f32 v43, v43\n" \
  "v_smfmac_f32_32x32x32_bf16 v[16:31], v[132:135], v[88:95], v144\n" \
  "s_waitcnt lgkmcnt(0)\n" \
  "s_barrier\n" \
  "v_exp_f32 v44, v44\n" \
  "v_exp_f32 v45, v45\n" \
  "v_exp_f32 v46, v46\n" \
  "v_exp_f32 v47, v47\n" \
  "v_mfma_f32_32x32x8_bf16 v[64:79], v[126:127], v[104:105], 0\n" \
  "v_exp_f32 v48, v48\n" \
  "v_exp_f32 v49, v49\n" \
  "v_exp_f32 v50, v50\n" \
  "v_exp_f32 v51, v51\n" \
  "v_mfma_f32_32x32x16_bf16 v[64:79], v[122:125], v[96:99], v[64:79]\n" \
  "ds_read_b128 v[108:111], v136 offset:0\n" \
  "v_exp_f32 v52, v52\n" \
  "v_exp_f32 v53, v53\n" \
  "v_exp_f32 v54, v54\n" \
  "v_exp_f32 v55, v55\n" \
  "v_smfmac_f32_32x32x32_bf16 v[0:15], v[114:117], v[32:39], v144\n" \
  "ds_read_b64 v[112:113], v137 offset:0\n" \
  "v_exp_f32 v56, v56\n" \
  "v_exp_f32 v57, v57\n" \
  "v_exp_f32 v58, v58\n" \
  "v_exp_f32 v59, v59\n" \
  "v_smfmac_f32_32x32x32_bf16 v[0:15], v[118:121], v[40:47], v144\n" \
  "ds_read_b128 v[128:131], v138 offset:43200\n" \
  "v_exp_f32 v60, v60\n" \
  "v_exp_f32 v61, v61\n" \
  "v_exp_f32 v62, v62\n" \
  "v_exp_f32 v63, v63\n" \
  "v_mfma_f32_32x32x8_bf16 v[80:95], v[126:127], v[106:107], 0\n" \
  "v_exp_f32 v64, v64\n" \
  "v_exp_f32 v65, v65\n" \
  "v_exp_f32 v66, v66\n" \
  "v_exp_f32 v67, v67\n" \
  "v_mfma_f32_32x32x16_bf16 v[80:95], v[122:125], v[100:103], v[80:95]\n" \
  "ds_read_b128 v[132:135], v138 offset:43872\n" \
  "v_exp_f32 v68, v68\n" \
  "v_exp_f32 v69, v69\n" \
  "v_exp_f32 v70, v70\n" \
  "v_exp_f32 v71, v71\n" \
  "v_smfmac_f32_32x32x32_bf16 v[16:31], v[114:117], v[48:55], v144\n" \
  "s_cmp_lg_u32 %[cnt], 0\n" \
  "v_exp_f32 v72, v72\n" \
  "v_exp_f32 v73, v73\n" \
  "v_exp_f32 v74, v74\n" \
  "v_exp_f32 v75, v75\n" \
  "v_smfmac_f32_32x32x32_bf16 v[16:31], v[118:121], v[56:63], v144\n" \
  "s_waitcnt lgkmcnt(0)\n" \
  "v_exp_f32 v76, v76\n" \
  "v_exp_f32 v77, v77\n" \
  "v_exp_f32 v78, v78\n" \
  "v_exp_f32 v79, v79\n" \
  "v_mfma_f32_32x32x8_bf16 v[32:47], v[112:113], v[104:105], 0\n" \
  "v_exp_f32 v80, v80\n" \
  "v_exp_f32 v81, v81\n" \
  "v_exp_f32 v82, v82\n" \
  "v_exp_f32 v83, v83\n" \
  "v_mfma_f32_32x32x16_bf16 v[32:47], v[108:111], v[96:99], v[32:47]\n" \
  "ds_read_b128 v[122:125], v136 offset:2880\n" \
  "v_exp_f32 v84, v84\n" \
  "v_exp_f32 v85, v85\n" \
  "v_exp_f32 v86, v86\n" \
  "v_exp_f32 v87, v87\n" \
  "v_smfmac_f32_32x32x32_bf16 v[0:15], v[128:131], v[64:71], v144\n" \
  "ds_read_b64 v[126:127], v137 offset:2880\n" \
  "v_exp_f32 v88, v88\n" \
  "v_exp_f32 v89, v89\n" \
  "v_exp_f32 v90, v90\n" \
  "v_exp_f32 v91, v91\n" \
  "v_smfmac_f32_32x32x32_bf16 v[0:15], v[132:135], v[72:79], v144\n" \
  "ds_read_b128 v[114:117], v138 offset:0\n" \
  "v_exp_f32 v92, v92\n" \
  "v_exp_f32 v93, v93\n" \
  "v_exp_f32 v94, v94\n" \
  "v_exp_f32 v95, v95\n" \
  "s_cbranch_scc1 1b\n" \
  "v_mfma_f32_32x32x8_bf16 v[48:63], v[112:113], v[106:107], 0\n" \
  "v_exp_f32 v32, v32\n" \
  "v_exp_f32 v33, v33\n" \
  "v_exp_f32 v34, v34\n" \
  "v_exp_f32 v35, v35\n" \
  "v_mfma_f32_32x32x16_bf16 v[48:63], v[108:111], v[100:103], v[48:63]\n" \
  "ds_read_b128 v[118:121], v138 offset:672\n" \
  "v_exp_f32 v36, v36\n" \
  "v_exp_f32 v37, v37\n" \
  "v_exp_f32 v38, v38\n" \
  "v_exp_f32 v39, v39\n" \
  "v_smfmac_f32_32x32x32_bf16 v[16:31], v[128:131], v[80:87], v144\n" \
  "v_exp_f32 v40, v40\n" \
  "v_exp_f32 v41, v41\n" \
  "v_exp_f32 v42, v42\n" \
  "v_exp_f32 v43, v43\n" \
  "v_smfmac_f32_32x32x32_bf16 v[16:31], v[132:135], v[88:95], v144\n" \
  "s_waitcnt lgkmcnt(0)\n" \
  "v_exp_f32 v44, v44\n" \
  "v_exp_f32 v45, v45\n" \
  "v_exp_f32 v46, v46\n" \
  "v_exp_f32 v47, v47\n" \
  "v_mfma_f32_32x32x8_bf16 v[64:79], v[126:127], v[104:105], 0\n" \
  "v_exp_f32 v48, v48\n" \
  "v_exp_f32 v49, v49\n" \
  "v_exp_f32 v50, v50\n" \
  "v_exp_f32 v51, v51\n" \
  "v_mfma_f32_32x32x16_bf16 v[64:79], v[122:125], v[96:99], v[64:79]\n" \
  "ds_read_b128 v[108:111], v136 offset:5760\n" \
  "v_exp_f32 v52, v52\n" \
  "v_exp_f32 v53, v53\n" \
  "v_exp_f32 v54, v54\n" \
  "v_exp_f32 v55, v55\n" \
  "v_smfmac_f32_32x32x32_bf16 v[0:15], v[114:117], v[32:39], v144\n" \
  "ds_read_b64 v[112:113], v137 offset:5760\n" \
  "v_exp_f32 v56, v56\n" \
  "v_exp_f32 v57, v57\n" \
  "v_exp_f32 v58, v58\n" \
  "v_exp_f32 v59, v59\n" \
  "v_smfmac_f32_32x32x32_bf16 v[0:15], v[118:121], v[40:47], v144\n" \
  "ds_read_b128 v[128:131], v138 offset:2880\n" \
  "v_exp_f32 v60, v60\n" \
  "v_exp_f32 v61, v61\n" \
  "v_exp_f32 v62, v62\n" \
  "v_exp_f32 v63, v63\n" \
  "v_mfma_f32_32x32x8_bf16 v[80:95], v[126:127], v[106:107], 0\n" \
  "v_exp_f32 v64, v64\n" \
  "v_exp_f32 v65, v65\n" \
  "v_exp_f32 v66, v66\n" \
  "v_exp_f32 v67, v67\n" \
  "v_mfma_f32_32x32x16_bf16 v[80:95], v[122:125], v[100:103], v[80:95]\n" \
  "ds_read_b128 v[132:135], v138 offset:3552\n" \
  "v_exp_f32 v68, v68\n" \
  "v_exp_f32 v69, v69\n" \
  "v_exp_f32 v70, v70\n" \
  "v_exp_f32 v71, v71\n" \
  "v_smfmac_f32_32x32x32_bf16 v[16:31], v[114:117], v[48:55], v144\n" \
  "v_exp_f32 v72, v72\n" \
  "v_exp_f32 v73, v73\n" \
  "v_exp_f32 v74, v74\n" \
  "v_exp_f32 v75, v75\n" \
  "v_smfmac_f32_32x32x32_bf16 v[16:31], v[118:121], v[56:63], v144\n" \
  "s_waitcnt lgkmcnt(0)\n" \
  "v_exp_f32 v76, v76\n" \
  "v_exp_f32 v77, v77\n" \
  "v_exp_f32 v78, v78\n" \
  "v_exp_f32 v79, v79\n" \
  "v_mfma_f32_32x32x8_bf16 v[32:47], v[112:113], v[104:105], 0\n" \
  "v_exp_f32 v80, v80\n" \
  "v_exp_f32 v81, v81\n" \
  "v_exp_f32 v82, v82\n" \
  "v_exp_f32 v83, v83\n" \
  "v_mfma_f32_32x32x16_bf16 v[32:47], v[108:111], v[96:99], v[32:47]\n" \
  "ds_read_b128 v[122:125], v136 offset:8640\n" \
  "v_exp_f32 v84, v84\n" \
  "v_exp_f32 v85, v85\n" \
  "v_exp_f32 v86, v86\n" \
  "v_exp_f32 v87, v87\n" \
  "v_smfmac_f32_32x32x32_bf16 v[0:15], v[128:131], v[64:71], v144\n" \
  "ds_read_b64 v[126:127], v137 offset:8640\n" \
  "v_exp_f32 v88, v88\n" \
  "v_exp_f32 v89, v89\n" \
  "v_exp_f32 v90, v90\n" \
  "v_exp_f32 v91, v91\n" \
  "v_smfmac_f32_32x32x32_bf16 v[0:15], v[132:135], v[72:79], v144\n" \
  "ds_read_b128 v[114:117], v138 offset:5760\n" \
  "v_exp_f32 v92, v92\n" \
  "v_exp_f32 v93, v93\n" \
  "v_exp_f32 v94, v94\n" \
  "v_exp_f32 v95, v95\n" \
  "v_mfma_f32_32x32x8_bf16 v[48:63], v[112:113], v[106:107], 0\n" \
  "v_exp_f32 v32, v32\n" \
  "v_exp_f32 v33, v33\n" \
  "v_exp_f32 v34, v34\n" \
  "v_exp_f32 v35, v35\n" \
  "v_mfma_f32_32x32x16_bf16 v[48:63], v[108:111], v[100:103], v[48:63]\n" \
  "ds_read_b128 v[118:121], v138 offset:6432\n" \
  "v_exp_f32 v36, v36\n" \
  "v_exp_f32 v37, v37\n" \
  "v_exp_f32 v38, v38\n" \
  "v_exp_f32 v39, v39\n" \
  "v_smfmac_f32_32x32x32_bf16 v[16:31], v[128:131], v[80:87], v144\n" \
  "v_exp_f32 v40, v40\n" \
  "v_exp_f32 v41, v41\n" \
  "v_exp_f32 v42, v42\n" \
  "v_exp_f32 v43, v43\n" \
  "v_smfmac_f32_32x32x32_bf16 v[16:31], v[132:135], v[88:95], v144\n" \
  "s_waitcnt lgkmcnt(0)\n" \
  "s_barrier\n" \
  "v_exp_f32 v44, v44\n" \
  "v_exp_f32 v45, v45\n" \
  "v_exp_f32 v46, v46\n" \
  "v_exp_f32 v47, v47\n" \
  "v_mfma_f32_32x32x8_bf16 v[64:79], v[126:127], v[104:105], 0\n" \
  "v_exp_f32 v48, v48\n" \
  "v_exp_f32 v49, v49\n" \
  "v_exp_f32 v50, v50\n" \
  "v_exp_f32 v51, v51\n" \
  "v_mfma_f32_32x32x16_bf16 v[64:79], v[122:125], v[96:99], v[64:79]\n" \
  "ds_read_b128 v[108:111], v136 offset:11520\n" \
  "v_exp_f32 v52, v52\n" \
  "v_exp_f32 v53, v53\n" \
  "v_exp_f32 v54, v54\n" \
  "v_exp_f32 v55, v55\n" \
  "v_smfmac_f32_32x32x32_bf16 v[0:15], v[114:117], v[32:39], v144\n" \
  "ds_read_b64 v[112:113], v137 offset:11520\n" \
  "v_exp_f32 v56, v56\n" \
  "v_exp_f32 v57, v57\n" \
  "v_exp_f32 v58, v58\n" \
  "v_exp_f32 v59, v59\n" \
  "v_smfmac_f32_32x32x32_bf16 v[0:15], v[118:121], v[40:47], v144\n" \
  "ds_read_b128 v[128:131], v138 offset:8640\n" \
  "v_exp_f32 v60, v60\n" \
  "v_exp_f32 v61, v61\n" \
  "v_exp_f32 v62, v62\n" \
  "v_exp_f32 v63, v63\n" \
  "v_mfma_f32_32x32x8_bf16 v[80:95], v[126:127], v[106:107], 0\n" \
  "v_exp_f32 v64, v64\n" \
  "v_exp_f32 v65, v65\n" \
  "v_exp_f32 v66, v66\n" \
  "v_exp_f32 v67, v67\n" \
  "v_mfma_f32_32x32x16_bf16 v[80:95], v[122:125], v[100:103], v[80:95]\n" \
  "ds_read_b128 v[132:135], v138 offset:9312\n" \
  "v_exp_f32 v68, v68\n" \
  "v_exp_f32 v69, v69\n" \
  "v_exp_f32 v70, v70\n" \
  "v_exp_f32 v71, v71\n" \
  "v_smfmac_f32_32x32x32_bf16 v[16:31], v[114:117], v[48:55], v144\n" \
  "v_exp_f32 v72, v72\n" \
  "v_exp_f32 v73, v73\n" \
  "v_exp_f32 v74, v74\n" \
  "v_exp_f32 v75, v75\n" \
  "v_smfmac_f32_32x32x32_bf16 v[16:31], v[118:121], v[56:63], v144\n" \
  "s_waitcnt lgkmcnt(0)\n" \
  "v_exp_f32 v76, v76\n" \
  "v_exp_f32 v77, v77\n" \
  "v_exp_f32 v78, v78\n" \
  "v_exp_f32 v79, v79\n" \
  "v_mfma_f32_32x32x8_bf16 v[32:47], v[112:113], v[104:105], 0\n" \
  "v_exp_f32 v80, v80\n" \
  "v_exp_f32 v81, v81\n" \
  "v_exp_f32 v82, v82\n" \
  "v_exp_f32 v83, v83\n" \
  "v_mfma_f32_32x32x16_bf16 v[32:47], v[108:111], v[96:99], v[32:47]\n" \
  "ds_read_b128 v[122:125], v136 offset:14400\n" \
  "v_exp_f32 v84, v84\n" \
  "v_exp_f32 v85, v85\n" \
  "v_exp_f32 v86, v86\n" \
  "v_exp_f32 v87, v87\n" \
  "v_smfmac_f32_32x32x32_bf16 v[0:15], v[128:131], v[64:71], v144\n" \
  "ds_read_b64 v[126:127], v137 offset:14400\n" \
  "v_exp_f32 v88, v88\n" \
  "v_exp_f32 v89, v89\n" \
  "v_exp_f32 v90, v90\n" \
  "v_exp_f32 v91, v91\n" \
  "v_smfmac_f32_32x32x32_bf16 v[0:15], v[132:135], v[72:79], v144\n" \
  "ds_read_b128 v[114:117], v138 offset:11520\n" \
  "v_exp_f32 v92, v92\n" \
  "v_exp_f32 v93, v93\n" \
  "v_exp_f32 v94, v94\n" \
  "v_exp_f32 v95, v95\n" \
  "v_mfma_f32_32x32x8_bf16 v[48:63], v[112:113], v[106:107], 0\n" \
  "v_exp_f32 v32, v32\n" \
  "v_exp_f32 v33, v33\n" \
  "v_exp_f32 v34, v34\n" \
  "v_exp_f32 v35, v35\n" \
  "v_mfma_f32_32x32x16_bf16 v[48:63], v[108:111], v[100:103], v[48:63]\n" \
  "ds_read_b128 v[118:121], v138 offset:12192\n" \
  "v_exp_f32 v36, v36\n" \
  "v_exp_f32 v37, v37\n" \
  "v_exp_f32 v38, v38\n" \
  "v_exp_f32 v39, v39\n" \
  "v_smfmac_f32_32x32x32_bf16 v[16:31], v[128:131], v[80:87], v144\n" \
  "v_exp_f32 v40, v40\n" \
  "v_exp_f32 v41, v41\n" \
  "v_exp_f32 v42, v42\n" \
  "v_exp_f32 v43, v43\n" \
  "v_smfmac_f32_32x32x32_bf16 v[16:31], v[132:135], v[88:95], v144\n" \
  "s_waitcnt lgkmcnt(0)\n" \
  "v_exp_f32 v44, v44\n" \
  "v_exp_f32 v45, v45\n" \
  "v_exp_f32 v46, v46\n" \
  "v_exp_f32 v47, v47\n" \
  "v_mfma_f32_32x32x8_bf16 v[64:79], v[126:127], v[104:105], 0\n" \
  "s_add_u32 %[t0], %[w1s], 0\n" \
  "buffer_load_dwordx4 v[164:167], v141, %[srd], %[t0] offen offset:0 sc1\n" \
  "v_exp_f32 v48, v48\n" \
  "v_exp_f32 v49, v49\n" \
  "v_exp_f32 v50, v50\n" \
  "v_exp_f32 v51, v51\n" \
  "v_mfma_f32_32x32x16_bf16 v[64:79], v[122:125], v[96:99], v[64:79]\n" \
  "ds_read_b128 v[108:111], v136 offset:17280\n" \
  "v_exp_f32 v52, v52\n" \
  "v_exp_f32 v53, v53\n" \
  "v_exp_f32 v54, v54\n" \
  "v_exp_f32 v55, v55\n" \
  "v_smfmac_f32_32x32x32_bf16 v[0:15], v[114:117], v[32:39], v144\n" \
  "ds_read_b64 v[112:113], v137 offset:17280\n" \
  "v_exp_f32 v56, v56\n" \
  "v_exp_f32 v57, v57\n" \
  "v_exp_f32 v58, v58\n" \
  "v_exp_f32 v59, v59\n" \
  "v_smfmac_f32_32x32x32_bf16 v[0:15], v[118:121], v[40:47], v144\n" \
  "ds_read_b128 v[128:131], v138 offset:14400\n" \
  "v_exp_f32 v60, v60\n" \
  "v_exp_f32 v61, v61\n" \
  "v_exp_f32 v62, v62\n" \
  "v_exp_f32 v63, v63\n" \
  "v_mfma_f32_32x32x8_bf16 v[80:95], v[126:127], v[106:107], 0\n" \
  "v_exp_f32 v64, v64\n" \
  "v_exp_f32 v65, v65\n" \
  "v_exp_f32 v66, v66\n" \
  "v_exp_f32 v67, v67\n" \
  "v_mfma_f32_32x32x16_bf16 v[80:95], v[122:125], v[100:103], v[80:95]\n" \
  "ds_read_b128 v[132:135], v138 offset:15072\n" \
  "v_exp_f32 v68, v68\n" \
  "v_exp_f32 v69, v69\n" \
  "v_exp_f32 v70, v70\n" \
  "v_exp_f32 v71, v71\n" \
  "v_smfmac_f32_32x32x32_bf16 v[16:31], v[114:117], v[48:55], v144\n" \
  "buffer_load_dwordx4 v[168:171], v141, %[srd], %[t0] offen offset:1024 sc1\n" \
  "v_exp_f32 v72, v72\n" \
  "v_exp_f32 v73, v73\n" \
  "v_exp_f32 v74, v74\n" \
  "v_exp_f32 v75, v75\n" \
  "v_smfmac_f32_32x32x32_bf16 v[16:31], v[118:121], v[56:63], v144\n" \
  "s_waitcnt lgkmcnt(0)\n" \
  "v_exp_f32 v76, v76\n" \
  "v_exp_f32 v77, v77\n" \
  "v_exp_f32 v78, v78\n" \
  "v_exp_f32 v79, v79\n" \
  "v_mfma_f32_32x32x8_bf16 v[32:47], v[112:113], v[104:105], 0\n" \
  "buffer_load_dwordx4 v[172:175], v141, %[srd], %[t0] offen offset:2048 sc1\n" \
  "v_exp_f32 v80, v80\n" \
  "v_exp_f32 v81, v81\n" \
  "v_exp_f32 v82, v82\n" \
  "v_exp_f32 v83, v83\n" \
  "v_mfma_f32_32x32x16_bf16 v[32:47], v[108:111], v[96:99], v[32:47]\n" \
  "ds_read_b128 v[122:125], v136 offset:20160\n" \
  "v_exp_f32 v84, v84\n" \
  "v_exp_f32 v85, v85\n" \
  "v_exp_f32 v86, v86\n" \
  "v_exp_f32 v87, v87\n" \
  "v_smfmac_f32_32x32x32_bf16 v[0:15], v[128:131], v[64:71], v144\n" \
  "ds_read_b64 v[126:127], v137 offset:20160\n" \
  "v_exp_f32 v88, v88\n" \
  "v_exp_f32 v89, v89\n" \
  "v_exp_f32 v90, v90\n" \
  "v_exp_f32 v91, v91\n" \
  "v_smfmac_f32_32x32x32_bf16 v[0:15], v[132:135], v[72:79], v144\n" \
  "ds_read_b128 v[114:117], v138 offset:17280\n" \
  "v_exp_f32 v92, v92\n" \
  "v_exp_f32 v93, v93\n" \
  "v_exp_f32 v94, v94\n" \
  "v_exp_f32 v95, v95\n" \
  "v_mfma_f32_32x32x8_bf16 v[48:63], v[112:113], v[106:107], 0\n" \
  "v_exp_f32 v32, v32\n" \
  "v_exp_f32 v33, v33\n" \
  "v_exp_f32 v34, v34\n" \
  "v_exp_f32 v35, v35\n" \
  "v_mfma_f32_32x32x16_bf16 v[48:63], v[108:111], v[100:103], v[48:63]\n" \
  "ds_read_b128 v[118:121], v138 offset:17952\n" \
  "v_exp_f32 v36, v36\n" \
  "v_exp_f32 v37, v37\n" \
  "v_exp_f32 v38, v38\n" \
  "v_exp_f32 v39, v39\n" \
  "v_smfmac_f32_32x32x32_bf16 v[16:31], v[128:131], v[80:87], v144\n" \
  "buffer_load_dwordx4 v[176:179], v141, %[srd], %[t0] offen offset:3072 sc1\n" \
  "v_exp_f32 v40, v40\n" \
  "v_exp_f32 v41, v41\n" \
  "v_exp_f32 v42, v42\n" \
  "v_exp_f32 v43, v43\n" \
  "v_smfmac_f32_32x32x32_bf16 v[16:31], v[132:135], v[88:95], v144\n" \
  "s_waitcnt lgkmcnt(0)\n" \
  "s_barrier\n" \
  "v_exp_f32 v44, v44\n" \
  "v_exp_f32 v45, v45\n" \
  "v_exp_f32 v46, v46\n" \
  "v_exp_f32 v47, v47\n" \
  "v_mfma_f32_32x32x8_bf16 v[64:79], v[126:127], v[104:105], 0\n" \
  "s_add_u32 %[t0], %[w1s], 4096\n" \
  "buffer_load_dwordx4 v[180:183], v141, %[srd], %[t0] offen offset:0 sc1\n" \
  "v_exp_f32 v48, v48\n" \
  "v_exp_f32 v49, v49\n" \
  "v_exp_f32 v50, v50\n" \
  "v_exp_f32 v51, v51\n" \
  "v_mfma_f32_32x32x16_bf16 v[64:79], v[122:125], v[96:99], v[64:79]\n" \
  "ds_read_b128 v[108:111], v136 offset:23040\n" \
  "v_exp_f32 v52, v52\n" \
  "v_exp_f32 v53, v53\n" \
  "v_exp_f32 v54, v54\n" \
  "v_exp_f32 v55, v55\n" \
  "v_smfmac_f32_32x32x32_bf16 v[0:15], v[114:117], v[32:39], v144\n" \
  "ds_read_b64 v[112:113], v137 offset:23040\n" \
  "v_exp_f32 v56, v56\n" \
  "v_exp_f32 v57, v57\n" \
  "v_exp_f32 v58, v58\n" \
  "v_exp_f32 v59, v59\n" \
  "v_smfmac_f32_32x32x32_bf16 v[0:15], v[118:121], v[40:47], v144\n" \
  "ds_read_b128 v[128:131], v138 offset:20160\n" \
  "v_exp_f32 v60, v60\n" \
  "v_exp_f32 v61, v61\n" \
  "v_exp_f32 v62, v62\n" \
  "v_exp_f32 v63, v63\n" \
  "v_mfma_f32_32x32x8_bf16 v[80:95], v[126:127], v[106:107], 0\n" \
  "v_exp_f32 v64, v64\n" \
  "v_exp_f32 v65, v65\n" \
  "v_exp_f32 v66, v66\n" \
  "v_exp_f32 v67, v67\n" \
  "v_mfma_f32_32x32x16_bf16 v[80:95], v[122:125], v[100:103], v[80:95]\n" \
  "ds_read_b128 v[132:135], v138 offset:20832\n" \
  "v_exp_f32 v68, v68\n" \
  "v_exp_f32 v69, v69\n" \
  "v_exp_f32 v70, v70\n" \
  "v_exp_f32 v71, v71\n" \
  "v_smfmac_f32_32x32x32_bf16 v[16:31], v[114:117], v[48:55], v144\n" \
  "buffer_load_dwordx4 v[184:187], v141, %[srd], %[t0] offen offset:1024 sc1\n" \
  "v_exp_f32 v72, v72\n" \
  "v_exp_f32 v73, v73\n" \
  "v_exp_f32 v74, v74\n" \
  "v_exp_f32 v75, v75\n" \
  "v_smfmac_f32_32x32x32_bf16 v[16:31], v[118:121], v[56:63], v144\n" \
  "s_waitcnt lgkmcnt(0)\n" \
  "v_exp_f32 v76, v76\n" \
  "v_exp_f32 v77, v77\n" \
  "v_exp_f32 v78, v78\n" \
  "v_exp_f32 v79, v79\n" \
  "v_mfma_f32_32x32x8_bf16 v[32:47], v[112:113], v[104:105], 0\n" \
  "buffer_load_dwordx4 v[188:191], v141, %[srd], %[t0] offen offset:2048 sc1\n" \
  "v_exp_f32 v80, v80\n" \
  "v_exp_f32 v81, v81\n" \
  "v_exp_f32 v82, v82\n" \
  "v_exp_f32 v83, v83\n" \
  "v_mfma_f32_32x32x16_bf16 v[32:47], v[108:111], v[96:99], v[32:47]\n" \
  "ds_read_b128 v[122:125], v136 offset:25920\n" \
  "v_exp_f32 v84, v84\n" \
  "v_exp_f32 v85, v85\n" \
  "v_exp_f32 v86, v86\n" \
  "v_exp_f32 v87, v87\n" \
  "v_smfmac_f32_32x32x32_bf16 v[0:15], v[128:131], v[64:71], v144\n" \
  "ds_read_b64 v[126:127], v137 offset:25920\n" \
  "v_exp_f32 v88, v88\n" \
  "v_exp_f32 v89, v89\n" \
  "v_exp_f32 v90, v90\n" \
  "v_exp_f32 v91, v91\n" \
  "v_smfmac_f32_32x32x32_bf16 v[0:15], v[132:135], v[72:79], v144\n" \
  "ds_read_b128 v[114:117], v138 offset:23040\n" \
  "v_exp_f32 v92, v92\n" \
  "v_exp_f32 v93, v93\n" \
  "v_exp_f32 v94, v94\n" \
  "v_exp_f32 v95, v95\n" \
  "v_mfma_f32_32x32x8_bf16 v[48:63], v[112:113], v[106:107], 0\n" \
  "v_exp_f32 v32, v32\n" \
  "v_exp_f32 v33, v33\n" \
  "v_exp_f32 v34, v34\n" \
  "v_exp_f32 v35, v35\n" \
  "v_mfma_f32_32x32x16_bf16 v[48:63], v[108:111], v[100:103], v[48:63]\n" \
  "ds_read_b128 v[118:121], v138 offset:23712\n" \
  "v_exp_f32 v36, v36\n" \
  "v_exp_f32 v37, v37\n" \
  "v_exp_f32 v38, v38\n" \
  "v_exp_f32 v39, v39\n" \
  "v_smfmac_f32_32x32x32_bf16 v[16:31], v[128:131], v[80:87], v144\n" \
  "s_add_u32 %[t0], %[w1s], 442368\n" \
  "buffer_load_dwordx4 v[192:195], v141, %[srd], %[t0] offen offset:0 sc1\n" \
  "v_exp_f32 v40, v40\n" \
  "v_exp_f32 v41, v41\n" \
  "v_exp_f32 v42, v42\n" \
  "v_exp_f32 v43, v43\n" \
  "v_smfmac_f32_32x32x32_bf16 v[16:31], v[132:135], v[88:95], v144\n" \
  "s_waitcnt lgkmcnt(0)\n" \
  "v_exp_f32 v44, v44\n" \
  "v_exp_f32 v45, v45\n" \
  "v_exp_f32 v46, v46\n" \
  "v_exp_f32 v47, v47\n" \
  "v_mfma_f32_32x32x8_bf16 v[64:79], v[126:127], v[104:105], 0\n" \
  "buffer_load_dwordx4 v[196:199], v141, %[srd], %[t0] offen offset:1024 sc1\n" \
  "v_exp_f32 v48, v48\n" \
  "v_exp_f32 v49, v49\n" \
  "v_exp_f32 v50, v50\n" \
  "v_exp_f32 v51, v51\n" \
  "v_mfma_f32_32x32x16_bf16 v[64:79], v[122:125], v[96:99], v[64:79]\n" \
  "ds_read_b128 v[108:111], v136 offset:28800\n" \
  "v_exp_f32 v52, v52\n" \
  "v_exp_f32 v53, v53\n" \
  "v_exp_f32 v54, v54\n" \
  "v_exp_f32 v55, v55\n" \
  "v_smfmac_f32_32x32x32_bf16 v[0:15], v[114:117], v[32:39], v144\n" \
  "ds_read_b64 v[112:113], v137 offset:28800\n" \
  "v_exp_f32 v56, v56\n" \
  "v_exp_f32 v57, v57\n" \
  "v_exp_f32 v58, v58\n" \
  "v_exp_f32 v59, v59\n" \
  "v_smfmac_f32_32x32x32_bf16 v[0:15], v[118:121], v[40:47], v144\n" \
  "ds_read_b128 v[128:131], v138 offset:25920\n" \
  "v_exp_f32 v60, v60\n" \
  "v_exp_f32 v61, v61\n" \
  "v_exp_f32 v62, v62\n" \
  "v_exp_f32 v63, v63\n" \
  "v_mfma_f32_32x32x8_bf16 v[80:95], v[126:127], v[106:107], 0\n" \
  "v_exp_f32 v64, v64\n" \
  "v_exp_f32 v65, v65\n" \
  "v_exp_f32 v66, v66\n" \
  "v_exp_f32 v67, v67\n" \
  "v_mfma_f32_32x32x16_bf16 v[80:95], v[122:125], v[100:103], v[80:95]\n" \
  "ds_read_b128 v[132:135], v138 offset:26592\n" \
  "v_exp_f32 v68, v68\n" \
  "v_exp_f32 v69, v69\n" \
  "v_exp_f32 v70, v70\n" \
  "v_exp_f32 v71, v71\n" \
  "v_smfmac_f32_32x32x32_bf16 v[16:31], v[114:117], v[48:55], v144\n" \
  "buffer_load_dwordx4 v[200:203], v141, %[srd], %[t0] offen offset:2048 sc1\n" \
  "v_exp_f32 v72, v72\n" \
  "v_exp_f32 v73, v73\n" \
  "v_exp_f32 v74, v74\n" \
  "v_exp_f32 v75, v75\n" \
  "v_smfmac_f32_32x32x32_bf16 v[16:31], v[118:121], v[56:63], v144\n" \
  "s_waitcnt lgkmcnt(0)\n" \
  "v_exp_f32 v76, v76\n" \
  "v_exp_f32 v77, v77\n" \
  "v_exp_f32 v78, v78\n" \
  "v_exp_f32 v79, v79\n" \
  "v_mfma_f32_32x32x8_bf16 v[32:47], v[112:113], v[104:105], 0\n" \
  "buffer_load_dwordx4 v[204:207], v141, %[srd], %[t0] offen offset:3072 sc1\n" \
  "v_exp_f32 v80, v80\n" \
  "v_exp_f32 v81, v81\n" \
  "v_exp_f32 v82, v82\n" \
  "v_exp_f32 v83, v83\n" \
  "v_mfma_f32_32x32x16_bf16 v[32:47], v[108:111], v[96:99], v[32:47]\n" \
  "ds_read_b128 v[122:125], v136 offset:31680\n" \
  "v_exp_f32 v84, v84\n" \
  "v_exp_f32 v85, v85\n" \
  "v_exp_f32 v86, v86\n" \
  "v_exp_f32 v87, v87\n" \
  "v_smfmac_f32_32x32x32_bf16 v[0:15], v[128:131], v[64:71], v144\n" \
  "ds_read_b64 v[126:127], v137 offset:31680\n" \
  "v_exp_f32 v88, v88\n" \
  "v_exp_f32 v89, v89\n" \
  "v_exp_f32 v90, v90\n" \
  "v_exp_f32 v91, v91\n" \
  "v_smfmac_f32_32x32x32_bf16 v[0:15], v[132:135], v[72:79], v144\n" \
  "ds_read_b128 v[114:117], v138 offset:28800\n" \
  "v_exp_f32 v92, v92\n" \
  "v_exp_f32 v93, v93\n" \
  "v_exp_f32 v94, v94\n" \
  "v_exp_f32 v95, v95\n" \
  "v_mfma_f32_32x32x8_bf16 v[48:63], v[112:113], v[106:107], 0\n" \
  "v_exp_f32 v32, v32\n" \
  "v_exp_f32 v33, v33\n" \
  "v_exp_f32 v34, v34\n" \
  "v_exp_f32 v35, v35\n" \
  "v_mfma_f32_32x32x16_bf16 v[48:63], v[108:111], v[100:103], v[48:63]\n" \
  "ds_read_b128 v[118:121], v138 offset:29472\n" \
  "v_exp_f32 v36, v36\n" \
  "v_exp_f32 v37, v37\n" \
  "v_exp_f32 v38, v38\n" \
  "v_exp_f32 v39, v39\n" \
  "v_smfmac_f32_32x32x32_bf16 v[16:31], v[128:131], v[80:87], v144\n" \
  "s_add_u32 %[t0], %[w1s], 446464\n" \
  "buffer_load_dwordx4 v[208:211], v141, %[srd], %[t0] offen offset:0 sc1\n" \
  "v_exp_f32 v40, v40\n" \
  "v_exp_f32 v41, v41\n" \
  "v_exp_f32 v42, v42\n" \
  "v_exp_f32 v43, v43\n" \
  "v_smfmac_f32_32x32x32_bf16 v[16:31], v[132:135], v[88:95], v144\n" \
  "s_waitcnt lgkmcnt(0)\n" \
  "s_barrier\n" \
  "v_exp_f32 v44, v44\n" \
  "v_exp_f32 v45, v45\n" \
  "v_exp_f32 v46, v46\n" \
  "v_exp_f32 v47, v47\n" \
  "v_mfma_f32_32x32x8_bf16 v[64:79], v[126:127], v[104:105], 0\n" \
  "buffer_load_dwordx4 v[212:215], v141, %[srd], %[t0] offen offset:1024 sc1\n" \
  "v_exp_f32 v48, v48\n" \
  "v_exp_f32 v49, v49\n" \
  "v_exp_f32 v50, v50\n" \
  "v_exp_f32 v51, v51\n" \
  "v_mfma_f32_32x32x16_bf16 v[64:79], v[122:125], v[96:99], v[64:79]\n" \
  ".Lk_pad1:\n" \
  "s_cbranch_execz .Lk_ret1\n" \
  "ds_read_b128 v[108:111], v136 offset:34560\n" \
  "v_exp_f32 v52, v52\n" \
  "v_exp_f32 v53, v53\n" \
  "v_exp_f32 v54, v54\n" \
  "v_exp_f32 v55, v55\n" \
  "v_smfmac_f32_32x32x32_bf16 v[0:15], v[114:117], v[32:39], v144\n" \
  "ds_read_b64 v[112:113], v137 offset:34560\n" \
  "v_exp_f32 v56, v56\n" \
  "v_exp_f32 v57, v57\n" \
  "v_exp_f32 v58, v58\n" \
  "v_exp_f32 v59, v59\n" \
  "v_smfmac_f32_32x32x32_bf16 v[0:15], v[118:121], v[40:47], v144\n" \
  "ds_read_b128 v[128:131], v138 offset:31680\n" \
  "v_exp_f32 v60, v60\n" \
  "v_exp_f32 v61, v61\n" \
  "v_exp_f32 v62, v62\n" \
  "v_exp_f32 v63, v63\n" \
  "v_mfma_f32_32x32x8_bf16 v[80:95], v[126:127], v[106:107], 0\n" \
  "v_exp_f32 v64, v64\n" \
  "v_exp_f32 v65, v65\n" \
  "v_exp_f32 v66, v66\n" \
  "v_exp_f32 v67, v67\n" \
  "v_mfma_f32_32x32x16_bf16 v[80:95], v[122:125], v[100:103], v[80:95]\n" \
  "ds_read_b128 v[132:135], v138 offset:32352\n" \
  "v_exp_f32 v68, v68\n" \
  "v_exp_f32 v69, v69\n" \
  "v_exp_f32 v70, v70\n" \
  "v_exp_f32 v71, v71\n" \
  "v_smfmac_f32_32x32x32_bf16 v[16:31], v[114:117], v[48:55], v144\n" \
  "buffer_load_dwordx4 v[216:219], v141, %[srd], %[t0] offen offset:2048 sc1\n" \
  "v_exp_f32 v72, v72\n" \
  "v_exp_f32 v73, v73\n" \
  "v_exp_f32 v74, v74\n" \
  "v_exp_f32 v75, v75\n" \
  "v_smfmac_f32_32x32x32_bf16 v[16:31], v[118:121], v[56:63], v144\n" \
  "s_waitcnt lgkmcnt(0)\n" \
  "v_exp_f32 v76, v76\n" \
  "v_exp_f32 v77, v77\n" \
  "v_exp_f32 v78, v78\n" \
  "v_exp_f32 v79, v79\n" \
  "v_mfma_f32_32x32x8_bf16 v[32:47], v[112:113], v[104:105], 0\n" \
  "s_add_u32 %[t0], %[w1s], 884736\n" \
  "buffer_load_dwordx4 v[220:223], v141, %[srd], %[t0] offen offset:0 sc1\n" \
  "v_exp_f32 v80, v80\n" \
  "v_exp_f32 v81, v81\n" \
  "v_exp_f32 v82, v82\n" \
  "v_exp_f32 v83, v83\n" \
  "v_mfma_f32_32x32x16_bf16 v[32:47], v[108:111], v[96:99], v[32:47]\n" \
  "ds_read_b128 v[122:125], v136 offset:37440\n" \
  "v_exp_f32 v84, v84\n" \
  "v_exp_f32 v85, v85\n" \
  "v_exp_f32 v86, v86\n" \
  "v_exp_f32 v87, v87\n" \
  "v_smfmac_f32_32x32x32_bf16 v[0:15], v[128:131], v[64:71], v144\n" \
  "ds_read_b64 v[126:127], v137 offset:37440\n" \
  "v_exp_f32 v88, v88\n" \
  "v_exp_f32 v89, v89\n" \
  "v_exp_f32 v90, v90\n" \
  "v_exp_f32 v91, v91\n" \
  "v_smfmac_f32_32x32x32_bf16 v[0:15], v[132:135], v[72:79], v144\n" \
  "ds_read_b128 v[114:117], v138 offset:34560\n" \
  "v_exp_f32 v92, v92\n" \
  "v_exp_f32 v93, v93\n" \
  "v_exp_f32 v94, v94\n" \
  "v_exp_f32 v95, v95\n" \
  "v_mfma_f32_32x32x8_bf16 v[48:63], v[112:113], v[106:107], 0\n" \
  "v_exp_f32 v32, v32\n" \
  "v_exp_f32 v33, v33\n" \
  "v_exp_f32 v34, v34\n" \
  "v_exp_f32 v35, v35\n" \
  "v_mfma_f32_32x32x16_bf16 v[48:63], v[108:111], v[100:103], v[48:63]\n" \
  "ds_read_b128 v[118:121], v138 offset:35232\n" \
  "v_exp_f32 v36, v36\n" \
  "v_exp_f32 v37, v37\n" \
  "v_exp_f32 v38, v38\n" \
  "v_exp_f32 v39, v39\n" \
  "v_smfmac_f32_32x32x32_bf16 v[16:31], v[128:131], v[80:87], v144\n" \
  "buffer_load_dwordx4 v[224:227], v141, %[srd], %[t0] offen offset:1024 sc1\n" \
  "v_exp_f32 v40, v40\n" \
  "v_exp_f32 v41, v41\n" \
  "v_exp_f32 v42, v42\n" \
  "v_exp_f32 v43, v43\n" \
  "v_smfmac_f32_32x32x32_bf16 v[16:31], v[132:135], v[88:95], v144\n" \
  "s_waitcnt lgkmcnt(0)\n" \
  "v_exp_f32 v44, v44\n" \
  "v_exp_f32 v45, v45\n" \
  "v_exp_f32 v46, v46\n" \
  "v_exp_f32 v47, v47\n" \
  "v_mfma_f32_32x32x8_bf16 v[64:79], v[126:127], v[104:105], 0\n" \
  "buffer_load_dwordx4 v[228:231], v141, %[srd], %[t0] offen offset:2048 sc1\n" \
  "v_exp_f32 v48, v48\n" \
  "v_exp_f32 v49, v49\n" \
  "v_exp_f32 v50, v50\n" \
  "v_exp_f32 v51, v51\n" \
  "v_mfma_f32_32x32x16_bf16 v[64:79], v[122:125], v[96:99], v[64:79]\n" \
  "ds_read_b128 v[108:111], v136 offset:40320\n" \
  "v_exp_f32 v52, v52\n" \
  "v_exp_f32 v53, v53\n" \
  "v_exp_f32 v54, v54\n" \
  "v_exp_f32 v55, v55\n" \
  "v_smfmac_f32_32x32x32_bf16 v[0:15], v[114:117], v[32:39], v144\n" \
  "ds_read_b64 v[112:113], v137 offset:40320\n" \
  "v_exp_f32 v56, v56\n" \
  "v_exp_f32 v57, v57\n" \
  "v_exp_f32 v58, v58\n" \
  "v_exp_f32 v59, v59\n" \
  "v_smfmac_f32_32x32x32_bf16 v[0:15], v[118:121], v[40:47], v144\n" \
  "ds_read_b128 v[128:131], v138 offset:37440\n" \
  "v_exp_f32 v60, v60\n" \
  "v_exp_f32 v61, v61\n" \
  "v_exp_f32 v62, v62\n" \
  "v_exp_f32 v63, v63\n" \
  "v_mfma_f32_32x32x8_bf16 v[80:95], v[126:127], v[106:107], 0\n" \
  "v_exp_f32 v64, v64\n" \
  "v_exp_f32 v65, v65\n" \
  "v_exp_f32 v66, v66\n" \
  "v_exp_f32 v67, v67\n" \
  "v_mfma_f32_32x32x16_bf16 v[80:95], v[122:125], v[100:103], v[80:95]\n" \
  "ds_read_b128 v[132:135], v138 offset:38112\n" \
  "v_exp_f32 v68, v68\n" \
  "v_exp_f32 v69, v69\n" \
  "v_exp_f32 v70, v70\n" \
  "v_exp_f32 v71, v71\n" \
  "v_smfmac_f32_32x32x32_bf16 v[16:31], v[114:117], v[48:55], v144\n" \
  "buffer_load_dwordx4 v[232:235], v141, %[srd], %[t0] offen offset:3072 sc1\n" \
  "v_exp_f32 v72, v72\n" \
  "v_exp_f32 v73, v73\n" \
  "v_exp_f32 v74, v74\n" \
  "v_exp_f32 v75, v75\n" \
  "v_smfmac_f32_32x32x32_bf16 v[16:31], v[118:121], v[56:63], v144\n" \
  "s_waitcnt lgkmcnt(0)\n" \
  "v_exp_f32 v76, v76\n" \
  "v_exp_f32 v77, v77\n" \
  "v_exp_f32 v78, v78\n" \
  "v_exp_f32 v79, v79\n" \
  "v_mfma_f32_32x32x8_bf16 v[32:47], v[112:113], v[104:105], 0\n" \
  "s_add_u32 %[t0], %[w1s], 888832\n" \
  "buffer_load_dwordx4 v[236:239], v141, %[srd], %[t0] offen offset:0 sc1\n" \
  "v_exp_f32 v80, v80\n" \
  "v_exp_f32 v81, v81\n" \
  "v_exp_f32 v82, v82\n" \
  "v_exp_f32 v83, v83\n" \
  "v_mfma_f32_32x32x16_bf16 v[32:47], v[108:111], v[96:99], v[32:47]\n" \
  "ds_read_b128 v[122:125], v136 offset:43200\n" \
  "v_exp_f32 v84, v84\n" \
  "v_exp_f32 v85, v85\n" \
  "v_exp_f32 v86, v86\n" \
  "v_exp_f32 v87, v87\n" \
  "v_smfmac_f32_32x32x32_bf16 v[0:15], v[128:131], v[64:71], v144\n" \
  "ds_read_b64 v[126:127], v137 offset:43200\n" \
  "v_exp_f32 v88, v88\n" \
  "v_exp_f32 v89, v89\n" \
  "v_exp_f32 v90, v90\n" \
  "v_exp_f32 v91, v91\n" \
  "v_smfmac_f32_32x32x32_bf16 v[0:15], v[132:135], v[72:79], v144\n" \
  "ds_read_b128 v[114:117], v138 offset:40320\n" \
  "v_exp_f32 v92, v92\n" \
  "v_exp_f32 v93, v93\n" \
  "v_exp_f32 v94, v94\n" \
  "v_exp_f32 v95, v95\n" \
  "v_mfma_f32_32x32x8_bf16 v[48:63], v[112:113], v[106:107], 0\n" \
  "v_exp_f32 v32, v32\n" \
  "v_exp_f32 v33, v33\n" \
  "v_exp_f32 v34, v34\n" \
  "v_exp_f32 v35, v35\n" \
  "v_mfma_f32_32x32x16_bf16 v[48:63], v[108:111], v[100:103], v[48:63]\n" \
  "ds_read_b128 v[118:121], v138 offset:40992\n" \
  "v_exp_f32 v36, v36\n" \
  "v_exp_f32 v37, v37\n" \
  "v_exp_f32 v38, v38\n" \
  "v_exp_f32 v39, v39\n" \
  "v_smfmac_f32_32x32x32_bf16 v[16:31], v[128:131], v[80:87], v144\n" \
  "buffer_load_dwordx4 v[240:243], v141, %[srd], %[t0] offen offset:1024 sc1\n" \
  "v_exp_f32 v40, v40\n" \
  "v_exp_f32 v41, v41\n" \
  "v_exp_f32 v42, v42\n" \
  "v_exp_f32 v43, v43\n" \
  "v_smfmac_f32_32x32x32_bf16 v[16:31], v[132:135], v[88:95], v144\n" \
  "s_waitcnt lgkmcnt(0)\n" \
  "v_exp_f32 v44, v44\n" \
  "v_exp_f32 v45, v45\n" \
  "v_exp_f32 v46, v46\n" \
  "v_exp_f32 v47, v47\n" \
  "v_mfma_f32_32x32x8_bf16 v[64:79], v[126:127], v[104:105], 0\n" \
  "buffer_load_dwordx4 v[244:247], v141, %[srd], %[t0] offen offset:2048 sc1\n" \
  "v_exp_f32 v48, v48\n" \
  "v_exp_f32 v49, v49\n" \
  "v_exp_f32 v50, v50\n" \
  "v_exp_f32 v51, v51\n" \
  "v_mfma_f32_32x32x16_bf16 v[64:79], v[122:125], v[96:99], v[64:79]\n" \
  "v_exp_f32 v52, v52\n" \
  "v_exp_f32 v53, v53\n" \
  "v_exp_f32 v54, v54\n" \
  "v_exp_f32 v55, v55\n" \
  "v_smfmac_f32_32x32x32_bf16 v[0:15], v[114:117], v[32:39], v144\n" \
  "v_exp_f32 v56, v56\n" \
  "v_exp_f32 v57, v57\n" \
  "v_exp_f32 v58, v58\n" \
  "v_exp_f32 v59, v59\n" \
  "v_smfmac_f32_32x32x32_bf16 v[0:15], v[118:121], v[40:47], v144\n" \
  "ds_read_b128 v[128:131], v138 offset:43200\n" \
  "v_exp_f32 v60, v60\n" \
  "v_exp_f32 v61, v61\n" \
  "v_exp_f32 v62, v62\n" \
  "v_exp_f32 v63, v63\n" \
  "v_mfma_f32_32x32x8_bf16 v[80:95], v[126:127], v[106:107], 0\n" \
  "v_exp_f32 v64, v64\n" \
  "v_exp_f32 v65, v65\n" \
  "v_exp_f32 v66, v66\n" \
  "v_exp_f32 v67, v67\n" \
  "v_mfma_f32_32x32x16_bf16 v[80:95], v[122:125], v[100:103], v[80:95]\n" \
  "ds_read_b128 v[132:135], v138 offset:43872\n" \
  "v_exp_f32 v68, v68\n" \
  "v_exp_f32 v69, v69\n" \
  "v_exp_f32 v70, v70\n" \
  "v_exp_f32 v71, v71\n" \
  "v_smfmac_f32_32x32x32_bf16 v[16:31], v[114:117], v[48:55], v144\n" \
  "v_exp_f32 v72, v72\n" \
  "v_exp_f32 v73, v73\n" \
  "v_exp_f32 v74, v74\n" \
  "v_exp_f32 v75, v75\n" \
  "v_smfmac_f32_32x32x32_bf16 v[16:31], v[118:121], v[56:63], v144\n" \
  "s_waitcnt vmcnt(0) lgkmcnt(0)\n" \
  "s_barrier\n" \
  "v_exp_f32 v76, v76\n" \
  "v_exp_f32 v77, v77\n" \
  "v_exp_f32 v78, v78\n" \
  "v_exp_f32 v79, v79\n" \
  "v_smfmac_f32_32x32x32_bf16 v[0:15], v[128:131], v[64:71], v144\n" \
  "v_exp_f32 v80, v80\n" \
  "v_exp_f32 v81, v81\n" \
  "v_exp_f32 v82, v82\n" \
  "v_exp_f32 v83, v83\n" \
  "v_smfmac_f32_32x32x32_bf16 v[0:15], v[132:135], v[72:79], v144\n" \
  "v_exp_f32 v84, v84\n" \
  "v_exp_f32 v85, v85\n" \
  "v_exp_f32 v86, v86\n" \
  "v_exp_f32 v87, v87\n" \
  "v_exp_f32 v88, v88\n" \
  "v_exp_f32 v89, v89\n" \
  "v_smfmac_f32_32x32x32_bf16 v[16:31], v[128:131], v[80:87], v144\n" \
  "v_exp_f32 v90, v90\n" \
  "v_exp_f32 v91, v91\n" \
  "v_exp_f32 v92, v92\n" \
  "v_exp_f32 v93, v93\n" \
  "v_exp_f32 v94, v94\n" \
  "v_rcp_f32 v32, v8\n" \
  "v_exp_f32 v95, v95\n" \
  "v_mov_b32 v33, v32\n" \
  "s_nop 0\n" \
  "v_smfmac_f32_32x32x32_bf16 v[16:31], v[132:135], v[88:95], v144\n" \
  "v_permlane32_swap_b32 v32, v33\n" \
  "v_pk_mul_f32 v[34:35], v[0:1], v[32:33] op_sel:[0,1] op_sel_hi:[1,1]\n" \
  "v_pk_mul_f32 v[36:37], v[2:3], v[32:33] op_sel:[0,1] op_sel_hi:[1,1]\n" \
  "v_pk_mul_f32 v[38:39], v[4:5], v[32:33] op_sel:[0,1] op_sel_hi:[1,1]\n" \
  "v_pk_mul_f32 v[40:41], v[6:7], v[32:33] op_sel:[0,1] op_sel_hi:[1,1]\n" \
  "v_pk_mul_f32 v[42:43], v[8:9], v[32:33] op_sel:[0,1] op_sel_hi:[1,1]\n" \
  "v_pk_mul_f32 v[44:45], v[10:11], v[32:33] op_sel:[0,1] op_sel_hi:[1,1]\n" \
  "v_cvt_pk_bf16_f32 v64, v34, v35\n" \
  "v_cvt_pk_bf16_f32 v65, v36, v37\n" \
  "v_cvt_pk_bf16_f32 v66, v38, v39\n" \
  "v_cvt_pk_bf16_f32 v67, v40, v41\n" \
  "v_cvt_pk_bf16_f32 v68, v42, v43\n" \
  "v_rcp_f32 v48, v24\n" \
  "v_cvt_pk_bf16_f32 v69, v44, v45\n" \
  "v_mov_b32 v49, v48\n" \
  "buffer_store_dwordx4 v[64:67], v139, %[srd], %[os0] offen sc1\n" \
  "s_mov_b32 exec_hi, 0\n" \
  "buffer_store_dwordx2 v[68:69], v140, %[srd], %[os0] offen sc1\n" \
  "s_mov_b32 exec_hi, -1\n" \
  "v_permlane32_swap_b32 v48, v49\n" \
  "v_pk_mul_f32 v[50:51], v[16:17], v[48:49] op_sel:[0,1] op_sel_hi:[1,1]\n" \
  "v_pk_mul_f32 v[52:53], v[18:19], v[48:49] op_sel:[0,1] op_sel_hi:[1,1]\n" \
  "v_pk_mul_f32 v[54:55], v[20:21], v[48:49] op_sel:[0,1] op_sel_hi:[1,1]\n" \
  "v_pk_mul_f32 v[56:57], v[22:23], v[48:49] op_sel:[0,1] op_sel_hi:[1,1]\n" \
  "v_pk_mul_f32 v[58:59], v[24:25], v[48:49] op_sel:[0,1] op_sel_hi:[1,1]\n" \
  "v_pk_mul_f32 v[60:61], v[26:27], v[48:49] op_sel:[0,1] op_sel_hi:[1,1]\n" \
  "v_cvt_pk_bf16_f32 v72, v50, v51\n" \
  "v_cvt_pk_bf16_f32 v73, v52, v53\n" \
  "v_cvt_pk_bf16_f32 v74, v54, v55\n" \
  "v_cvt_pk_bf16_f32 v75, v56, v57\n" \
  "v_cvt_pk_bf16_f32 v76, v58, v59\n" \
  "v_cvt_pk_bf16_f32 v77, v60, v61\n" \
  "buffer_store_dwordx4 v[72:75], v139, %[srd], %[os1] offen sc1\n" \
  "s_mov_b32 exec_hi, 0\n" \
  "buffer_store_dwordx2 v[76:77], v140, %[srd], %[os1] offen sc1\n" \
  "s_mov_b32 exec_hi, -1\n" \
  "v_max3_f32 v146, |v0|, |v1|, |v2|\n" \
  "v_max3_f32 v147, |v16|, |v17|, |v18|\n" \
  "v_max3_f32 v146, v146, |v3|, |v4|\n" \
  "v_max3_f32 v147, v147, |v19|, |v20|\n" \
  "v_max3_f32 v146, v146, |v5|, |v6|\n" \
  "v_max3_f32 v147, v147, |v21|, |v22|\n" \
  "v_max3_f32 v146, v146, |v7|, |v8|\n" \
  "v_max3_f32 v147, v147, |v23|, |v24|\n" \
  "v_max3_f32 v146, v146, |v9|, |v10|\n" \
  "v_max3_f32 v147, v147, |v25|, |v26|\n" \
  "v_max3_f32 v146, v146, |v11|, |v27|\n" \
  "v_add_f32 v148, v8, v24\n" \
  "v_max_f32 v146, v146, v147\n" \
  "v_add_f32 v146, v146, v148\n" \
  "v_cmp_ngt_f32 vcc, 0x7149f2ca, v146\n" \
  "v_min_f32 v148, v8, v24\n" \
  "s_nop 0\n" \
  "s_or_b32 %[t0], vcc_lo, vcc_hi\n" \
  "v_cmp_gt_f32 vcc, 0x21800000, v148\n" \
  "s_nop 1\n" \
  "s_or_b32 %[t0], %[t0], vcc_hi\n"
#define P2_CLOB "v0", "v1", "v2", "v3", "v4", "v5", "v6", "v7", "v8", "v9", "v10", "v11", "v12", "v13", "v14", "v15", "v16", "v17", "v18", "v19", "v20", "v21", "v22", "v23", "v24", "v25", "v26", "v27", "v28", "v29", "v30", "v31", "v32", "v33", "v34", "v35", "v36", "v37", "v38", "v39", "v40", "v41", "v42", "v43", "v44", "v45", "v46", "v47", "v48", "v49", "v50", "v51", "v52", "v53", "v54", "v55", "v56", "v57", "v58", "v59", "v60", "v61", "v62", "v63", "v64", "v65", "v66", "v67", "v68", "v69", "v70", "v71", "v72", "v73", "v74", "v75", "v76", "v77", "v78", "v79", "v80", "v81", "v82", "v83", "v84", "v85", "v86", "v87", "v88", "v89", "v90", "v91", "v92", "v93", "v94", "v95", "v96", "v97", "v98", "v99", "v100", "v101", "v102", "v103", "v104", "v105", "v106", "v107", "v108", "v109", "v110", "v111", "v112", "v113", "v114", "v115", "v116", "v117", "v118", "v119", "v120", "v121", "v122", "v123", "v124", "v125", "v126", "v127", "v128", "v129", "v130", "v131", "v132", "v133", "v134", "v135", "v144", "v145", "v146", "v147", "v148", "v149", "v150", "v151"
#define P3_ASM \
  "v_add_u32 v128, 61440, v116\n" \
  "s_mov_b32 %[so], %[ab]\n" \
  "s_mov_b64 exec, 65535\n" \
  "s_waitcnt vmcnt(0)\n" \
  "buffer_atomic_add v126, v125, %[srd], 0 offen sc1\n" \
  "s_mov_b64 exec, -1\n" \
  "v_mov_b32 v108, 0\n" \
  "s_mov_b32 %[cnt], 0x40000\n" \
  "s_mov_b64 exec, 1\n" \
  "global_atomic_add v109, v119, v108, %[fl] sc0 sc1\n" \
  "s_mov_b64 exec, -1\n" \
  "v_mov_b64 v[142:143], 0\n" \
  "ds_read_b64 v[20:21], v123 offset:0\n" \
  "ds_read_b64 v[22:23], v123 offset:32\n" \
  "ds_read_b64 v[24:25], v123 offset:64\n" \
  "ds_read_b64 v[26:27], v123 offset:96\n" \
  "ds_read_b64 v[28:29], v123 offset:128\n" \
  "v_mov_b32 v144, 0x3f803f80\n" \
  "v_mov_b32 v145, 0x3f803f80\n" \
  "v_mov_b32 v146, 0x3f803f80\n" \
  "v_mov_b32 v147, 0x3f803f80\n" \
  "s_waitcnt lgkmcnt(0)\n" \
  "v_lshlrev_b32 v0, 16, v20\n" \
  "v_and_b32 v1, 0xffff0000, v20\n" \
  "v_lshlrev_b32 v2, 16, v21\n" \
  "v_and_b32 v3, 0xffff0000, v21\n" \
  "v_lshlrev_b32 v4, 16, v22\n" \
  "v_and_b32 v5, 0xffff0000, v22\n" \
  "v_lshlrev_b32 v6, 16, v23\n" \
  "v_and_b32 v7, 0xffff0000, v23\n" \
  "v_lshlrev_b32 v8, 16, v24\n" \
  "v_and_b32 v9, 0xffff0000, v24\n" \
  "v_lshlrev_b32 v10, 16, v25\n" \
  "v_and_b32 v11, 0xffff0000, v25\n" \
  "v_lshlrev_b32 v12, 16, v26\n" \
  "v_and_b32 v13, 0xffff0000, v26\n" \
  "v_lshlrev_b32 v14, 16, v27\n" \
  "v_and_b32 v15, 0xffff0000, v27\n" \
  "v_lshlrev_b32 v16, 16, v28\n" \
  "v_and_b32 v17, 0xffff0000, v28\n" \
  "v_lshlrev_b32 v18, 16, v29\n" \
  "v_and_b32 v19, 0xffff0000, v29\n" \
  "ds_read_b128 v[76:79], v116 offset:0\n" \
  "ds_read_b128 v[80:83], v116 offset:1024\n" \
  "ds_read_b128 v[84:87], v116 offset:2048\n" \
  "ds_read_b128 v[88:91], v116 offset:3072\n" \
  "ds_read_b128 v[92:95], v116 offset:4096\n" \
  "ds_read_b128 v[96:99], v116 offset:5120\n" \
  "ds_read_b128 v[100:103], v116 offset:6144\n" \
  "ds_read_b128 v[104:107], v116 offset:7168\n" \
  "s_mov_b64 exec, 1\n" \
  "global_atomic_add v110, v119, v108, %[fl] sc0 sc1\n" \
  "9:\n" \
  "s_waitcnt vmcnt(1)\n" \
  "v_cmp_ne_u32 vcc, 32, v109\n" \
  "s_cbranch_vccz 8f\n" \
  "global_atomic_add v109, v119, v108, %[fl] sc0 sc1\n" \
  "s_waitcnt vmcnt(1)\n" \
  "v_cmp_ne_u32 vcc, 32, v110\n" \
  "s_cbranch_vccz 8f\n" \
  "global_atomic_add v110, v119, v108, %[fl] sc0 sc1\n" \
  "s_sub_u32 %[cnt], %[cnt], 1\n" \
  "s_cbranch_scc0 9b\n" \
  "8:\n" \
  "s_mov_b64 exec, -1\n" \
  "buffer_load_dwordx4 v[20:23], v117, %[srd], %[so] offen offset:0 sc1\n" \
  "buffer_load_dwordx4 v[24:27], v117, %[srd], %[so] offen offset:2048 sc1\n" \
  "s_add_u32 %[so], %[so], 4096\n" \
  "buffer_load_dwordx4 v[28:31], v117, %[srd], %[so] offen offset:0 sc1\n" \
  "buffer_load_dwordx4 v[32:35], v117, %[srd], %[so] offen offset:2048 sc1\n" \
  "s_add_u32 %[so], %[so], 4096\n" \
  "buffer_load_dwordx4 v[36:39], v117, %[srd], %[so] offen offset:0 sc1\n" \
  "buffer_load_dwordx4 v[40:43], v117, %[srd], %[so] offen offset:2048 sc1\n" \
  "s_add_u32 %[so], %[so], 4096\n" \
  "buffer_load_dwordx4 v[44:47], v117, %[srd], %[so] offen offset:0 sc1\n" \
  "buffer_load_dwordx4 v[48:51], v117, %[srd], %[so] offen offset:2048 sc1\n" \
  "s_add_u32 %[so], %[so], 4096\n" \
  "buffer_load_dwordx4 v[52:55], v117, %[srd], %[so] offen offset:0 sc1\n" \
  "buffer_load_dwordx4 v[56:59], v117, %[srd], %[so] offen offset:2048 sc1\n" \
  "s_add_u32 %[so], %[so], 4096\n" \
  "s_waitcnt vmcnt(9) lgkmcnt(6)\n" \
  "v_mfma_f32_16x16x32_bf16 v[0:3], v[76:79], v[20:23], v[0:3]\n" \
  "ds_read_b128 v[76:79], v116 offset:8192\n" \
  "buffer_load_dwordx4 v[60:63], v117, %[srd], %[so] offen offset:0 sc1\n" \
  "v_mfma_f32_16x16x32_bf16 v[4:7], v[80:83], v[20:23], v[4:7]\n" \
  "s_waitcnt lgkmcnt(5)\n" \
  "ds_read_b128 v[80:83], v116 offset:9216\n" \
  "v_mfma_f32_16x16x32_bf16 v[8:11], v[84:87], v[20:23], v[8:11]\n" \
  "ds_read_b128 v[84:87], v116 offset:10240\n" \
  "v_mfma_f32_16x16x32_bf16 v[12:15], v[88:91], v[20:23], v[12:15]\n" \
  "s_waitcnt lgkmcnt(5)\n" \
  "ds_read_b128 v[88:91], v116 offset:11264\n" \
  "v_mfma_f32_16x16x32_bf16 v[16:19], v[92:95], v[20:23], v[16:19]\n" \
  "s_waitcnt vmcnt(9)\n" \
  "ds_read_b128 v[92:95], v116 offset:12288\n" \
  "v_mfma_f32_16x16x32_bf16 v[0:3], v[96:99], v[24:27], v[0:3]\n" \
  "s_waitcnt lgkmcnt(5)\n" \
  "ds_read_b128 v[96:99], v116 offset:13312\n" \
  "buffer_load_dwordx4 v[64:67], v117, %[srd], %[so] offen offset:2048 sc1\n" \
  "v_mfma_f32_16x16x32_bf16 v[4:7], v[100:103], v[24:27], v[4:7]\n" \
  "ds_read_b128 v[100:103], v116 offset:14336\n" \
  "v_mfma_f32_16x16x32_bf16 v[8:11], v[104:107], v[24:27], v[8:11]\n" \
  "s_waitcnt lgkmcnt(5)\n" \
  "ds_read_b128 v[104:107], v116 offset:15360\n" \
  "s_add_u32 %[so], %[so], 4096\n" \
  "v_mfma_f32_16x16x32_bf16 v[12:15], v[76:79], v[24:27], v[12:15]\n" \
  "ds_read_b128 v[76:79], v116 offset:16384\n" \
  "v_mfma_f32_16x16x32_bf16 v[16:19], v[80:83], v[24:27], v[16:19]\n" \
  "s_waitcnt vmcnt(9) lgkmcnt(5)\n" \
  "ds_read_b128 v[80:83], v116 offset:17408\n" \
  "v_mfma_f32_16x16x32_bf16 v[0:3], v[84:87], v[28:31], v[0:3]\n" \
  "ds_read_b128 v[84:87], v116 offset:18432\n" \
  "buffer_load_dwordx4 v[68:71], v117, %[srd], %[so] offen offset:0 sc1\n" \
  "v_mfma_f32_16x16x32_bf16 v[4:7], v[88:91], v[28:31], v[4:7]\n" \
  "s_waitcnt lgkmcnt(5)\n" \
  "ds_read_b128 v[88:91], v116 offset:19456\n" \
  "v_mfma_f32_16x16x32_bf16 v[8:11], v[92:95], v[28:31], v[8:11]\n" \
  "ds_read_b128 v[92:95], v116 offset:20480\n" \
  "v_mfma_f32_16x16x32_bf16 v[12:15], v[96:99], v[28:31], v[12:15]\n" \
  "s_waitcnt lgkmcnt(5)\n" \
  "ds_read_b128 v[96:99], v116 offset:21504\n" \
  "v_mfma_f32_16x16x32_bf16 v[16:19], v[100:103], v[28:31], v[16:19]\n" \
  "s_waitcnt vmcnt(9)\n" \
  "ds_read_b128 v[100:103], v116 offset:22528\n" \
  "v_mfma_f32_16x16x32_bf16 v[0:3], v[104:107], v[32:35], v[0:3]\n" \
  "s_waitcnt lgkmcnt(5)\n" \
  "ds_read_b128 v[104:107], v116 offset:23552\n" \
  "buffer_load_dwordx4 v[72:75], v117, %[srd], %[so] offen offset:2048 sc1\n" \
  "v_mfma_f32_16x16x32_bf16 v[4:7], v[76:79], v[32:35], v[4:7]\n" \
  "ds_read_b128 v[76:79], v116 offset:24576\n" \
  "v_mfma_f32_16x16x32_bf16 v[8:11], v[80:83], v[32:35], v[8:11]\n" \
  "s_waitcnt lgkmcnt(5)\n" \
  "ds_read_b128 v[80:83], v116 offset:25600\n" \
  "s_add_u32 %[so], %[so], 4096\n" \
  "v_mfma_f32_16x16x32_bf16 v[12:15], v[84:87], v[32:35], v[12:15]\n" \
  "ds_read_b128 v[84:87], v116 offset:26624\n" \
  "v_mfma_f32_16x16x32_bf16 v[16:19], v[88:91], v[32:35], v[16:19]\n" \
  "s_waitcnt vmcnt(9) lgkmcnt(5)\n" \
  "ds_read_b128 v[88:91], v116 offset:27648\n" \
  "v_mfma_f32_16x16x32_bf16 v[0:3], v[92:95], v[36:39], v[0:3]\n" \
  "ds_read_b128 v[92:95], v116 offset:28672\n" \
  "buffer_load_dwordx4 v[20:23], v117, %[srd], %[so] offen offset:0 sc1\n" \
  "v_mfma_f32_16x16x32_bf16 v[4:7], v[96:99], v[36:39], v[4:7]\n" \
  "s_waitcnt lgkmcnt(5)\n" \
  "ds_read_b128 v[96:99], v116 offset:29696\n" \
  "v_mfma_f32_16x16x32_bf16 v[8:11], v[100:103], v[36:39], v[8:11]\n" \
  "ds_read_b128 v[100:103], v116 offset:30720\n" \
  "v_mfma_f32_16x16x32_bf16 v[12:15], v[104:107], v[36:39], v[12:15]\n" \
  "s_waitcnt lgkmcnt(5)\n" \
  "ds_read_b128 v[104:107], v116 offset:31744\n" \
  "v_mfma_f32_16x16x32_bf16 v[16:19], v[76:79], v[36:39], v[16:19]\n" \
  "s_waitcnt vmcnt(9)\n" \
  "ds_read_b128 v[76:79], v116 offset:32768\n" \
  "v_mfma_f32_16x16x32_bf16 v[0:3], v[80:83], v[40:43], v[0:3]\n" \
  "s_waitcnt lgkmcnt(5)\n" \
  "ds_read_b128 v[80:83], v116 offset:33792\n" \
  "buffer_load_dwordx4 v[24:27], v117, %[srd], %[so] offen offset:2048 sc1\n" \
  "v_mfma_f32_16x16x32_bf16 v[4:7], v[84:87], v[40:43], v[4:7]\n" \
  "ds_read_b128 v[84:87], v116 offset:34816\n" \
  "v_mfma_f32_16x16x32_bf16 v[8:11], v[88:91], v[40:43], v[8:11]\n" \
  "s_waitcnt lgkmcnt(5)\n" \
  "ds_read_b128 v[88:91], v116 offset:35840\n" \
  "s_add_u32 %[so], %[so], 4096\n" \
  "v_mfma_f32_16x16x32_bf16 v[12:15], v[92:95], v[40:43], v[12:15]\n" \
  "ds_read_b128 v[92:95], v116 offset:36864\n" \
  "v_mfma_f32_16x16x32_bf16 v[16:19], v[96:99], v[40:43], v[16:19]\n" \
  "s_waitcnt vmcnt(9) lgkmcnt(5)\n" \
  "ds_read_b128 v[96:99], v116 offset:37888\n" \
  "v_mfma_f32_16x16x32_bf16 v[0:3], v[100:103], v[44:47], v[0:3]\n" \
  "ds_read_b128 v[100:103], v116 offset:38912\n" \
  "buffer_load_dwordx4 v[28:31], v117, %[srd], %[so] offen offset:0 sc1\n" \
  "v_mfma_f32_16x16x32_bf16 v[4:7], v[104:107], v[44:47], v[4:7]\n" \
  "s_waitcnt lgkmcnt(5)\n" \
  "ds_read_b128 v[104:107], v116 offset:39936\n" \
  "v_mfma_f32_16x16x32_bf16 v[8:11], v[76:79], v[44:47], v[8:11]\n" \
  "ds_read_b128 v[76:79], v116 offset:40960\n" \
  "v_mfma_f32_16x16x32_bf16 v[12:15], v[80:83], v[44:47], v[12:15]\n" \
  "s_waitcnt lgkmcnt(5)\n" \
  "ds_read_b128 v[80:83], v116 offset:41984\n" \
  "v_mfma_f32_16x16x32_bf16 v[16:19], v[84:87], v[44:47], v[16:19]\n" \
  "s_waitcnt vmcnt(9)\n" \
  "ds_read_b128 v[84:87], v116 offset:43008\n" \
  "v_mfma_f32_16x16x32_bf16 v[0:3], v[88:91], v[48:51], v[0:3]\n" \
  "s_waitcnt lgkmcnt(5)\n" \
  "ds_read_b128 v[88:91], v116 offset:44032\n" \
  "buffer_load_dwordx4 v[32:35], v117, %[srd], %[so] offen offset:2048 sc1\n" \
  "v_mfma_f32_16x16x32_bf16 v[4:7], v[92:95], v[48:51], v[4:7]\n" \
  "ds_read_b128 v[92:95], v116 offset:45056\n" \
  "v_mfma_f32_16x16x32_bf16 v[8:11], v[96:99], v[48:51], v[8:11]\n" \
  "s_waitcnt lgkmcnt(5)\n" \
  "ds_read_b128 v[96:99], v116 offset:46080\n" \
  "s_add_u32 %[so], %[so], 4096\n" \
  "v_mfma_f32_16x16x32_bf16 v[12:15], v[100:103], v[48:51], v[12:15]\n" \
  "ds_read_b128 v[100:103], v116 offset:47104\n" \
  "v_mfma_f32_16x16x32_bf16 v[16:19], v[104:107], v[48:51], v[16:19]\n" \
  "s_waitcnt vmcnt(9) lgkmcnt(5)\n" \
  "ds_read_b128 v[104:107], v116 offset:48128\n" \
  "v_mfma_f32_16x16x32_bf16 v[0:3], v[76:79], v[52:55], v[0:3]\n" \
  "ds_read_b128 v[76:79], v116 offset:49152\n" \
  "buffer_load_dwordx4 v[36:39], v117, %[srd], %[so] offen offset:0 sc1\n" \
  "v_mfma_f32_16x16x32_bf16 v[4:7], v[80:83], v[52:55], v[4:7]\n" \
  "s_waitcnt lgkmcnt(5)\n" \
  "ds_read_b128 v[80:83], v116 offset:50176\n" \
  "v_mfma_f32_16x16x32_bf16 v[8:11], v[84:87], v[52:55], v[8:11]\n" \
  "ds_read_b128 v[84:87], v116 offset:51200\n" \
  "v_mfma_f32_16x16x32_bf16 v[12:15], v[88:91], v[52:55], v[12:15]\n" \
  "s_waitcnt lgkmcnt(5)\n" \
  "ds_read_b128 v[88:91], v116 offset:52224\n" \
  "v_mfma_f32_16x16x32_bf16 v[16:19], v[92:95], v[52:55], v[16:19]\n" \
  "s_waitcnt vmcnt(9)\n" \
  "ds_read_b128 v[92:95], v116 offset:53248\n" \
  "v_mfma_f32_16x16x32_bf16 v[0:3], v[96:99], v[56:59], v[0:3]\n" \
  "s_waitcnt lgkmcnt(5)\n" \
  "ds_read_b128 v[96:99], v116 offset:54272\n" \
  "buffer_load_dwordx4 v[40:43], v117, %[srd], %[so] offen offset:2048 sc1\n" \
  "v_mfma_f32_16x16x32_bf16 v[4:7], v[100:103], v[56:59], v[4:7]\n" \
  "ds_read_b128 v[100:103], v116 offset:55296\n" \
  "v_mfma_f32_16x16x32_bf16 v[8:11], v[104:107], v[56:59], v[8:11]\n" \
  "s_waitcnt lgkmcnt(5)\n" \
  "ds_read_b128 v[104:107], v116 offset:56320\n" \
  "s_add_u32 %[so], %[so], 4096\n" \
  "v_mfma_f32_16x16x32_bf16 v[12:15], v[76:79], v[56:59], v[12:15]\n" \
  "ds_read_b128 v[76:79], v116 offset:57344\n" \
  "v_mfma_f32_16x16x32_bf16 v[16:19], v[80:83], v[56:59], v[16:19]\n" \
  "s_waitcnt vmcnt(9) lgkmcnt(5)\n" \
  "ds_read_b128 v[80:83], v116 offset:58368\n" \
  "v_mfma_f32_16x16x32_bf16 v[0:3], v[84:87], v[60:63], v[0:3]\n" \
  "ds_read_b128 v[84:87], v116 offset:59392\n" \
  "v_mfma_f32_16x16x32_bf16 v[4:7], v[88:91], v[60:63], v[4:7]\n" \
  "s_waitcnt lgkmcnt(5)\n" \
  "ds_read_b128 v[88:91], v116 offset:60416\n" \
  "v_mfma_f32_16x16x32_bf16 v[8:11], v[92:95], v[60:63], v[8:11]\n" \
  "ds_read_b128 v[92:95], v128 offset:0\n" \
  "v_mfma_f32_16x16x32_bf16 v[12:15], v[96:99], v[60:63], v[12:15]\n" \
  "s_waitcnt lgkmcnt(5)\n" \
  "ds_read_b128 v[96:99], v128 offset:1024\n" \
  "v_mfma_f32_16x16x32_bf16 v[16:19], v[100:103], v[60:63], v[16:19]\n" \
  "s_waitcnt vmcnt(8)\n" \
  "ds_read_b128 v[100:103], v128 offset:2048\n" \
  "v_mfma_f32_16x16x32_bf16 v[0:3], v[104:107], v[64:67], v[0:3]\n" \
  "s_waitcnt lgkmcnt(5)\n" \
  "ds_read_b128 v[104:107], v128 offset:3072\n" \
  "v_mfma_f32_16x16x32_bf16 v[4:7], v[76:79], v[64:67], v[4:7]\n" \
  "ds_read_b128 v[76:79], v128 offset:4096\n" \
  "v_mfma_f32_16x16x32_bf16 v[8:11], v[80:83], v[64:67], v[8:11]\n" \
  "s_waitcnt lgkmcnt(5)\n" \
  "ds_read_b128 v[80:83], v128 offset:5120\n" \
  "v_mfma_f32_16x16x32_bf16 v[12:15], v[84:87], v[64:67], v[12:15]\n" \
  "ds_read_b128 v[84:87], v128 offset:6144\n" \
  "v_mfma_f32_16x16x32_bf16 v[16:19], v[88:91], v[64:67], v[16:19]\n" \
  "s_waitcnt vmcnt(7) lgkmcnt(5)\n" \
  "ds_read_b128 v[88:91], v128 offset:7168\n" \
  "v_mfma_f32_16x16x32_bf16 v[0:3], v[92:95], v[68:71], v[0:3]\n" \
  "ds_read_b128 v[92:95], v128 offset:8192\n" \
  "v_mfma_f32_16x16x32_bf16 v[4:7], v[96:99], v[68:71], v[4:7]\n" \
  "s_waitcnt lgkmcnt(5)\n" \
  "ds_read_b128 v[96:99], v128 offset:9216\n" \
  "v_mfma_f32_16x16x32_bf16 v[8:11], v[100:103], v[68:71], v[8:11]\n" \
  "ds_read_b128 v[100:103], v128 offset:10240\n" \
  "v_mfma_f32_16x16x32_bf16 v[12:15], v[104:107], v[68:71], v[12:15]\n" \
  "s_waitcnt lgkmcnt(5)\n" \
  "ds_read_b128 v[104:107], v128 offset:11264\n" \
  "v_mfma_f32_16x16x32_bf16 v[16:19], v[76:79], v[68:71], v[16:19]\n" \
  "s_waitcnt vmcnt(6)\n" \
  "ds_read_b128 v[76:79], v128 offset:12288\n" \
  "v_mfma_f32_16x16x32_bf16 v[0:3], v[80:83], v[72:75], v[0:3]\n" \
  "s_waitcnt lgkmcnt(5)\n" \
  "ds_read_b128 v[80:83], v128 offset:13312\n" \
  "v_mfma_f32_16x16x32_bf16 v[4:7], v[84:87], v[72:75], v[4:7]\n" \
  "ds_read_b128 v[84:87], v128 offset:14336\n" \
  "v_mfma_f32_16x16x32_bf16 v[8:11], v[88:91], v[72:75], v[8:11]\n" \
  "s_waitcnt lgkmcnt(5)\n" \
  "ds_read_b128 v[88:91], v128 offset:15360\n" \
  "v_mfma_f32_16x16x32_bf16 v[12:15], v[92:95], v[72:75], v[12:15]\n" \
  "ds_read_b128 v[92:95], v128 offset:16384\n" \
  "v_mfma_f32_16x16x32_bf16 v[16:19], v[96:99], v[72:75], v[16:19]\n" \
  "s_waitcnt vmcnt(5) lgkmcnt(5)\n" \
  "ds_read_b128 v[96:99], v128 offset:17408\n" \
  "v_mfma_f32_16x16x32_bf16 v[0:3], v[100:103], v[20:23], v[0:3]\n" \
  "ds_read_b128 v[100:103], v128 offset:18432\n" \
  "v_mfma_f32_16x16x32_bf16 v[4:7], v[104:107], v[20:23], v[4:7]\n" \
  "s_waitcnt lgkmcnt(5)\n" \
  "ds_read_b128 v[104:107], v128 offset:19456\n" \
  "v_mfma_f32_16x16x32_bf16 v[8:11], v[76:79], v[20:23], v[8:11]\n" \
  "ds_read_b128 v[76:79], v128 offset:20480\n" \
  "v_mfma_f32_16x16x32_bf16 v[12:15], v[80:83], v[20:23], v[12:15]\n" \
  "s_waitcnt lgkmcnt(5)\n" \
  "ds_read_b128 v[80:83], v128 offset:21504\n" \
  "v_mfma_f32_16x16x32_bf16 v[16:19], v[84:87], v[20:23], v[16:19]\n" \
  "s_waitcnt vmcnt(4)\n" \
  "ds_read_b128 v[84:87], v128 offset:22528\n" \
  "v_mfma_f32_16x16x32_bf16 v[0:3], v[88:91], v[24:27], v[0:3]\n" \
  "s_waitcnt lgkmcnt(5)\n" \
  "ds_read_b128 v[88:91], v128 offset:23552\n" \
  "v_mfma_f32_16x16x32_bf16 v[4:7], v[92:95], v[24:27], v[4:7]\n" \
  ".Lk_pad2:\n" \
  "s_cbranch_execz .Lk_ret2\n" \
  "ds_read_b128 v[92:95], v128 offset:24576\n" \
  "v_mfma_f32_16x16x32_bf16 v[8:11], v[96:99], v[24:27], v[8:11]\n" \
  "s_waitcnt lgkmcnt(5)\n" \
  "ds_read_b128 v[96:99], v128 offset:25600\n" \
  "v_mfma_f32_16x16x32_bf16 v[12:15], v[100:103], v[24:27], v[12:15]\n" \
  "ds_read_b128 v[100:103], v128 offset:26624\n" \
  "v_mfma_f32_16x16x32_bf16 v[16:19], v[104:107], v[24:27], v[16:19]\n" \
  "s_waitcnt vmcnt(3) lgkmcnt(5)\n" \
  "ds_read_b128 v[104:107], v128 offset:27648\n" \
  "v_mfma_f32_16x16x32_bf16 v[0:3], v[76:79], v[28:31], v[0:3]\n" \
  "ds_read_b128 v[76:79], v128 offset:28672\n" \
  "v_mfma_f32_16x16x32_bf16 v[4:7], v[80:83], v[28:31], v[4:7]\n" \
  "s_waitcnt lgkmcnt(5)\n" \
  "ds_read_b128 v[80:83], v128 offset:29696\n" \
  "v_mfma_f32_16x16x32_bf16 v[8:11], v[84:87], v[28:31], v[8:11]\n" \
  "ds_read_b128 v[84:87], v128 offset:30720\n" \
  "v_mfma_f32_16x16x32_bf16 v[12:15], v[88:91], v[28:31], v[12:15]\n" \
  "s_waitcnt lgkmcnt(5)\n" \
  "ds_read_b128 v[88:91], v128 offset:31744\n" \
  "v_mfma_f32_16x16x32_bf16 v[16:19], v[92:95], v[28:31], v[16:19]\n" \
  "s_waitcnt vmcnt(2)\n" \
  "ds_read_b128 v[92:95], v128 offset:32768\n" \
  "v_mfma_f32_16x16x32_bf16 v[0:3], v[96:99], v[32:35], v[0:3]\n" \
  "s_waitcnt lgkmcnt(5)\n" \
  "ds_read_b128 v[96:99], v128 offset:33792\n" \
  "v_mfma_f32_16x16x32_bf16 v[4:7], v[100:103], v[32:35], v[4:7]\n" \
  "ds_read_b128 v[100:103], v128 offset:34816\n" \
  "v_mfma_f32_16x16x32_bf16 v[8:11], v[104:107], v[32:35], v[8:11]\n" \
  "s_waitcnt lgkmcnt(5)\n" \
  "ds_read_b128 v[104:107], v128 offset:35840\n" \
  "v_mfma_f32_16x16x32_bf16 v[12:15], v[76:79], v[32:35], v[12:15]\n" \
  "ds_read_b128 v[76:79], v128 offset:36864\n" \
  "v_mfma_f32_16x16x32_bf16 v[16:19], v[80:83], v[32:35], v[16:19]\n" \
  "s_waitcnt vmcnt(1) lgkmcnt(5)\n" \
  "ds_read_b128 v[80:83], v128 offset:37888\n" \
  "v_mfma_f32_16x16x32_bf16 v[0:3], v[84:87], v[36:39], v[0:3]\n" \
  "ds_read_b128 v[84:87], v128 offset:38912\n" \
  "v_mfma_f32_16x16x32_bf16 v[4:7], v[88:91], v[36:39], v[4:7]\n" \
  "s_waitcnt lgkmcnt(5)\n" \
  "ds_read_b128 v[88:91], v128 offset:39936\n" \
  "v_mfma_f32_16x16x32_bf16 v[8:11], v[92:95], v[36:39], v[8:11]\n" \
  "v_mfma_f32_16x16x32_bf16 v[12:15], v[96:99], v[36:39], v[12:15]\n" \
  "s_waitcnt lgkmcnt(4)\n" \
  "v_mfma_f32_16x16x32_bf16 v[16:19], v[100:103], v[36:39], v[16:19]\n" \
  "s_waitcnt vmcnt(0)\n" \
  "v_mfma_f32_16x16x32_bf16 v[0:3], v[104:107], v[40:43], v[0:3]\n" \
  "s_waitcnt lgkmcnt(2)\n" \
  "v_mfma_f32_16x16x32_bf16 v[4:7], v[76:79], v[40:43], v[4:7]\n" \
  "v_mfma_f32_16x16x32_bf16 v[8:11], v[80:83], v[40:43], v[8:11]\n" \
  "s_waitcnt lgkmcnt(0)\n" \
  "v_mfma_f32_16x16x32_bf16 v[12:15], v[84:87], v[40:43], v[12:15]\n" \
  "v_mfma_f32_16x16x32_bf16 v[16:19], v[88:91], v[40:43], v[16:19]\n" \
  "s_barrier\n" \
  "s_nop 2\n" \
  "v_cvt_pk_bf16_f32 v132, v0, v1\n" \
  "v_cvt_pk_bf16_f32 v133, v2, v3\n" \
  "v_cvt_pk_bf16_f32 v134, v4, v5\n" \
  "v_cvt_pk_bf16_f32 v135, v6, v7\n" \
  "s_nop 0\n" \
  "v_permlane16_swap_b32 v132, v134\n" \
  "v_permlane16_swap_b32 v133, v135\n" \
  "buffer_store_dwordx4 v[132:135], v122, %[srd], %[hs] offen offset:0 sc1\n" \
  "s_nop 0\n" \
  "v_mfma_f32_16x16x32_bf16 v[152:155], v[132:135], v[132:135], 0\n" \
  "v_mfma_f32_16x16x32_bf16 v[148:151], v[144:147], v[132:135], 0\n" \
  "v_cvt_pk_bf16_f32 v136, v8, v9\n" \
  "v_cvt_pk_bf16_f32 v137, v10, v11\n" \
  "v_cvt_pk_bf16_f32 v138, v12, v13\n" \
  "v_cvt_pk_bf16_f32 v139, v14, v15\n" \
  "s_nop 0\n" \
  "v_permlane16_swap_b32 v136, v138\n" \
  "v_permlane16_swap_b32 v137, v139\n" \
  "buffer_store_dwordx4 v[136:139], v122, %[srd], %[hs] offen offset:2048 sc1\n" \
  "s_nop 0\n" \
  "v_mfma_f32_16x16x32_bf16 v[152:155], v[136:139], v[136:139], v[152:155]\n" \
  "v_mfma_f32_16x16x32_bf16 v[148:151], v[144:147], v[136:139], v[148:151]\n" \
  "v_cvt_pk_bf16_f32 v140, v16, v17\n" \
  "v_cvt_pk_bf16_f32 v141, v18, v19\n" \
  "s_nop 0\n" \
  "v_permlane16_swap_b32 v140, v142\n" \
  "v_permlane16_swap_b32 v141, v143\n" \
  "s_mov_b32 exec_lo, 0xffff\n" \
  "s_mov_b32 exec_hi, 0xffff\n" \
  "s_add_u32 %[so], %[hs], 4096\n" \
  "buffer_store_dwordx4 v[140:143], v122, %[srd], %[so] offen sc1\n" \
  "s_mov_b64 exec, -1\n" \
  "v_mfma_f32_16x16x32_bf16 v[152:155], v[140:143], v[140:143], v[152:155]\n" \
  "v_mfma_f32_16x16x32_bf16 v[148:151], v[144:147], v[140:143], v[148:151]\n" \
  "s_mov_b32 vcc_lo, 0xaaaaaaaa\n" \
  "s_mov_b32 vcc_hi, 0xaaaaaaaa\n" \
  "s_nop 4\n" \
  "v_cndmask_b32 v152, v152, v153, vcc\n" \
  "v_cndmask_b32 v154, v154, v155, vcc\n" \
  "s_mov_b32 vcc_lo, 0xcccccccc\n" \
  "s_mov_b32 vcc_hi, 0xcccccccc\n" \
  "v_cndmask_b32 v149, v152, v154, vcc\n" \
  "s_mov_b32 exec_lo, 0xf0000f\n" \
  "s_mov_b32 exec_hi, 0xf0000f00\n" \
  "buffer_store_dwordx2 v[148:149], v124, %[srd], 0 offen sc1\n" \
  "s_mov_b64 exec, -1\n"
#define P5_ASM \
  "s_mov_b64 exec, 255\n" \
  "s_waitcnt vmcnt(3)\n" \
  "buffer_atomic_add v126, v125, %[srd], 0 offen sc1\n" \
  "s_mov_b64 exec, -1\n" \
  "v_add_u32 v128, 61440, v116\n" \
  "v_add_u32 v129, 122880, v116\n" \
  "v_mov_b64 v[20:21], 0\n" \
  "v_mov_b64 v[22:23], 0\n" \
  "v_mov_b64 v[24:25], 0\n" \
  "v_mov_b64 v[26:27], 0\n" \
  "v_mov_b64 v[28:29], 0\n" \
  "v_mov_b64 v[30:31], 0\n" \
  "v_mov_b64 v[32:33], 0\n" \
  "v_mov_b64 v[34:35], 0\n" \
  "v_mov_b64 v[36:37], 0\n" \
  "v_mov_b64 v[38:39], 0\n" \
  "ds_read_b128 v[132:135], v116 offset:0\n" \
  "ds_read_b128 v[136:139], v116 offset:1024\n" \
  "ds_read_b128 v[140:143], v116 offset:2048\n" \
  "ds_read_b128 v[144:147], v116 offset:3072\n" \
  "ds_read_b128 v[148:151], v116 offset:4096\n" \
  "ds_read_b128 v[152:155], v116 offset:10240\n" \
  "ds_read_b128 v[156:159], v116 offset:11264\n" \
  "ds_read_b128 v[160:163], v116 offset:12288\n" \
  "ds_read_b128 v[164:167], v116 offset:13312\n" \
  "ds_read_b128 v[168:171], v116 offset:14336\n" \
  "v_mov_b32 v130, 0\n" \
  "s_mov_b32 %[cnt], 0x40000\n" \
  "s_mov_b64 exec, 1\n" \
  "global_atomic_add v88, v119, v130, %[fl] sc0 sc1\n" \
  "s_mov_b64 exec, -1\n" \
  "ds_read_b128 v[172:175], v116 offset:20480\n" \
  "ds_read_b128 v[176:179], v116 offset:21504\n" \
  "ds_read_b128 v[180:183], v116 offset:22528\n" \
  "ds_read_b128 v[184:187], v116 offset:23552\n" \
  "ds_read_b128 v[188:191], v116 offset:24576\n" \
  "ds_read_b128 v[192:195], v116 offset:30720\n" \
  "ds_read_b128 v[196:199], v116 offset:31744\n" \
  "ds_read_b128 v[200:203], v116 offset:32768\n" \
  "ds_read_b128 v[204:207], v116 offset:33792\n" \
  "ds_read_b128 v[208:211], v116 offset:34816\n" \
  "s_mov_b64 exec, 1\n" \
  "global_atomic_add v89, v119, v130, %[fl] sc0 sc1\n" \
  "s_mov_b64 exec, -1\n" \
  "ds_read_b128 v[212:215], v116 offset:40960\n" \
  "ds_read_b128 v[216:219], v116 offset:41984\n" \
  "ds_read_b128 v[220:223], v116 offset:43008\n" \
  "ds_read_b128 v[224:227], v116 offset:44032\n" \
  "ds_read_b128 v[228:231], v116 offset:45056\n" \
  "ds_read_b128 v[232:235], v116 offset:51200\n" \
  "ds_read_b128 v[236:239], v116 offset:52224\n" \
  "ds_read_b128 v[240:243], v116 offset:53248\n" \
  "ds_read_b128 v[244:247], v116 offset:54272\n" \
  "ds_read_b128 v[248:251], v116 offset:55296\n" \
  "s_mov_b64 exec, 1\n" \
  "global_atomic_add v90, v119, v130, %[fl] sc0 sc1\n" \
  "s_mov_b64 exec, -1\n" \
  "s_mov_b64 exec, 255\n" \
  "s_waitcnt vmcnt(3)\n" \
  "buffer_atomic_add v126, v125, %[srd], 0 offen offset:1024 sc1\n" \
  "s_mov_b64 exec, -1\n" \
  "s_mov_b64 exec, 1\n" \
  "s_waitcnt vmcnt(3)\n" \
  "v_cmp_ne_u32 vcc, 16, v88\n" \
  "s_cbranch_vccz 8f\n" \
  "global_atomic_add v88, v119, v130, %[fl] sc0 sc1\n" \
  "s_waitcnt vmcnt(3)\n" \
  "v_cmp_ne_u32 vcc, 16, v89\n" \
  "s_cbranch_vccz 8f\n" \
  "global_atomic_add v89, v119, v130, %[fl] sc0 sc1\n" \
  "s_waitcnt vmcnt(3)\n" \
  "v_cmp_ne_u32 vcc, 16, v90\n" \
  "s_cbranch_vccz 8f\n" \
  "global_atomic_add v90, v119, v130, %[fl] sc0 sc1\n" \
  "9:\n" \
  "s_waitcnt vmcnt(2)\n" \
  "v_cmp_ne_u32 vcc, 16, v88\n" \
  "s_cbranch_vccz 8f\n" \
  "global_atomic_add v88, v119, v130, %[fl] sc0 sc1\n" \
  "s_waitcnt vmcnt(2)\n" \
  "v_cmp_ne_u32 vcc, 16, v89\n" \
  "s_cbranch_vccz 8f\n" \
  "global_atomic_add v89, v119, v130, %[fl] sc0 sc1\n" \
  "s_waitcnt vmcnt(2)\n" \
  "v_cmp_ne_u32 vcc, 16, v90\n" \
  "s_cbranch_vccz 8f\n" \
  "global_atomic_add v90, v119, v130, %[fl] sc0 sc1\n" \
  "s_sub_u32 %[cnt], %[cnt], 1\n" \
  "s_cbranch_scc0 9b\n" \
  "8:\n" \
  "s_mov_b64 exec, -1\n" \
  "s_add_u32 %[so], %[ab], 43008\n" \
  "buffer_load_dwordx4 v[40:43], v117, %[srd], %[so] offen sc1\n" \
  "buffer_load_dwordx4 v[44:47], v118, %[srd], %[so] offen sc1\n" \
  "s_add_u32 %[so], %[ab], 51200\n" \
  "buffer_load_dwordx4 v[48:51], v117, %[srd], %[so] offen sc1\n" \
  "buffer_load_dwordx4 v[52:55], v118, %[srd], %[so] offen sc1\n" \
  "s_waitcnt lgkmcnt(0)\n" \
  "s_barrier\n" \
  "ds_read_b128 v[96:99], v128 offset:0\n" \
  "ds_read_b128 v[100:103], v128 offset:1024\n" \
  "ds_read_b128 v[104:107], v128 offset:2048\n" \
  "ds_read_b128 v[108:111], v128 offset:3072\n" \
  "ds_read_b128 v[112:115], v128 offset:4096\n" \
  "s_add_u32 %[so], %[ab], 57344\n" \
  "buffer_load_dwordx4 v[56:59], v117, %[srd], %[so] offen sc1\n" \
  "buffer_load_dwordx4 v[60:63], v118, %[srd], %[so] offen sc1\n" \
  "s_add_u32 %[so], %[ab], 65536\n" \
  "buffer_load_dwordx4 v[64:67], v117, %[srd], %[so] offen sc1\n" \
  "buffer_load_dwordx4 v[68:71], v118, %[srd], %[so] offen sc1\n" \
  "s_add_u32 %[so], %[ab], 71680\n" \
  "buffer_load_dwordx4 v[72:75], v117, %[srd], %[so] offen sc1\n" \
  "buffer_load_dwordx4 v[76:79], v118, %[srd], %[so] offen sc1\n" \
  "s_waitcnt vmcnt(8) lgkmcnt(4)\n" \
  "v_mfma_f32_16x16x32_bf16 v[0:3], v[96:99], v[40:43], v[0:3]\n" \
  "s_add_u32 %[so], %[ab], 79872\n" \
  "buffer_load_dwordx4 v[80:83], v117, %[srd], %[so] offen sc1\n" \
  "v_mfma_f32_16x16x32_bf16 v[20:23], v[96:99], v[44:47], v[20:23]\n" \
  "s_waitcnt lgkmcnt(3)\n" \
  "ds_read_b128 v[96:99], v128 offset:10240\n" \
  "v_mfma_f32_16x16x32_bf16 v[24:27], v[100:103], v[44:47], v[24:27]\n" \
  "v_mfma_f32_16x16x32_bf16 v[4:7], v[100:103], v[40:43], v[4:7]\n" \
  "s_waitcnt lgkmcnt(3)\n" \
  "ds_read_b128 v[100:103], v128 offset:11264\n" \
  "v_mfma_f32_16x16x32_bf16 v[8:11], v[104:107], v[40:43], v[8:11]\n" \
  "buffer_load_dwordx4 v[84:87], v118, %[srd], %[so] offen sc1\n" \
  "v_mfma_f32_16x16x32_bf16 v[28:31], v[104:107], v[44:47], v[28:31]\n" \
  "s_waitcnt lgkmcnt(3)\n" \
  "ds_read_b128 v[104:107], v128 offset:12288\n" \
  "v_mfma_f32_16x16x32_bf16 v[32:35], v[108:111], v[44:47], v[32:35]\n" \
  "v_mfma_f32_16x16x32_bf16 v[12:15], v[108:111], v[40:43], v[12:15]\n" \
  "s_waitcnt lgkmcnt(3)\n" \
  "ds_read_b128 v[108:111], v128 offset:13312\n" \
  "v_mfma_f32_16x16x32_bf16 v[16:19], v[112:115], v[40:43], v[16:19]\n" \
  "v_mfma_f32_16x16x32_bf16 v[36:39], v[112:115], v[44:47], v[36:39]\n" \
  "s_waitcnt vmcnt(8) lgkmcnt(3)\n" \
  "ds_read_b128 v[112:115], v128 offset:14336\n" \
  "v_mfma_f32_16x16x32_bf16 v[0:3], v[96:99], v[48:51], v[0:3]\n" \
  "s_add_u32 %[so], %[ab], 0\n" \
  "buffer_load_dwordx4 v[88:91], v117, %[srd], %[so] offen sc1\n" \
  "v_mfma_f32_16x16x32_bf16 v[20:23], v[96:99], v[52:55], v[20:23]\n" \
  "s_waitcnt lgkmcnt(3)\n" \
  "ds_read_b128 v[96:99], v128 offset:20480\n" \
  "v_mfma_f32_16x16x32_bf16 v[24:27], v[100:103], v[52:55], v[24:27]\n" \
  "v_mfma_f32_16x16x32_bf16 v[4:7], v[100:103], v[48:51], v[4:7]\n" \
  "s_waitcnt lgkmcnt(3)\n" \
  "ds_read_b128 v[100:103], v128 offset:21504\n" \
  "v_mfma_f32_16x16x32_bf16 v[8:11], v[104:107], v[48:51], v[8:11]\n" \
  "buffer_load_dwordx4 v[92:95], v118, %[srd], %[so] offen sc1\n" \
  "v_mfma_f32_16x16x32_bf16 v[28:31], v[104:107], v[52:55], v[28:31]\n" \
  "s_waitcnt lgkmcnt(3)\n" \
  "ds_read_b128 v[104:107], v128 offset:22528\n" \
  "v_mfma_f32_16x16x32_bf16 v[32:35], v[108:111], v[52:55], v[32:35]\n" \
  "v_mfma_f32_16x16x32_bf16 v[12:15], v[108:111], v[48:51], v[12:15]\n" \
  "s_waitcnt lgkmcnt(3)\n" \
  "ds_read_b128 v[108:111], v128 offset:23552\n" \
  "v_mfma_f32_16x16x32_bf16 v[16:19], v[112:115], v[48:51], v[16:19]\n" \
  ".Lk_pad4:\n" \
  "s_cbranch_execz .Lk_ret4\n" \
  "v_mfma_f32_16x16x32_bf16 v[36:39], v[112:115], v[52:55], v[36:39]\n" \
  "s_waitcnt vmcnt(8) lgkmcnt(3)\n" \
  "ds_read_b128 v[112:115], v128 offset:24576\n" \
  "v_mfma_f32_16x16x32_bf16 v[0:3], v[96:99], v[56:59], v[0:3]\n" \
  "s_add_u32 %[so], %[ab], 8192\n" \
  "buffer_load_dwordx4 v[40:43], v117, %[srd], %[so] offen sc1\n" \
  "v_mfma_f32_16x16x32_bf16 v[20:23], v[96:99], v[60:63], v[20:23]\n" \
  "s_waitcnt lgkmcnt(3)\n" \
  "ds_read_b128 v[96:99], v128 offset:30720\n" \
  "v_mfma_f32_16x16x32_bf16 v[24:27], v[100:103], v[60:63], v[24:27]\n" \
  "v_mfma_f32_16x16x32_bf16 v[4:7], v[100:103], v[56:59], v[4:7]\n" \
  "s_waitcnt lgkmcnt(3)\n" \
  "ds_read_b128 v[100:103], v128 offset:31744\n" \
  "v_mfma_f32_16x16x32_bf16 v[8:11], v[104:107], v[56:59], v[8:11]\n" \
  "buffer_load_dwordx4 v[44:47], v118, %[srd], %[so] offen sc1\n" \
  "v_mfma_f32_16x16x32_bf16 v[28:31], v[104:107], v[60:63], v[28:31]\n" \
  "s_waitcnt lgkmcnt(3)\n" \
  "ds_read_b128 v[104:107], v128 offset:32768\n" \
  "v_mfma_f32_16x16x32_bf16 v[32:35], v[108:111], v[60:63], v[32:35]\n" \
  "v_mfma_f32_16x16x32_bf16 v[12:15], v[108:111], v[56:59], v[12:15]\n" \
  "s_waitcnt lgkmcnt(3)\n" \
  "ds_read_b128 v[108:111], v128 offset:33792\n" \
  "v_mfma_f32_16x16x32_bf16 v[16:19], v[112:115], v[56:59], v[16:19]\n" \
  "v_mfma_f32_16x16x32_bf16 v[36:39], v[112:115], v[60:63], v[36:39]\n" \
  "s_waitcnt vmcnt(8) lgkmcnt(3)\n" \
  "ds_read_b128 v[112:115], v128 offset:34816\n" \
  "v_mfma_f32_16x16x32_bf16 v[0:3], v[96:99], v[64:67], v[0:3]\n" \
  "s_add_u32 %[so], %[ab], 14336\n" \
  "buffer_load_dwordx4 v[48:51], v117, %[srd], %[so] offen sc1\n" \
  "v_mfma_f32_16x16x32_bf16 v[20:23], v[96:99], v[68:71], v[20:23]\n" \
  "s_waitcnt lgkmcnt(3)\n" \
  "ds_read_b128 v[96:99], v128 offset:40960\n" \
  "v_mfma_f32_16x16x32_bf16 v[24:27], v[100:103], v[68:71], v[24:27]\n" \
  "v_mfma_f32_16x16x32_bf16 v[4:7], v[100:103], v[64:67], v[4:7]\n" \
  "s_waitcnt lgkmcnt(3)\n" \
  "ds_read_b128 v[100:103], v128 offset:41984\n" \
  "v_mfma_f32_16x16x32_bf16 v[8:11], v[104:107], v[64:67], v[8:11]\n" \
  "buffer_load_dwordx4 v[52:55], v118, %[srd], %[so] offen sc1\n" \
  "v_mfma_f32_16x16x32_bf16 v[28:31], v[104:107], v[68:71], v[28:31]\n" \
  "s_waitcnt lgkmcnt(3)\n" \
  "ds_read_b128 v[104:107], v128 offset:43008\n" \
  "v_mfma_f32_16x16x32_bf16 v[32:35], v[108:111], v[68:71], v[32:35]\n" \
  "v_mfma_f32_16x16x32_bf16 v[12:15], v[108:111], v[64:67], v[12:15]\n" \
  "s_waitcnt lgkmcnt(3)\n" \
  "ds_read_b128 v[108:111], v128 offset:44032\n" \
  "v_mfma_f32_16x16x32_bf16 v[16:19], v[112:115], v[64:67], v[16:19]\n" \
  "s_mov_b64 exec, 1\n" \
  "global_atomic_add v131, v119, v130, %[fl] offset:1024 sc0 sc1\n" \
  "s_mov_b64 exec, -1\n" \
  "v_mfma_f32_16x16x32_bf16 v[36:39], v[112:115], v[68:71], v[36:39]\n" \
  "s_waitcnt vmcnt(9) lgkmcnt(3)\n" \
  "ds_read_b128 v[112:115], v128 offset:45056\n" \
  "v_mfma_f32_16x16x32_bf16 v[0:3], v[96:99], v[72:75], v[0:3]\n" \
  "s_add_u32 %[so], %[ab], 22528\n" \
  "buffer_load_dwordx4 v[56:59], v117, %[srd], %[so] offen sc1\n" \
  "v_mfma_f32_16x16x32_bf16 v[20:23], v[96:99], v[76:79], v[20:23]\n" \
  "s_waitcnt lgkmcnt(3)\n" \
  "ds_read_b128 v[96:99], v128 offset:51200\n" \
  "v_mfma_f32_16x16x32_bf16 v[24:27], v[100:103], v[76:79], v[24:27]\n" \
  "v_mfma_f32_16x16x32_bf16 v[4:7], v[100:103], v[72:75], v[4:7]\n" \
  "s_waitcnt lgkmcnt(3)\n" \
  "ds_read_b128 v[100:103], v128 offset:52224\n" \
  "v_mfma_f32_16x16x32_bf16 v[8:11], v[104:107], v[72:75], v[8:11]\n" \
  "buffer_load_dwordx4 v[60:63], v118, %[srd], %[so] offen sc1\n" \
  "v_mfma_f32_16x16x32_bf16 v[28:31], v[104:107], v[76:79], v[28:31]\n" \
  "s_waitcnt lgkmcnt(3)\n" \
  "ds_read_b128 v[104:107], v128 offset:53248\n" \
  "v_mfma_f32_16x16x32_bf16 v[32:35], v[108:111], v[76:79], v[32:35]\n" \
  "v_mfma_f32_16x16x32_bf16 v[12:15], v[108:111], v[72:75], v[12:15]\n" \
  "s_waitcnt lgkmcnt(3)\n" \
  "ds_read_b128 v[108:111], v128 offset:54272\n" \
  "v_mfma_f32_16x16x32_bf16 v[16:19], v[112:115], v[72:75], v[16:19]\n" \
  "v_mfma_f32_16x16x32_bf16 v[36:39], v[112:115], v[76:79], v[36:39]\n" \
  "s_waitcnt vmcnt(9) lgkmcnt(3)\n" \
  "ds_read_b128 v[112:115], v128 offset:55296\n" \
  "v_mfma_f32_16x16x32_bf16 v[0:3], v[96:99], v[80:83], v[0:3]\n" \
  "s_add_u32 %[so], %[ab], 28672\n" \
  "buffer_load_dwordx4 v[64:67], v117, %[srd], %[so] offen sc1\n" \
  "v_mfma_f32_16x16x32_bf16 v[20:23], v[96:99], v[84:87], v[20:23]\n" \
  "s_waitcnt lgkmcnt(3)\n" \
  "ds_read_b128 v[96:99], v129 offset:0\n" \
  "v_mfma_f32_16x16x32_bf16 v[24:27], v[100:103], v[84:87], v[24:27]\n" \
  "v_mfma_f32_16x16x32_bf16 v[4:7], v[100:103], v[80:83], v[4:7]\n" \
  "s_waitcnt lgkmcnt(3)\n" \
  "ds_read_b128 v[100:103], v129 offset:1024\n" \
  "v_mfma_f32_16x16x32_bf16 v[8:11], v[104:107], v[80:83], v[8:11]\n" \
  "buffer_load_dwordx4 v[68:71], v118, %[srd], %[so] offen sc1\n" \
  "v_mfma_f32_16x16x32_bf16 v[28:31], v[104:107], v[84:87], v[28:31]\n" \
  "s_waitcnt lgkmcnt(3)\n" \
  "ds_read_b128 v[104:107], v129 offset:2048\n" \
  "v_mfma_f32_16x16x32_bf16 v[32:35], v[108:111], v[84:87], v[32:35]\n" \
  "v_mfma_f32_16x16x32_bf16 v[12:15], v[108:111], v[80:83], v[12:15]\n" \
  "s_waitcnt lgkmcnt(3)\n" \
  "ds_read_b128 v[108:111], v129 offset:3072\n" \
  "v_mfma_f32_16x16x32_bf16 v[16:19], v[112:115], v[80:83], v[16:19]\n" \
  "v_mfma_f32_16x16x32_bf16 v[36:39], v[112:115], v[84:87], v[36:39]\n" \
  "s_barrier\n" \
  "s_waitcnt vmcnt(9)\n" \
  "ds_read_b128 v[112:115], v129 offset:4096\n" \
  "v_mfma_f32_16x16x32_bf16 v[0:3], v[132:135], v[88:91], v[0:3]\n" \
  "s_add_u32 %[so], %[ab], 36864\n" \
  "buffer_load_dwordx4 v[72:75], v117, %[srd], %[so] offen sc1\n" \
  "v_mfma_f32_16x16x32_bf16 v[20:23], v[132:135], v[92:95], v[20:23]\n" \
  "v_mfma_f32_16x16x32_bf16 v[24:27], v[136:139], v[92:95], v[24:27]\n" \
  "v_mfma_f32_16x16x32_bf16 v[4:7], v[136:139], v[88:91], v[4:7]\n" \
  "v_mfma_f32_16x16x32_bf16 v[8:11], v[140:143], v[88:91], v[8:11]\n" \
  "buffer_load_dwordx4 v[76:79], v118, %[srd], %[so] offen sc1\n" \
  "v_mfma_f32_16x16x32_bf16 v[28:31], v[140:143], v[92:95], v[28:31]\n" \
  "v_mfma_f32_16x16x32_bf16 v[32:35], v[144:147], v[92:95], v[32:35]\n" \
  "v_mfma_f32_16x16x32_bf16 v[12:15], v[144:147], v[88:91], v[12:15]\n" \
  "v_mfma_f32_16x16x32_bf16 v[16:19], v[148:151], v[88:91], v[16:19]\n" \
  "v_mfma_f32_16x16x32_bf16 v[36:39], v[148:151], v[92:95], v[36:39]\n" \
  "s_waitcnt vmcnt(9)\n" \
  "v_mfma_f32_16x16x32_bf16 v[0:3], v[152:155], v[40:43], v[0:3]\n" \
  "s_add_u32 %[so], %[ab], 86016\n" \
  "buffer_load_dwordx4 v[80:83], v117, %[srd], %[so] offen sc1\n" \
  "v_mfma_f32_16x16x32_bf16 v[20:23], v[152:155], v[44:47], v[20:23]\n" \
  "v_mfma_f32_16x16x32_bf16 v[24:27], v[156:159], v[44:47], v[24:27]\n" \
  "v_mfma_f32_16x16x32_bf16 v[4:7], v[156:159], v[40:43], v[4:7]\n" \
  "v_mfma_f32_16x16x32_bf16 v[8:11], v[160:163], v[40:43], v[8:11]\n" \
  "buffer_load_dwordx4 v[84:87], v118, %[srd], %[so] offen sc1\n" \
  "v_mfma_f32_16x16x32_bf16 v[28:31], v[160:163], v[44:47], v[28:31]\n" \
  "v_mfma_f32_16x16x32_bf16 v[32:35], v[164:167], v[44:47], v[32:35]\n" \
  "v_mfma_f32_16x16x32_bf16 v[12:15], v[164:167], v[40:43], v[12:15]\n" \
  "v_mfma_f32_16x16x32_bf16 v[16:19], v[168:171], v[40:43], v[16:19]\n" \
  "v_mfma_f32_16x16x32_bf16 v[36:39], v[168:171], v[44:47], v[36:39]\n" \
  "s_waitcnt vmcnt(9)\n" \
  "v_mfma_f32_16x16x32_bf16 v[0:3], v[172:175], v[48:51], v[0:3]\n" \
  "s_add_u32 %[so], %[ab], 94208\n" \
  "buffer_load_dwordx4 v[88:91], v117, %[srd], %[so] offen sc1\n" \
  "v_mfma_f32_16x16x32_bf16 v[20:23], v[172:175], v[52:55], v[20:23]\n" \
  "v_mfma_f32_16x16x32_bf16 v[24:27], v[176:179], v[52:55], v[24:27]\n" \
  "v_mfma_f32_16x16x32_bf16 v[4:7], v[176:179], v[48:51], v[4:7]\n" \
  "v_mfma_f32_16x16x32_bf16 v[8:11], v[180:183], v[48:51], v[8:11]\n" \
  "buffer_load_dwordx4 v[92:95], v118, %[srd], %[so] offen sc1\n" \
  "v_mfma_f32_16x16x32_bf16 v[28:31], v[180:183], v[52:55], v[28:31]\n" \
  "v_mfma_f32_16x16x32_bf16 v[32:35], v[184:187], v[52:55], v[32:35]\n" \
  "v_mfma_f32_16x16x32_bf16 v[12:15], v[184:187], v[48:51], v[12:15]\n" \
  "v_mfma_f32_16x16x32_bf16 v[16:19], v[188:191], v[48:51], v[16:19]\n" \
  "v_mfma_f32_16x16x32_bf16 v[36:39], v[188:191], v[52:55], v[36:39]\n" \
  "s_waitcnt vmcnt(8)\n" \
  "v_mfma_f32_16x16x32_bf16 v[0:3], v[192:195], v[56:59], v[0:3]\n" \
  "s_add_u32 %[so], %[ab], 100352\n" \
  "buffer_load_dwordx4 v[40:43], v117, %[srd], %[so] offen sc1\n" \
  "v_mfma_f32_16x16x32_bf16 v[20:23], v[192:195], v[60:63], v[20:23]\n" \
  "v_mfma_f32_16x16x32_bf16 v[24:27], v[196:199], v[60:63], v[24:27]\n" \
  "v_mfma_f32_16x16x32_bf16 v[4:7], v[196:199], v[56:59], v[4:7]\n" \
  "v_mfma_f32_16x16x32_bf16 v[8:11], v[200:203], v[56:59], v[8:11]\n" \
  "buffer_load_dwordx4 v[44:47], v118, %[srd], %[so] offen sc1\n" \
  "v_mfma_f32_16x16x32_bf16 v[28:31], v[200:203], v[60:63], v[28:31]\n" \
  "v_mfma_f32_16x16x32_bf16 v[32:35], v[204:207], v[60:63], v[32:35]\n" \
  "v_mfma_f32_16x16x32_bf16 v[12:15], v[204:207], v[56:59], v[12:15]\n" \
  "v_mfma_f32_16x16x32_bf16 v[16:19], v[208:211], v[56:59], v[16:19]\n" \
  "v_mfma_f32_16x16x32_bf16 v[36:39], v[208:211], v[60:63], v[36:39]\n" \
  "s_waitcnt vmcnt(8)\n" \
  "v_readfirstlane_b32 %[cnt], v131\n" \
  "s_nop 0\n" \
  "s_cmp_lg_u32 %[cnt], 16\n" \
  "s_cbranch_scc1 4f\n" \
  "3:\n" \
  "v_mfma_f32_16x16x32_bf16 v[0:3], v[212:215], v[64:67], v[0:3]\n" \
  "s_add_u32 %[so], %[ab], 4096\n" \
  "buffer_load_dwordx4 v[48:51], v117, %[srd], %[so] offen sc1\n" \
  "v_mfma_f32_16x16x32_bf16 v[20:23], v[212:215], v[68:71], v[20:23]\n" \
  "v_mfma_f32_16x16x32_bf16 v[24:27], v[216:219], v[68:71], v[24:27]\n" \
  "v_mfma_f32_16x16x32_bf16 v[4:7], v[216:219], v[64:67], v[4:7]\n" \
  "v_mfma_f32_16x16x32_bf16 v[8:11], v[220:223], v[64:67], v[8:11]\n" \
  "buffer_load_dwordx4 v[52:55], v118, %[srd], %[so] offen sc1\n" \
  "v_mfma_f32_16x16x32_bf16 v[28:31], v[220:223], v[68:71], v[28:31]\n" \
  "v_mfma_f32_16x16x32_bf16 v[32:35], v[224:227], v[68:71], v[32:35]\n" \
  "v_mfma_f32_16x16x32_bf16 v[12:15], v[224:227], v[64:67], v[12:15]\n" \
  "v_mfma_f32_16x16x32_bf16 v[16:19], v[228:231], v[64:67], v[16:19]\n" \
  "v_mfma_f32_16x16x32_bf16 v[36:39], v[228:231], v[68:71], v[36:39]\n" \
  "s_waitcnt vmcnt(8)\n" \
  "v_mfma_f32_16x16x32_bf16 v[0:3], v[232:235], v[72:75], v[0:3]\n" \
  "s_add_u32 %[so], %[ab], 18432\n" \
  "buffer_load_dwordx4 v[56:59], v117, %[srd], %[so] offen sc1\n" \
  "v_mfma_f32_16x16x32_bf16 v[20:23], v[232:235], v[76:79], v[20:23]\n" \
  "v_mfma_f32_16x16x32_bf16 v[24:27], v[236:239], v[76:79], v[24:27]\n" \
  "v_mfma_f32_16x16x32_bf16 v[4:7], v[236:239], v[72:75], v[4:7]\n" \
  "v_mfma_f32_16x16x32_bf16 v[8:11], v[240:243], v[72:75], v[8:11]\n" \
  "buffer_load_dwordx4 v[60:63], v118, %[srd], %[so] offen sc1\n" \
  "v_mfma_f32_16x16x32_bf16 v[28:31], v[240:243], v[76:79], v[28:31]\n" \
  "v_mfma_f32_16x16x32_bf16 v[32:35], v[244:247], v[76:79], v[32:35]\n" \
  "v_mfma_f32_16x16x32_bf16 v[12:15], v[244:247], v[72:75], v[12:15]\n" \
  "v_mfma_f32_16x16x32_bf16 v[16:19], v[248:251], v[72:75], v[16:19]\n" \
  "v_mfma_f32_16x16x32_bf16 v[36:39], v[248:251], v[76:79], v[36:39]\n" \
  "s_waitcnt vmcnt(8) lgkmcnt(4)\n" \
  "v_mfma_f32_16x16x32_bf16 v[0:3], v[96:99], v[80:83], v[0:3]\n" \
  "s_add_u32 %[so], %[ab], 32768\n" \
  "buffer_load_dwordx4 v[64:67], v117, %[srd], %[so] offen sc1\n" \
  "v_mfma_f32_16x16x32_bf16 v[20:23], v[96:99], v[84:87], v[20:23]\n" \
  "s_waitcnt lgkmcnt(3)\n" \
  "ds_read_b128 v[96:99], v129 offset:10240\n" \
  "v_mfma_f32_16x16x32_bf16 v[24:27], v[100:103], v[84:87], v[24:27]\n" \
  "v_mfma_f32_16x16x32_bf16 v[4:7], v[100:103], v[80:83], v[4:7]\n" \
  "s_waitcnt lgkmcnt(3)\n" \
  "ds_read_b128 v[100:103], v129 offset:11264\n" \
  "v_mfma_f32_16x16x32_bf16 v[8:11], v[104:107], v[80:83], v[8:11]\n" \
  "buffer_load_dwordx4 v[68:71], v118, %[srd], %[so] offen sc1\n" \
  "v_mfma_f32_16x16x32_bf16 v[28:31], v[104:107], v[84:87], v[28:31]\n" \
  "s_waitcnt lgkmcnt(3)\n" \
  "ds_read_b128 v[104:107], v129 offset:12288\n" \
  "v_mfma_f32_16x16x32_bf16 v[32:35], v[108:111], v[84:87], v[32:35]\n" \
  "v_mfma_f32_16x16x32_bf16 v[12:15], v[108:111], v[80:83], v[12:15]\n" \
  "s_waitcnt lgkmcnt(3)\n" \
  "ds_read_b128 v[108:111], v129 offset:13312\n" \
  "v_mfma_f32_16x16x32_bf16 v[16:19], v[112:115], v[80:83], v[16:19]\n" \
  "v_mfma_f32_16x16x32_bf16 v[36:39], v[112:115], v[84:87], v[36:39]\n" \
  "s_waitcnt vmcnt(8) lgkmcnt(3)\n" \
  "ds_read_b128 v[112:115], v129 offset:14336\n" \
  "v_mfma_f32_16x16x32_bf16 v[0:3], v[96:99], v[88:91], v[0:3]\n" \
  "s_add_u32 %[so], %[ab], 47104\n" \
  "buffer_load_dwordx4 v[72:75], v117, %[srd], %[so] offen sc1\n" \
  "v_mfma_f32_16x16x32_bf16 v[20:23], v[96:99], v[92:95], v[20:23]\n" \
  "s_waitcnt lgkmcnt(3)\n" \
  "ds_read_b128 v[96:99], v129 offset:20480\n" \
  "v_mfma_f32_16x16x32_bf16 v[24:27], v[100:103], v[92:95], v[24:27]\n" \
  "v_mfma_f32_16x16x32_bf16 v[4:7], v[100:103], v[88:91], v[4:7]\n" \
  "s_waitcnt lgkmcnt(3)\n" \
  "ds_read_b128 v[100:103], v129 offset:21504\n" \
  "v_mfma_f32_16x16x32_bf16 v[8:11], v[104:107], v[88:91], v[8:11]\n" \
  "buffer_load_dwordx4 v[76:79], v118, %[srd], %[so] offen sc1\n" \
  "v_mfma_f32_16x16x32_bf16 v[28:31], v[104:107], v[92:95], v[28:31]\n" \
  "s_waitcnt lgkmcnt(3)\n" \
  "ds_read_b128 v[104:107], v129 offset:22528\n" \
  "v_mfma_f32_16x16x32_bf16 v[32:35], v[108:111], v[92:95], v[32:35]\n" \
  "v_mfma_f32_16x16x32_bf16 v[12:15], v[108:111], v[88:91], v[12:15]\n" \
  "s_waitcnt lgkmcnt(3)\n" \
  "ds_read_b128 v[108:111], v129 offset:23552\n" \
  "v_mfma_f32_16x16x32_bf16 v[16:19], v[112:115], v[88:91], v[16:19]\n" \
  "v_mfma_f32_16x16x32_bf16 v[36:39], v[112:115], v[92:95], v[36:39]\n" \
  "s_waitcnt vmcnt(8) lgkmcnt(3)\n" \
  "ds_read_b128 v[112:115], v129 offset:24576\n" \
  "v_mfma_f32_16x16x32_bf16 v[0:3], v[96:99], v[40:43], v[0:3]\n" \
  "s_add_u32 %[so], %[ab], 61440\n" \
  "buffer_load_dwordx4 v[80:83], v117, %[srd], %[so] offen sc1\n" \
  "v_mfma_f32_16x16x32_bf16 v[20:23], v[96:99], v[44:47], v[20:23]\n" \
  "s_waitcnt lgkmcnt(3)\n" \
  "ds_read_b128 v[96:99], v116 offset:0\n" \
  "v_mfma_f32_16x16x32_bf16 v[24:27], v[100:103], v[44:47], v[24:27]\n" \
  "v_mfma_f32_16x16x32_bf16 v[4:7], v[100:103], v[40:43], v[4:7]\n" \
  "s_waitcnt lgkmcnt(3)\n" \
  "ds_read_b128 v[100:103], v116 offset:1024\n" \
  "v_mfma_f32_16x16x32_bf16 v[8:11], v[104:107], v[40:43], v[8:11]\n" \
  "buffer_load_dwordx4 v[84:87], v118, %[srd], %[so] offen sc1\n" \
  "v_mfma_f32_16x16x32_bf16 v[28:31], v[104:107], v[44:47], v[28:31]\n" \
  "s_waitcnt lgkmcnt(3)\n" \
  "ds_read_b128 v[104:107], v116 offset:2048\n" \
  "v_mfma_f32_16x16x32_bf16 v[32:35], v[108:111], v[44:47], v[32:35]\n" \
  "v_mfma_f32_16x16x32_bf16 v[12:15], v[108:111], v[40:43], v[12:15]\n" \
  "s_waitcnt lgkmcnt(3)\n" \
  "ds_read_b128 v[108:111], v116 offset:3072\n" \
  "v_mfma_f32_16x16x32_bf16 v[16:19], v[112:115], v[40:43], v[16:19]\n" \
  "v_mov_b64 v[142:143], 0\n" \
  "v_mfma_f32_16x16x32_bf16 v[36:39], v[112:115], v[44:47], v[36:39]\n" \
  "s_waitcnt vmcnt(8) lgkmcnt(3)\n" \
  "ds_read_b128 v[112:115], v116 offset:4096\n" \
  "v_mfma_f32_16x16x32_bf16 v[0:3], v[96:99], v[48:51], v[0:3]\n" \
  "s_add_u32 %[so], %[ab], 75776\n" \
  "buffer_load_dwordx4 v[88:91], v117, %[srd], %[so] offen sc1\n" \
  "v_mfma_f32_16x16x32_bf16 v[20:23], v[96:99], v[52:55], v[20:23]\n" \
  "s_waitcnt lgkmcnt(3)\n" \
  "ds_read_b128 v[96:99], v116 offset:10240\n" \
  "v_mfma_f32_16x16x32_bf16 v[24:27], v[100:103], v[52:55], v[24:27]\n" \
  "v_mfma_f32_16x16x32_bf16 v[4:7], v[100:103], v[48:51], v[4:7]\n" \
  "s_waitcnt lgkmcnt(3)\n" \
  "ds_read_b128 v[100:103], v116 offset:11264\n" \
  "v_mfma_f32_16x16x32_bf16 v[8:11], v[104:107], v[48:51], v[8:11]\n" \
  "buffer_load_dwordx4 v[92:95], v118, %[srd], %[so] offen sc1\n" \
  "v_mfma_f32_16x16x32_bf16 v[28:31], v[104:107], v[52:55], v[28:31]\n" \
  "s_waitcnt lgkmcnt(3)\n" \
  "ds_read_b128 v[104:107], v116 offset:12288\n" \
  "v_mfma_f32_16x16x32_bf16 v[32:35], v[108:111], v[52:55], v[32:35]\n" \
  "v_mfma_f32_16x16x32_bf16 v[12:15], v[108:111], v[48:51], v[12:15]\n" \
  "s_waitcnt lgkmcnt(3)\n" \
  "ds_read_b128 v[108:111], v116 offset:13312\n" \
  "v_mfma_f32_16x16x32_bf16 v[16:19], v[112:115], v[48:51], v[16:19]\n" \
  "v_mfma_f32_16x16x32_bf16 v[36:39], v[112:115], v[52:55], v[36:39]\n" \
  "s_waitcnt vmcnt(8) lgkmcnt(3)\n" \
  "ds_read_b128 v[112:115], v116 offset:14336\n" \
  "v_mfma_f32_16x16x32_bf16 v[0:3], v[96:99], v[56:59], v[0:3]\n" \
  "s_add_u32 %[so], %[ab], 90112\n" \
  "buffer_load_dwordx4 v[40:43], v117, %[srd], %[so] offen sc1\n" \
  "v_mfma_f32_16x16x32_bf16 v[20:23], v[96:99], v[60:63], v[20:23]\n" \
  "s_waitcnt lgkmcnt(3)\n" \
  "ds_read_b128 v[96:99], v116 offset:20480\n" \
  "v_mfma_f32_16x16x32_bf16 v[24:27], v[100:103], v[60:63], v[24:27]\n" \
  "v_mfma_f32_16x16x32_bf16 v[4:7], v[100:103], v[56:59], v[4:7]\n" \
  "s_waitcnt lgkmcnt(3)\n" \
  "ds_read_b128 v[100:103], v116 offset:21504\n" \
  "v_mfma_f32_16x16x32_bf16 v[8:11], v[104:107], v[56:59], v[8:11]\n" \
  "buffer_load_dwordx4 v[44:47], v118, %[srd], %[so] offen sc1\n" \
  "v_mfma_f32_16x16x32_bf16 v[28:31], v[104:107], v[60:63], v[28:31]\n" \
  "s_waitcnt lgkmcnt(3)\n" \
  "ds_read_b128 v[104:107], v116 offset:22528\n" \
  "v_mfma_f32_16x16x32_bf16 v[32:35], v[108:111], v[60:63], v[32:35]\n" \
  "v_mfma_f32_16x16x32_bf16 v[12:15], v[108:111], v[56:59], v[12:15]\n" \
  "s_waitcnt lgkmcnt(3)\n" \
  "ds_read_b128 v[108:111], v116 offset:23552\n" \
  "v_mfma_f32_16x16x32_bf16 v[16:19], v[112:115], v[56:59], v[16:19]\n" \
  "v_mfma_f32_16x16x32_bf16 v[36:39], v[112:115], v[60:63], v[36:39]\n" \
  "s_waitcnt vmcnt(8) lgkmcnt(3)\n" \
  "ds_read_b128 v[112:115], v116 offset:24576\n" \
  "v_mfma_f32_16x16x32_bf16 v[0:3], v[96:99], v[64:67], v[0:3]\n" \
  "s_add_u32 %[so], %[ab], 104448\n" \
  "buffer_load_dwordx4 v[48:51], v117, %[srd], %[so] offen sc1\n" \
  "v_mfma_f32_16x16x32_bf16 v[20:23], v[96:99], v[68:71], v[20:23]\n" \
  "s_waitcnt lgkmcnt(3)\n" \
  "ds_read_b128 v[96:99], v116 offset:30720\n" \
  "v_mfma_f32_16x16x32_bf16 v[24:27], v[100:103], v[68:71], v[24:27]\n" \
  "v_mfma_f32_16x16x32_bf16 v[4:7], v[100:103], v[64:67], v[4:7]\n" \
  "s_waitcnt lgkmcnt(3)\n" \
  "ds_read_b128 v[100:103], v116 offset:31744\n" \
  "v_mfma_f32_16x16x32_bf16 v[8:11], v[104:107], v[64:67], v[8:11]\n" \
  "buffer_load_dwordx4 v[52:55], v118, %[srd], %[so] offen sc1\n" \
  "v_mfma_f32_16x16x32_bf16 v[28:31], v[104:107], v[68:71], v[28:31]\n" \
  "s_waitcnt lgkmcnt(3)\n" \
  "ds_read_b128 v[104:107], v116 offset:32768\n" \
  "v_mfma_f32_16x16x32_bf16 v[32:35], v[108:111], v[68:71], v[32:35]\n" \
  "v_mfma_f32_16x16x32_bf16 v[12:15], v[108:111], v[64:67], v[12:15]\n" \
  "s_waitcnt lgkmcnt(3)\n" \
  "ds_read_b128 v[108:111], v116 offset:33792\n" \
  "v_mfma_f32_16x16x32_bf16 v[16:19], v[112:115], v[64:67], v[16:19]\n" \
  "v_mfma_f32_16x16x32_bf16 v[36:39], v[112:115], v[68:71], v[36:39]\n" \
  "s_waitcnt vmcnt(8) lgkmcnt(3)\n" \
  "ds_read_b128 v[112:115], v116 offset:34816\n" \
  "v_mfma_f32_16x16x32_bf16 v[0:3], v[96:99], v[72:75], v[0:3]\n" \
  "s_add_u32 %[so], %[ab7], 12288\n" \
  "buffer_load_dwordx4 v[56:59], v117, %[srd], %[so] offen sc1\n" \
  "v_mfma_f32_16x16x32_bf16 v[20:23], v[96:99], v[76:79], v[20:23]\n" \
  "s_waitcnt lgkmcnt(3)\n" \
  "ds_read_b128 v[96:99], v116 offset:40960\n" \
  "v_mfma_f32_16x16x32_bf16 v[24:27], v[100:103], v[76:79], v[24:27]\n" \
  "v_mfma_f32_16x16x32_bf16 v[4:7], v[100:103], v[72:75], v[4:7]\n" \
  "s_waitcnt lgkmcnt(3)\n" \
  "ds_read_b128 v[100:103], v116 offset:41984\n" \
  "v_mfma_f32_16x16x32_bf16 v[8:11], v[104:107], v[72:75], v[8:11]\n" \
  "buffer_load_dwordx4 v[60:63], v118, %[srd], %[so] offen sc1\n" \
  "v_mfma_f32_16x16x32_bf16 v[28:31], v[104:107], v[76:79], v[28:31]\n" \
  "s_waitcnt lgkmcnt(3)\n" \
  "ds_read_b128 v[104:107], v116 offset:43008\n" \
  "v_mfma_f32_16x16x32_bf16 v[32:35], v[108:111], v[76:79], v[32:35]\n" \
  "v_mfma_f32_16x16x32_bf16 v[12:15], v[108:111], v[72:75], v[12:15]\n" \
  "s_waitcnt lgkmcnt(3)\n" \
  "ds_read_b128 v[108:111], v116 offset:44032\n" \
  "v_mfma_f32_16x16x32_bf16 v[16:19], v[112:115], v[72:75], v[16:19]\n" \
  "v_mfma_f32_16x16x32_bf16 v[36:39], v[112:115], v[76:79], v[36:39]\n" \
  "s_waitcnt vmcnt(8) lgkmcnt(3)\n" \
  "ds_read_b128 v[112:115], v116 offset:45056\n" \
  "v_mfma_f32_16x16x32_bf16 v[0:3], v[96:99], v[80:83], v[0:3]\n" \
  "s_add_u32 %[so], %[ab7], 40960\n" \
  "buffer_load_dwordx4 v[64:67], v117, %[srd], %[so] offen sc1\n" \
  "v_mfma_f32_16x16x32_bf16 v[20:23], v[96:99], v[84:87], v[20:23]\n" \
  "s_waitcnt lgkmcnt(3)\n" \
  "ds_read_b128 v[96:99], v116 offset:51200\n" \
  "v_mfma_f32_16x16x32_bf16 v[24:27], v[100:103], v[84:87], v[24:27]\n" \
  "v_mfma_f32_16x16x32_bf16 v[4:7], v[100:103], v[80:83], v[4:7]\n" \
  "s_waitcnt lgkmcnt(3)\n" \
  "ds_read_b128 v[100:103], v116 offset:52224\n" \
  "v_mfma_f32_16x16x32_bf16 v[8:11], v[104:107], v[80:83], v[8:11]\n" \
  "buffer_load_dwordx4 v[68:71], v118, %[srd], %[so] offen sc1\n" \
  "v_mfma_f32_16x16x32_bf16 v[28:31], v[104:107], v[84:87], v[28:31]\n" \
  "s_waitcnt lgkmcnt(3)\n" \
  "ds_read_b128 v[104:107], v116 offset:53248\n" \
  "v_mfma_f32_16x16x32_bf16 v[32:35], v[108:111], v[84:87], v[32:35]\n" \
  "v_mfma_f32_16x16x32_bf16 v[12:15], v[108:111], v[80:83], v[12:15]\n" \
  "s_waitcnt lgkmcnt(3)\n" \
  "ds_read_b128 v[108:111], v116 offset:54272\n" \
  "v_mfma_f32_16x16x32_bf16 v[16:19], v[112:115], v[80:83], v[16:19]\n" \
  "v_mfma_f32_16x16x32_bf16 v[36:39], v[112:115], v[84:87], v[36:39]\n" \
  "s_barrier\n" \
  "s_waitcnt vmcnt(8) lgkmcnt(3)\n" \
  "ds_read_b128 v[112:115], v116 offset:55296\n" \
  "v_mfma_f32_16x16x32_bf16 v[0:3], v[96:99], v[88:91], v[0:3]\n" \
  "s_add_u32 %[so], %[ab7], 69632\n" \
  "buffer_load_dwordx4 v[72:75], v117, %[srd], %[so] offen sc1\n" \
  "v_mfma_f32_16x16x32_bf16 v[20:23], v[96:99], v[92:95], v[20:23]\n" \
  "s_waitcnt lgkmcnt(3)\n" \
  "ds_read_b128 v[96:99], v128 offset:0\n" \
  "v_mfma_f32_16x16x32_bf16 v[24:27], v[100:103], v[92:95], v[24:27]\n" \
  "v_mfma_f32_16x16x32_bf16 v[4:7], v[100:103], v[88:91], v[4:7]\n" \
  "s_waitcnt lgkmcnt(3)\n" \
  "ds_read_b128 v[100:103], v128 offset:1024\n" \
  "v_mfma_f32_16x16x32_bf16 v[8:11], v[104:107], v[88:91], v[8:11]\n" \
  "buffer_load_dwordx4 v[76:79], v118, %[srd], %[so] offen sc1\n" \
  "v_mfma_f32_16x16x32_bf16 v[28:31], v[104:107], v[92:95], v[28:31]\n" \
  "s_waitcnt lgkmcnt(3)\n" \
  "ds_read_b128 v[104:107], v128 offset:2048\n" \
  "v_mfma_f32_16x16x32_bf16 v[32:35], v[108:111], v[92:95], v[32:35]\n" \
  "v_mfma_f32_16x16x32_bf16 v[12:15], v[108:111], v[88:91], v[12:15]\n" \
  "s_waitcnt lgkmcnt(3)\n" \
  "ds_read_b128 v[108:111], v128 offset:3072\n" \
  "v_mfma_f32_16x16x32_bf16 v[16:19], v[112:115], v[88:91], v[16:19]\n" \
  "v_mfma_f32_16x16x32_bf16 v[36:39], v[112:115], v[92:95], v[36:39]\n" \
  "s_waitcnt vmcnt(8) lgkmcnt(3)\n" \
  "ds_read_b128 v[112:115], v128 offset:4096\n" \
  "v_mfma_f32_16x16x32_bf16 v[0:3], v[96:99], v[40:43], v[0:3]\n" \
  "s_add_u32 %[so], %[ab5], 98304\n" \
  "buffer_load_dwordx4 v[80:83], v117, %[srd], %[so] offen sc1\n" \
  "v_mfma_f32_16x16x32_bf16 v[20:23], v[96:99], v[44:47], v[20:23]\n" \
  "s_waitcnt lgkmcnt(3)\n" \
  "ds_read_b128 v[96:99], v128 offset:10240\n" \
  "v_mfma_f32_16x16x32_bf16 v[24:27], v[100:103], v[44:47], v[24:27]\n" \
  "v_mfma_f32_16x16x32_bf16 v[4:7], v[100:103], v[40:43], v[4:7]\n" \
  "s_waitcnt lgkmcnt(3)\n" \
  "ds_read_b128 v[100:103], v128 offset:11264\n" \
  "v_mfma_f32_16x16x32_bf16 v[8:11], v[104:107], v[40:43], v[8:11]\n" \
  "buffer_load_dwordx4 v[84:87], v118, %[srd], %[so] offen sc1\n" \
  "v_mfma_f32_16x16x32_bf16 v[28:31], v[104:107], v[44:47], v[28:31]\n" \
  "s_waitcnt lgkmcnt(3)\n" \
  "ds_read_b128 v[104:107], v128 offset:12288\n" \
  "v_mfma_f32_16x16x32_bf16 v[32:35], v[108:111], v[44:47], v[32:35]\n" \
  "v_mfma_f32_16x16x32_bf16 v[12:15], v[108:111], v[40:43], v[12:15]\n" \
  "s_waitcnt lgkmcnt(3)\n" \
  "ds_read_b128 v[108:111], v128 offset:13312\n" \
  "v_mfma_f32_16x16x32_bf16 v[16:19], v[112:115], v[40:43], v[16:19]\n" \
  "v_mfma_f32_16x16x32_bf16 v[36:39], v[112:115], v[44:47], v[36:39]\n" \
  "s_waitcnt vmcnt(8) lgkmcnt(3)\n" \
  "ds_read_b128 v[112:115], v128 offset:14336\n" \
  "v_mfma_f32_16x16x32_bf16 v[0:3], v[96:99], v[48:51], v[0:3]\n" \
  "v_mfma_f32_16x16x32_bf16 v[20:23], v[96:99], v[52:55], v[20:23]\n" \
  "s_waitcnt lgkmcnt(3)\n" \
  "ds_read_b128 v[96:99], v128 offset:20480\n" \
  "v_mfma_f32_16x16x32_bf16 v[24:27], v[100:103], v[52:55], v[24:27]\n" \
  "v_mfma_f32_16x16x32_bf16 v[4:7], v[100:103], v[48:51], v[4:7]\n" \
  "s_waitcnt lgkmcnt(3)\n" \
  "ds_read_b128 v[100:103], v128 offset:21504\n" \
  "v_mfma_f32_16x16x32_bf16 v[8:11], v[104:107], v[48:51], v[8:11]\n" \
  "v_mfma_f32_16x16x32_bf16 v[28:31], v[104:107], v[52:55], v[28:31]\n" \
  "s_waitcnt lgkmcnt(3)\n" \
  "ds_read_b128 v[104:107], v128 offset:22528\n" \
  "v_mfma_f32_16x16x32_bf16 v[32:35], v[108:111], v[52:55], v[32:35]\n" \
  "v_mfma_f32_16x16x32_bf16 v[12:15], v[108:111], v[48:51], v[12:15]\n" \
  "s_waitcnt lgkmcnt(3)\n" \
  "ds_read_b128 v[108:111], v128 offset:23552\n" \
  "v_mfma_f32_16x16x32_bf16 v[16:19], v[112:115], v[48:51], v[16:19]\n" \
  "v_mfma_f32_16x16x32_bf16 v[36:39], v[112:115], v[52:55], v[36:39]\n" \
  "s_waitcnt vmcnt(6) lgkmcnt(3)\n" \
  "ds_read_b128 v[112:115], v128 offset:24576\n" \
  "v_mfma_f32_16x16x32_bf16 v[0:3], v[96:99], v[56:59], v[0:3]\n" \
  "v_mfma_f32_16x16x32_bf16 v[20:23], v[96:99], v[60:63], v[20:23]\n" \
  "s_waitcnt lgkmcnt(3)\n" \
  "ds_read_b128 v[96:99], v128 offset:30720\n" \
  "v_mfma_f32_16x16x32_bf16 v[24:27], v[100:103], v[60:63], v[24:27]\n" \
  "v_mfma_f32_16x16x32_bf16 v[4:7], v[100:103], v[56:59], v[4:7]\n" \
  "s_waitcnt lgkmcnt(3)\n" \
  "ds_read_b128 v[100:103], v128 offset:31744\n" \
  "v_mfma_f32_16x16x32_bf16 v[8:11], v[104:107], v[56:59], v[8:11]\n" \
  "v_mfma_f32_16x16x32_bf16 v[28:31], v[104:107], v[60:63], v[28:31]\n" \
  "s_waitcnt lgkmcnt(3)\n" \
  "ds_read_b128 v[104:107], v128 offset:32768\n" \
  "v_mfma_f32_16x16x32_bf16 v[32:35], v[108:111], v[60:63], v[32:35]\n" \
  "v_mfma_f32_16x16x32_bf16 v[12:15], v[108:111], v[56:59], v[12:15]\n" \
  "s_waitcnt lgkmcnt(3)\n" \
  "ds_read_b128 v[108:111], v128 offset:33792\n" \
  "v_mfma_f32_16x16x32_bf16 v[16:19], v[112:115], v[56:59], v[16:19]\n" \
  "v_mfma_f32_16x16x32_bf16 v[36:39], v[112:115], v[60:63], v[36:39]\n" \
  "s_waitcnt vmcnt(4) lgkmcnt(3)\n" \
  "ds_read_b128 v[112:115], v128 offset:34816\n" \
  "v_mfma_f32_16x16x32_bf16 v[0:3], v[96:99], v[64:67], v[0:3]\n" \
  "v_mfma_f32_16x16x32_bf16 v[20:23], v[96:99], v[68:71], v[20:23]\n" \
  "s_waitcnt lgkmcnt(3)\n" \
  "ds_read_b128 v[96:99], v128 offset:40960\n" \
  "v_mfma_f32_16x16x32_bf16 v[24:27], v[100:103], v[68:71], v[24:27]\n" \
  "v_mfma_f32_16x16x32_bf16 v[4:7], v[100:103], v[64:67], v[4:7]\n" \
  "s_waitcnt lgkmcnt(3)\n" \
  "ds_read_b128 v[100:103], v128 offset:41984\n" \
  "v_mfma_f32_16x16x32_bf16 v[8:11], v[104:107], v[64:67], v[8:11]\n" \
  "v_mfma_f32_16x16x32_bf16 v[28:31], v[104:107], v[68:71], v[28:31]\n" \
  "s_waitcnt lgkmcnt(3)\n" \
  "ds_read_b128 v[104:107], v128 offset:43008\n" \
  "v_mfma_f32_16x16x32_bf16 v[32:35], v[108:111], v[68:71], v[32:35]\n" \
  "v_mfma_f32_16x16x32_bf16 v[12:15], v[108:111], v[64:67], v[12:15]\n" \
  "s_waitcnt lgkmcnt(3)\n" \
  "ds_read_b128 v[108:111], v128 offset:44032\n" \
  "v_mfma_f32_16x16x32_bf16 v[16:19], v[112:115], v[64:67], v[16:19]\n" \
  "v_mfma_f32_16x16x32_bf16 v[36:39], v[112:115], v[68:71], v[36:39]\n" \
  "s_waitcnt vmcnt(2) lgkmcnt(3)\n" \
  "ds_read_b128 v[112:115], v128 offset:45056\n" \
  "v_mfma_f32_16x16x32_bf16 v[0:3], v[96:99], v[72:75], v[0:3]\n" \
  "v_mfma_f32_16x16x32_bf16 v[20:23], v[96:99], v[76:79], v[20:23]\n" \
  "s_waitcnt lgkmcnt(3)\n" \
  "ds_read_b128 v[96:99], v128 offset:51200\n" \
  "v_mfma_f32_16x16x32_bf16 v[24:27], v[100:103], v[76:79], v[24:27]\n" \
  "v_mfma_f32_16x16x32_bf16 v[4:7], v[100:103], v[72:75], v[4:7]\n" \
  "s_waitcnt lgkmcnt(3)\n" \
  "ds_read_b128 v[100:103], v128 offset:52224\n" \
  "v_mfma_f32_16x16x32_bf16 v[8:11], v[104:107], v[72:75], v[8:11]\n" \
  "v_mfma_f32_16x16x32_bf16 v[28:31], v[104:107], v[76:79], v[28:31]\n" \
  "s_waitcnt lgkmcnt(3)\n" \
  "ds_read_b128 v[104:107], v128 offset:53248\n" \
  "v_mfma_f32_16x16x32_bf16 v[32:35], v[108:111], v[76:79], v[32:35]\n" \
  "v_mfma_f32_16x16x32_bf16 v[12:15], v[108:111], v[72:75], v[12:15]\n" \
  "s_waitcnt lgkmcnt(3)\n" \
  "ds_read_b128 v[108:111], v128 offset:54272\n" \
  "v_mfma_f32_16x16x32_bf16 v[16:19], v[112:115], v[72:75], v[16:19]\n" \
  "v_mfma_f32_16x16x32_bf16 v[36:39], v[112:115], v[76:79], v[36:39]\n" \
  "s_waitcnt vmcnt(0) lgkmcnt(3)\n" \
  "ds_read_b128 v[112:115], v128 offset:55296\n" \
  "v_mfma_f32_16x16x32_bf16 v[20:23], v[96:99], v[84:87], v[20:23]\n" \
  "s_waitcnt lgkmcnt(3)\n" \
  "v_mfma_f32_16x16x32_bf16 v[24:27], v[100:103], v[84:87], v[24:27]\n" \
  "s_waitcnt lgkmcnt(2)\n" \
  "v_mfma_f32_16x16x32_bf16 v[28:31], v[104:107], v[84:87], v[28:31]\n" \
  "s_waitcnt lgkmcnt(1)\n" \
  "v_mfma_f32_16x16x32_bf16 v[32:35], v[108:111], v[84:87], v[32:35]\n" \
  "s_waitcnt lgkmcnt(0)\n" \
  "v_mfma_f32_16x16x32_bf16 v[36:39], v[112:115], v[84:87], v[36:39]\n" \
  "v_mfma_f32_16x16x32_bf16 v[0:3], v[96:99], v[80:83], v[0:3]\n" \
  "v_mfma_f32_16x16x32_bf16 v[4:7], v[100:103], v[80:83], v[4:7]\n" \
  "v_mfma_f32_16x16x32_bf16 v[8:11], v[104:107], v[80:83], v[8:11]\n" \
  "v_mfma_f32_16x16x32_bf16 v[12:15], v[108:111], v[80:83], v[12:15]\n" \
  "v_mfma_f32_16x16x32_bf16 v[16:19], v[112:115], v[80:83], v[16:19]\n" \
  "ds_write_b128 v120, v[20:23] offset:0\n" \
  "ds_write_b128 v120, v[24:27] offset:1024\n" \
  "ds_write_b128 v120, v[28:31] offset:2048\n" \
  "ds_write_b128 v120, v[32:35] offset:3072\n" \
  "ds_write_b128 v120, v[36:39] offset:4096\n" \
  "s_waitcnt lgkmcnt(0)\n" \
  "s_barrier\n" \
  "ds_read_b128 v[40:43], v121 offset:0\n" \
  "ds_read_b128 v[44:47], v121 offset:1024\n" \
  "ds_read_b128 v[48:51], v121 offset:2048\n" \
  "ds_read_b128 v[52:55], v121 offset:3072\n" \
  "ds_read_b128 v[56:59], v121 offset:4096\n" \
  "s_waitcnt lgkmcnt(4)\n" \
  "v_pk_add_f32 v[0:1], v[0:1], v[40:41]\n" \
  "v_pk_add_f32 v[2:3], v[2:3], v[42:43]\n" \
  "s_waitcnt lgkmcnt(3)\n" \
  "v_pk_add_f32 v[4:5], v[4:5], v[44:45]\n" \
  "v_pk_add_f32 v[6:7], v[6:7], v[46:47]\n" \
  "v_cvt_pk_bf16_f32 v132, v0, v1\n" \
  "v_cvt_pk_bf16_f32 v133, v2, v3\n" \
  "v_cvt_pk_bf16_f32 v134, v4, v5\n" \
  "v_cvt_pk_bf16_f32 v135, v6, v7\n" \
  "s_nop 0\n" \
  "v_permlane16_swap_b32 v132, v134\n" \
  "v_permlane16_swap_b32 v133, v135\n" \
  "global_store_dwordx4 v122, v[132:135], %[out] offset:0\n" \
  "s_waitcnt lgkmcnt(2)\n" \
  "v_pk_add_f32 v[8:9], v[8:9], v[48:49]\n" \
  "v_pk_add_f32 v[10:11], v[10:11], v[50:51]\n" \
  "s_waitcnt lgkmcnt(1)\n" \
  "v_pk_add_f32 v[12:13], v[12:13], v[52:53]\n" \
  "v_pk_add_f32 v[14:15], v[14:15], v[54:55]\n" \
  "v_cvt_pk_bf16_f32 v136, v8, v9\n" \
  "v_cvt_pk_bf16_f32 v137, v10, v11\n" \
  "v_cvt_pk_bf16_f32 v138, v12, v13\n" \
  "v_cvt_pk_bf16_f32 v139, v14, v15\n" \
  "s_nop 0\n" \
  "v_permlane16_swap_b32 v136, v138\n" \
  "v_permlane16_swap_b32 v137, v139\n" \
  "global_store_dwordx4 v122, v[136:139], %[out] offset:64\n" \
  "s_waitcnt lgkmcnt(0)\n" \
  "v_pk_add_f32 v[16:17], v[16:17], v[56:57]\n" \
  "v_pk_add_f32 v[18:19], v[18:19], v[58:59]\n" \
  "v_cvt_pk_bf16_f32 v140, v16, v17\n" \
  "v_cvt_pk_bf16_f32 v141, v18, v19\n" \
  "s_nop 0\n" \
  "v_permlane16_swap_b32 v140, v142\n" \
  "v_permlane16_swap_b32 v141, v143\n" \
  "s_mov_b32 exec_lo, 0xffff\n" \
  "s_mov_b32 exec_hi, 0xffff\n" \
  "global_store_dwordx4 v122, v[140:143], %[out] offset:128\n" \
  "s_mov_b64 exec, -1\n" \
  "s_branch 5f\n" \
  "4:\n" \
  "s_mov_b32 %[cnt], 0x40000\n" \
  "s_mov_b64 exec, 1\n" \
  "6:\n" \
  "global_atomic_add v131, v119, v130, %[fl] offset:1024 sc0 sc1\n" \
  "s_waitcnt vmcnt(0)\n" \
  "v_cmp_ne_u32 vcc, 16, v131\n" \
  "s_cbranch_vccz 7f\n" \
  "s_sub_u32 %[cnt], %[cnt], 1\n" \
  "s_cbranch_scc0 6b\n" \
  "7:\n" \
  ".Lk_pad5:\n" \
  "s_cbranch_execz .Lk_ret5\n" \
  "s_mov_b64 exec, -1\n" \
  "s_branch 3b\n" \
  "5:\n"
#define P3_CLOB "v20", "v21", "v22", "v23", "v24", "v25", "v26", "v27", "v28", "v29", "v30", "v31", "v32", "v33", "v34", "v35", "v36", "v37", "v38", "v39", "v40", "v41", "v42", "v43", "v44", "v45", "v46", "v47", "v48", "v49", "v50", "v51", "v52", "v53", "v54", "v55", "v56", "v57", "v58", "v59", "v60", "v61", "v62", "v63", "v64", "v65", "v66", "v67", "v68", "v69", "v70", "v71", "v72", "v73", "v74", "v75", "v76", "v77", "v78", "v79", "v80", "v81", "v82", "v83", "v84", "v85", "v86", "v87", "v88", "v89", "v90", "v91", "v92", "v93", "v94", "v95", "v96", "v97", "v98", "v99", "v100", "v101", "v102", "v103", "v104", "v105", "v106", "v107", "v108", "v109", "v110", "v111", "v112", "v113", "v114", "v115", "v128", "v129", "v130", "v131", "v132", "v133", "v134", "v135", "v136", "v137", "v138", "v139", "v140", "v141", "v142", "v143", "v144", "v145", "v146", "v147", "v148", "v149", "v150", "v151", "v152", "v153", "v154", "v155", "v156", "v157", "v158", "v159"
#define P5_CLOB "v20", "v21", "v22", "v23", "v24", "v25", "v26", "v27", "v28", "v29", "v30", "v31", "v32", "v33", "v34", "v35", "v36", "v37", "v38", "v39", "v40", "v41", "v42", "v43", "v44", "v45", "v46", "v47", "v48", "v49", "v50", "v51", "v52", "v53", "v54", "v55", "v56", "v57", "v58", "v59", "v60", "v61", "v62", "v63", "v64", "v65", "v66", "v67", "v68", "v69", "v70", "v71", "v72", "v73", "v74", "v75", "v76", "v77", "v78", "v79", "v80", "v81", "v82", "v83", "v84", "v85", "v86", "v87", "v88", "v89", "v90", "v91", "v92", "v93", "v94", "v95", "v96", "v97", "v98", "v99", "v100", "v101", "v102", "v103", "v104", "v105", "v106", "v107", "v108", "v109", "v110", "v111", "v112", "v113", "v114", "v115", "v128", "v129", "v130", "v131", "v132", "v133", "v134", "v135", "v136", "v137", "v138", "v139", "v140", "v141", "v142", "v143", "v144", "v145", "v146", "v147", "v148", "v149", "v150", "v151", "v152", "v153", "v154", "v155", "v156", "v157", "v158", "v159", "v160", "v161", "v162", "v163", "v164", "v165", "v166", "v167", "v168", "v169", "v170", "v171", "v172", "v173", "v174", "v175", "v176", "v177", "v178", "v179", "v180", "v181", "v182", "v183", "v184", "v185", "v186", "v187", "v188", "v189", "v190", "v191", "v192", "v193", "v194", "v195", "v196", "v197", "v198", "v199", "v200", "v201", "v202", "v203", "v204", "v205", "v206", "v207", "v208", "v209", "v210", "v211", "v212", "v213", "v214", "v215", "v216", "v217", "v218", "v219", "v220", "v221", "v222", "v223", "v224", "v225", "v226", "v227", "v228", "v229", "v230", "v231", "v232", "v233", "v234", "v235", "v236", "v237", "v238", "v239", "v240", "v241", "v242", "v243", "v244", "v245", "v246", "v247", "v248", "v249", "v250", "v251"
#define P4_ASM \
  "v_add_u32 v112, 49152, v104\n" \
  "v_add_u32 v113, 106496, v104\n" \
  "s_mov_b32 %[so], %[ab]\n" \
  "v_add_u32 v114, %[da], v104\n" \
  "v_add_u32 v115, %[da0], v104\n" \
  "ds_write_b128 v115, v[164:167] offset:0\n" \
  "v_mov_b64 v[20:21], 0\n" \
  "v_mov_b64 v[22:23], 0\n" \
  "v_mov_b64 v[24:25], 0\n" \
  "ds_write_b128 v114, v[168:171] offset:1024\n" \
  "v_mov_b64 v[26:27], 0\n" \
  "v_mov_b64 v[28:29], 0\n" \
  "v_mov_b64 v[30:31], 0\n" \
  "ds_write_b128 v114, v[172:175] offset:2048\n" \
  "v_mov_b64 v[32:33], 0\n" \
  "v_mov_b64 v[34:35], 0\n" \
  "v_mov_b64 v[36:37], 0\n" \
  "ds_write_b128 v114, v[176:179] offset:3072\n" \
  "v_mov_b64 v[38:39], 0\n" \
  "v_mov_b64 v[40:41], 0\n" \
  "v_mov_b64 v[42:43], 0\n" \
  "ds_write_b128 v114, v[180:183] offset:4096\n" \
  "v_mov_b64 v[44:45], 0\n" \
  "v_mov_b64 v[46:47], 0\n" \
  "v_mov_b64 v[48:49], 0\n" \
  "ds_write_b128 v114, v[184:187] offset:5120\n" \
  "v_mov_b64 v[50:51], 0\n" \
  "v_mov_b64 v[52:53], 0\n" \
  "v_mov_b64 v[54:55], 0\n" \
  "ds_write_b128 v114, v[188:191] offset:6144\n" \
  "v_mov_b64 v[56:57], 0\n" \
  "v_mov_b64 v[58:59], 0\n" \
  "v_mov_b64 v[60:61], 0\n" \
  "ds_write_b128 v115, v[192:195] offset:28672\n" \
  "v_mov_b64 v[62:63], 0\n" \
  "v_mov_b64 v[64:65], 0\n" \
  "v_mov_b64 v[66:67], 0\n" \
  "v_mov_b64 v[68:69], 0\n" \
  "v_mov_b64 v[70:71], 0\n" \
  "v_mov_b64 v[72:73], 0\n" \
  "v_mov_b64 v[74:75], 0\n" \
  "s_mov_b64 exec, 255\n" \
  "s_waitcnt vmcnt(0)\n" \
  "buffer_atomic_add v111, v110, %[srd], 0 offen sc1\n" \
  "s_mov_b64 exec, -1\n" \
  "v_mov_b32 v160, 0\n" \
  "s_mov_b32 %[cnt], 0x40000\n" \
  "s_mov_b64 exec, 1\n" \
  "global_atomic_add v161, v106, v160, %[fl] sc0 sc1\n" \
  "s_mov_b64 exec, -1\n" \
  "ds_write_b128 v114, v[196:199] offset:29696\n" \
  "ds_write_b128 v114, v[200:203] offset:30720\n" \
  "ds_write_b128 v114, v[204:207] offset:31744\n" \
  "ds_write_b128 v114, v[208:211] offset:32768\n" \
  "ds_write_b128 v114, v[212:215] offset:33792\n" \
  "ds_write_b128 v114, v[216:219] offset:34816\n" \
  "ds_write_b128 v115, v[220:223] offset:57344\n" \
  "s_mov_b64 exec, 1\n" \
  "global_atomic_add v162, v106, v160, %[fl] sc0 sc1\n" \
  "s_mov_b64 exec, -1\n" \
  "ds_write_b128 v114, v[224:227] offset:58368\n" \
  "ds_write_b128 v114, v[228:231] offset:59392\n" \
  "ds_write_b128 v114, v[232:235] offset:60416\n" \
  "ds_write_b128 v114, v[236:239] offset:61440\n" \
  "ds_write_b128 v114, v[240:243] offset:62464\n" \
  "ds_write_b128 v114, v[244:247] offset:63488\n" \
  "s_mov_b64 exec, 1\n" \
  "9:\n" \
  "s_waitcnt vmcnt(1)\n" \
  "v_cmp_ne_u32 vcc, 16, v161\n" \
  "s_cbranch_vccz 8f\n" \
  "global_atomic_add v161, v106, v160, %[fl] sc0 sc1\n" \
  "s_waitcnt vmcnt(1)\n" \
  "v_cmp_ne_u32 vcc, 16, v162\n" \
  "s_cbranch_vccz 8f\n" \
  "global_atomic_add v162, v106, v160, %[fl] sc0 sc1\n" \
  "s_sub_u32 %[cnt], %[cnt], 1\n" \
  "s_cbranch_scc0 9b\n" \
  "8:\n" \
  "s_mov_b64 exec, -1\n" \
  "s_waitcnt lgkmcnt(0)\n" \
  "buffer_load_dwordx4 v[164:167], v105, %[srd], %[so] offen offset:0 sc1\n" \
  "buffer_load_dwordx4 v[168:171], v105, %[srd], %[so] offen offset:256 sc1\n" \
  "buffer_load_dwordx4 v[172:175], v105, %[srd], %[so] offen offset:2048 sc1\n" \
  "buffer_load_dwordx4 v[176:179], v105, %[srd], %[so] offen offset:2304 sc1\n" \
  "s_add_u32 %[so], %[so], 4096\n" \
  "s_barrier\n" \
  "ds_read_b128 v[76:79], v104 offset:0\n" \
  "ds_read_b128 v[80:83], v104 offset:1024\n" \
  "ds_read_b128 v[84:87], v104 offset:2048\n" \
  "ds_read_b128 v[88:91], v104 offset:3072\n" \
  "ds_read_b128 v[92:95], v104 offset:4096\n" \
  "ds_read_b128 v[96:99], v104 offset:5120\n" \
  "ds_read_b128 v[100:103], v104 offset:6144\n" \
  "buffer_load_dwordx4 v[180:183], v105, %[srd], %[so] offen offset:0 sc1\n" \
  "buffer_load_dwordx4 v[184:187], v105, %[srd], %[so] offen offset:256 sc1\n" \
  "buffer_load_dwordx4 v[188:191], v105, %[srd], %[so] offen offset:2048 sc1\n" \
  "buffer_load_dwordx4 v[192:195], v105, %[srd], %[so] offen offset:2304 sc1\n" \
  "s_add_u32 %[so], %[so], 4096\n" \
  "buffer_load_dwordx2 v[116:117], v107, %[srd], %[st0] offen offset:0 sc1\n" \
  "buffer_load_dwordx2 v[118:119], v107, %[srd], %[st1] offen offset:0 sc1\n" \
  "buffer_load_dwordx2 v[120:121], v107, %[srd], %[st0] offen offset:128 sc1\n" \
  "buffer_load_dwordx2 v[122:123], v107, %[srd], %[st1] offen offset:128 sc1\n" \
  "s_waitcnt vmcnt(10) lgkmcnt(5)\n" \
  "v_mfma_f32_16x16x32_bf16 v[20:23], v[76:79], v[164:167], v[20:23]\n" \
  "buffer_load_dwordx4 v[196:199], v105, %[srd], %[so] offen offset:0 sc1\n" \
  "v_mfma_f32_16x16x32_bf16 v[24:27], v[76:79], v[168:171], v[24:27]\n" \
  "ds_read_b128 v[76:79], v104 offset:7168\n" \
  "v_mfma_f32_16x16x32_bf16 v[32:35], v[80:83], v[168:171], v[32:35]\n" \
  "v_mfma_f32_16x16x32_bf16 v[28:31], v[80:83], v[164:167], v[28:31]\n" \
  "s_waitcnt lgkmcnt(4)\n" \
  "ds_read_b128 v[80:83], v104 offset:8192\n" \
  "v_mfma_f32_16x16x32_bf16 v[36:39], v[84:87], v[164:167], v[36:39]\n" \
  "buffer_load_dwordx4 v[200:203], v105, %[srd], %[so] offen offset:256 sc1\n" \
  "v_mfma_f32_16x16x32_bf16 v[40:43], v[84:87], v[168:171], v[40:43]\n" \
  "ds_read_b128 v[84:87], v104 offset:9216\n" \
  "v_mfma_f32_16x16x32_bf16 v[48:51], v[88:91], v[168:171], v[48:51]\n" \
  "v_mfma_f32_16x16x32_bf16 v[44:47], v[88:91], v[164:167], v[44:47]\n" \
  "s_waitcnt lgkmcnt(4)\n" \
  "ds_read_b128 v[88:91], v104 offset:10240\n" \
  "v_mfma_f32_16x16x32_bf16 v[52:55], v[92:95], v[164:167], v[52:55]\n" \
  "buffer_load_dwordx4 v[204:207], v105, %[srd], %[so] offen offset:2048 sc1\n" \
  "v_mfma_f32_16x16x32_bf16 v[56:59], v[92:95], v[168:171], v[56:59]\n" \
  "ds_read_b128 v[92:95], v104 offset:11264\n" \
  "v_mfma_f32_16x16x32_bf16 v[64:67], v[96:99], v[168:171], v[64:67]\n" \
  "v_mfma_f32_16x16x32_bf16 v[60:63], v[96:99], v[164:167], v[60:63]\n" \
  "s_waitcnt lgkmcnt(4)\n" \
  "ds_read_b128 v[96:99], v104 offset:12288\n" \
  "v_mfma_f32_16x16x32_bf16 v[68:71], v[100:103], v[164:167], v[68:71]\n" \
  "v_mfma_f32_16x16x32_bf16 v[72:75], v[100:103], v[168:171], v[72:75]\n" \
  "s_waitcnt vmcnt(11)\n" \
  "ds_read_b128 v[100:103], v104 offset:13312\n" \
  "v_mfma_f32_16x16x32_bf16 v[20:23], v[76:79], v[172:175], v[20:23]\n" \
  "buffer_load_dwordx4 v[208:211], v105, %[srd], %[so] offen offset:2304 sc1\n" \
  "s_add_u32 %[so], %[so], 4096\n" \
  "v_mfma_f32_16x16x32_bf16 v[24:27], v[76:79], v[176:179], v[24:27]\n" \
  "s_waitcnt lgkmcnt(4)\n" \
  "ds_read_b128 v[76:79], v104 offset:14336\n" \
  "v_mfma_f32_16x16x32_bf16 v[32:35], v[80:83], v[176:179], v[32:35]\n" \
  "v_mfma_f32_16x16x32_bf16 v[28:31], v[80:83], v[172:175], v[28:31]\n" \
  "ds_read_b128 v[80:83], v104 offset:15360\n" \
  "v_mfma_f32_16x16x32_bf16 v[36:39], v[84:87], v[172:175], v[36:39]\n" \
  "buffer_load_dwordx4 v[212:215], v105, %[srd], %[so] offen offset:0 sc1\n" \
  "v_mfma_f32_16x16x32_bf16 v[40:43], v[84:87], v[176:179], v[40:43]\n" \
  "s_waitcnt lgkmcnt(4)\n" \
  "ds_read_b128 v[84:87], v104 offset:16384\n" \
  "v_mfma_f32_16x16x32_bf16 v[48:51], v[88:91], v[176:179], v[48:51]\n" \
  "v_mfma_f32_16x16x32_bf16 v[44:47], v[88:91], v[172:175], v[44:47]\n" \
  "ds_read_b128 v[88:91], v104 offset:17408\n" \
  "v_mfma_f32_16x16x32_bf16 v[52:55], v[92:95], v[172:175], v[52:55]\n" \
  "buffer_load_dwordx4 v[216:219], v105, %[srd], %[so] offen offset:256 sc1\n" \
  "v_mfma_f32_16x16x32_bf16 v[56:59], v[92:95], v[176:179], v[56:59]\n" \
  "s_waitcnt lgkmcnt(4)\n" \
  "ds_read_b128 v[92:95], v104 offset:18432\n" \
  "v_mfma_f32_16x16x32_bf16 v[64:67], v[96:99], v[176:179], v[64:67]\n" \
  "v_mfma_f32_16x16x32_bf16 v[60:63], v[96:99], v[172:175], v[60:63]\n" \
  "ds_read_b128 v[96:99], v104 offset:19456\n" \
  "v_mfma_f32_16x16x32_bf16 v[68:71], v[100:103], v[172:175], v[68:71]\n" \
  "v_mfma_f32_16x16x32_bf16 v[72:75], v[100:103], v[176:179], v[72:75]\n" \
  "s_waitcnt vmcnt(12) lgkmcnt(4)\n" \
  "ds_read_b128 v[100:103], v104 offset:20480\n" \
  "v_mfma_f32_16x16x32_bf16 v[20:23], v[76:79], v[180:183], v[20:23]\n" \
  "buffer_load_dwordx4 v[220:223], v105, %[srd], %[so] offen offset:2048 sc1\n" \
  "v_mfma_f32_16x16x32_bf16 v[24:27], v[76:79], v[184:187], v[24:27]\n" \
  "ds_read_b128 v[76:79], v104 offset:21504\n" \
  "v_mfma_f32_16x16x32_bf16 v[32:35], v[80:83], v[184:187], v[32:35]\n" \
  "v_mfma_f32_16x16x32_bf16 v[28:31], v[80:83], v[180:183], v[28:31]\n" \
  "s_waitcnt lgkmcnt(4)\n" \
  "ds_read_b128 v[80:83], v104 offset:22528\n" \
  "v_mfma_f32_16x16x32_bf16 v[36:39], v[84:87], v[180:183], v[36:39]\n" \
  "buffer_load_dwordx4 v[224:227], v105, %[srd], %[so] offen offset:2304 sc1\n" \
  "s_add_u32 %[so], %[so], 4096\n" \
  "v_mfma_f32_16x16x32_bf16 v[40:43], v[84:87], v[184:187], v[40:43]\n" \
  "ds_read_b128 v[84:87], v104 offset:23552\n" \
  "v_mfma_f32_16x16x32_bf16 v[48:51], v[88:91], v[184:187], v[48:51]\n" \
  "v_mfma_f32_16x16x32_bf16 v[44:47], v[88:91], v[180:183], v[44:47]\n" \
  "s_waitcnt lgkmcnt(4)\n" \
  "ds_read_b128 v[88:91], v104 offset:24576\n" \
  "v_mfma_f32_16x16x32_bf16 v[52:55], v[92:95], v[180:183], v[52:55]\n" \
  "v_mfma_f32_16x16x32_bf16 v[56:59], v[92:95], v[184:187], v[56:59]\n" \
  "ds_read_b128 v[92:95], v104 offset:25600\n" \
  "v_mfma_f32_16x16x32_bf16 v[64:67], v[96:99], v[184:187], v[64:67]\n" \
  "buffer_load_dwordx4 v[228:231], v105, %[srd], %[so] offen offset:0 sc1\n" \
  "v_mfma_f32_16x16x32_bf16 v[60:63], v[96:99], v[180:183], v[60:63]\n" \
  "s_waitcnt lgkmcnt(4)\n" \
  "ds_read_b128 v[96:99], v104 offset:26624\n" \
  "v_mfma_f32_16x16x32_bf16 v[68:71], v[100:103], v[180:183], v[68:71]\n" \
  "v_mfma_f32_16x16x32_bf16 v[72:75], v[100:103], v[184:187], v[72:75]\n" \
  "s_waitcnt vmcnt(13)\n" \
  "ds_read_b128 v[100:103], v104 offset:27648\n" \
  "v_mfma_f32_16x16x32_bf16 v[20:23], v[76:79], v[188:191], v[20:23]\n" \
  "buffer_load_dwordx4 v[232:235], v105, %[srd], %[so] offen offset:256 sc1\n" \
  "s_waitcnt vmcnt(10)\n" \
  "v_mfma_f32_16x16x32_bf16 v[24:27], v[76:79], v[192:195], v[24:27]\n" \
  "s_waitcnt lgkmcnt(4)\n" \
  "ds_read_b128 v[76:79], v112 offset:0\n" \
  "v_mfma_f32_16x16x32_bf16 v[32:35], v[80:83], v[192:195], v[32:35]\n" \
  "v_add_f32 v116, v116, v118\n" \
  "v_mfma_f32_16x16x32_bf16 v[28:31], v[80:83], v[188:191], v[28:31]\n" \
  "ds_read_b128 v[80:83], v112 offset:1024\n" \
  "v_mfma_f32_16x16x32_bf16 v[36:39], v[84:87], v[188:191], v[36:39]\n" \
  "buffer_load_dwordx4 v[236:239], v105, %[srd], %[so] offen offset:2048 sc1\n" \
  "v_add_f32 v120, v120, v122\n" \
  "v_mfma_f32_16x16x32_bf16 v[40:43], v[84:87], v[192:195], v[40:43]\n" \
  "s_waitcnt lgkmcnt(4)\n" \
  "ds_read_b128 v[84:87], v112 offset:2048\n" \
  "v_mfma_f32_16x16x32_bf16 v[48:51], v[88:91], v[192:195], v[48:51]\n" \
  "v_add_f32 v117, v117, v119\n" \
  "v_mfma_f32_16x16x32_bf16 v[44:47], v[88:91], v[188:191], v[44:47]\n" \
  "ds_read_b128 v[88:91], v112 offset:3072\n" \
  "v_mfma_f32_16x16x32_bf16 v[52:55], v[92:95], v[188:191], v[52:55]\n" \
  "v_add_f32 v121, v121, v123\n" \
  "v_mfma_f32_16x16x32_bf16 v[56:59], v[92:95], v[192:195], v[56:59]\n" \
  "s_waitcnt lgkmcnt(4)\n" \
  "ds_read_b128 v[92:95], v112 offset:4096\n" \
  "v_mfma_f32_16x16x32_bf16 v[64:67], v[96:99], v[192:195], v[64:67]\n" \
  "buffer_load_dwordx4 v[240:243], v105, %[srd], %[so] offen offset:2304 sc1\n" \
  "s_add_u32 %[so], %[so], 4096\n" \
  "v_permlane16_swap_b32 v116, v120\n" \
  "v_mfma_f32_16x16x32_bf16 v[60:63], v[96:99], v[188:191], v[60:63]\n" \
  "ds_read_b128 v[96:99], v112 offset:5120\n" \
  "v_mfma_f32_16x16x32_bf16 v[68:71], v[100:103], v[188:191], v[68:71]\n" \
  "v_permlane16_swap_b32 v117, v121\n" \
  "v_mfma_f32_16x16x32_bf16 v[72:75], v[100:103], v[192:195], v[72:75]\n" \
  "s_waitcnt vmcnt(10) lgkmcnt(4)\n" \
  "ds_read_b128 v[100:103], v112 offset:6144\n" \
  "v_mfma_f32_16x16x32_bf16 v[20:23], v[76:79], v[196:199], v[20:23]\n" \
  "buffer_load_dwordx4 v[164:167], v105, %[srd], %[so] offen offset:0 sc1\n" \
  "v_add_f32 v116, v116, v120\n" \
  "v_mfma_f32_16x16x32_bf16 v[24:27], v[76:79], v[200:203], v[24:27]\n" \
  "ds_read_b128 v[76:79], v112 offset:7168\n" \
  "v_mfma_f32_16x16x32_bf16 v[32:35], v[80:83], v[200:203], v[32:35]\n" \
  "v_add_f32 v117, v117, v121\n" \
  "v_mfma_f32_16x16x32_bf16 v[28:31], v[80:83], v[196:199], v[28:31]\n" \
  "s_waitcnt lgkmcnt(4)\n" \
  "ds_read_b128 v[80:83], v112 offset:8192\n" \
  "v_mfma_f32_16x16x32_bf16 v[36:39], v[84:87], v[196:199], v[36:39]\n" \
  "buffer_load_dwordx4 v[168:171], v105, %[srd], %[so] offen offset:256 sc1\n" \
  "v_permlane32_swap_b32 v116, v117\n" \
  "v_mfma_f32_16x16x32_bf16 v[40:43], v[84:87], v[200:203], v[40:43]\n" \
  "ds_read_b128 v[84:87], v112 offset:9216\n" \
  "v_mfma_f32_16x16x32_bf16 v[48:51], v[88:91], v[200:203], v[48:51]\n" \
  "v_add_f32 v116, v116, v117\n" \
  "v_mfma_f32_16x16x32_bf16 v[44:47], v[88:91], v[196:199], v[44:47]\n" \
  "s_waitcnt lgkmcnt(4)\n" \
  "ds_read_b128 v[88:91], v112 offset:10240\n" \
  "v_mfma_f32_16x16x32_bf16 v[52:55], v[92:95], v[196:199], v[52:55]\n" \
  "v_mov_b32 v117, v116\n" \
  "v_mfma_f32_16x16x32_bf16 v[56:59], v[92:95], v[200:203], v[56:59]\n" \
  "ds_read_b128 v[92:95], v112 offset:11264\n" \
  "v_mfma_f32_16x16x32_bf16 v[64:67], v[96:99], v[200:203], v[64:67]\n" \
  "buffer_load_dwordx4 v[172:175], v105, %[srd], %[so] offen offset:2048 sc1\n" \
  "v_permlane32_swap_b32 v116, v117\n" \
  "v_mfma_f32_16x16x32_bf16 v[60:63], v[96:99], v[196:199], v[60:63]\n" \
  "s_waitcnt lgkmcnt(4)\n" \
  "ds_read_b128 v[96:99], v112 offset:12288\n" \
  "v_mfma_f32_16x16x32_bf16 v[68:71], v[100:103], v[196:199], v[68:71]\n" \
  "v_mul_f32 v116, 0xbacccccd, v116\n" \
  "v_mfma_f32_16x16x32_bf16 v[72:75], v[100:103], v[200:203], v[72:75]\n" \
  "s_waitcnt vmcnt(11)\n" \
  "ds_read_b128 v[100:103], v112 offset:13312\n" \
  "v_mfma_f32_16x16x32_bf16 v[20:23], v[76:79], v[204:207], v[20:23]\n" \
  "buffer_load_dwordx4 v[176:179], v105, %[srd], %[so] offen offset:2304 sc1\n" \
  "s_add_u32 %[so], %[so], 4096\n" \
  "v_mul_f32 v117, 0x3acccccd, v117\n" \
  "v_mfma_f32_16x16x32_bf16 v[24:27], v[76:79], v[208:211], v[24:27]\n" \
  "s_waitcnt lgkmcnt(4)\n" \
  "ds_read_b128 v[76:79], v112 offset:14336\n" \
  "v_mfma_f32_16x16x32_bf16 v[32:35], v[80:83], v[208:211], v[32:35]\n" \
  "v_fma_f32 v117, -v116, v116, v117\n" \
  "v_mfma_f32_16x16x32_bf16 v[28:31], v[80:83], v[204:207], v[28:31]\n" \
  "ds_read_b128 v[80:83], v112 offset:15360\n" \
  "v_mfma_f32_16x16x32_bf16 v[36:39], v[84:87], v[204:207], v[36:39]\n" \
  "v_max_f32 v117, 0, v117\n" \
  "v_mfma_f32_16x16x32_bf16 v[40:43], v[84:87], v[208:211], v[40:43]\n" \
  "s_waitcnt lgkmcnt(4)\n" \
  "ds_read_b128 v[84:87], v112 offset:16384\n" \
  "v_mfma_f32_16x16x32_bf16 v[48:51], v[88:91], v[208:211], v[48:51]\n" \
  "buffer_load_dwordx4 v[180:183], v105, %[srd], %[so] offen offset:0 sc1\n" \
  "v_add_f32 v117, 0x3727c5ac, v117\n" \
  "v_mfma_f32_16x16x32_bf16 v[44:47], v[88:91], v[204:207], v[44:47]\n" \
  "ds_read_b128 v[88:91], v112 offset:17408\n" \
  "v_mfma_f32_16x16x32_bf16 v[52:55], v[92:95], v[204:207], v[52:55]\n" \
  "v_rsq_f32 v117, v117\n" \
  "v_mfma_f32_16x16x32_bf16 v[56:59], v[92:95], v[208:211], v[56:59]\n" \
  "s_waitcnt lgkmcnt(4)\n" \
  "ds_read_b128 v[92:95], v112 offset:18432\n" \
  "v_mfma_f32_16x16x32_bf16 v[64:67], v[96:99], v[208:211], v[64:67]\n" \
  "buffer_load_dwordx4 v[184:187], v105, %[srd], %[so] offen offset:256 sc1\n" \
  "v_mov_b32 v120, v116\n" \
  "v_mfma_f32_16x16x32_bf16 v[60:63], v[96:99], v[204:207], v[60:63]\n" \
  "ds_read_b128 v[96:99], v112 offset:19456\n" \
  "v_mfma_f32_16x16x32_bf16 v[68:71], v[100:103], v[204:207], v[68:71]\n" \
  "v_mov_b32 v121, v117\n" \
  "v_mfma_f32_16x16x32_bf16 v[72:75], v[100:103], v[208:211], v[72:75]\n" \
  "s_waitcnt vmcnt(12) lgkmcnt(4)\n" \
  "ds_read_b128 v[100:103], v112 offset:20480\n" \
  "v_mfma_f32_16x16x32_bf16 v[20:23], v[76:79], v[212:215], v[20:23]\n" \
  "buffer_load_dwordx4 v[188:191], v105, %[srd], %[so] offen offset:2048 sc1\n" \
  "v_permlane16_swap_b32 v116, v120\n" \
  "v_mfma_f32_16x16x32_bf16 v[24:27], v[76:79], v[216:219], v[24:27]\n" \
  "ds_read_b128 v[76:79], v112 offset:21504\n" \
  "v_mfma_f32_16x16x32_bf16 v[32:35], v[80:83], v[216:219], v[32:35]\n" \
  "v_permlane16_swap_b32 v117, v121\n" \
  "v_mfma_f32_16x16x32_bf16 v[28:31], v[80:83], v[212:215], v[28:31]\n" \
  "s_waitcnt lgkmcnt(4)\n" \
  "ds_read_b128 v[80:83], v112 offset:22528\n" \
  "v_mfma_f32_16x16x32_bf16 v[36:39], v[84:87], v[212:215], v[36:39]\n" \
  "v_mfma_f32_16x16x32_bf16 v[40:43], v[84:87], v[216:219], v[40:43]\n" \
  "ds_read_b128 v[84:87], v112 offset:23552\n" \
  "v_mfma_f32_16x16x32_bf16 v[48:51], v[88:91], v[216:219], v[48:51]\n" \
  "buffer_load_dwordx4 v[192:195], v105, %[srd], %[so] offen offset:2304 sc1\n" \
  "s_add_u32 %[so], %[so], 4096\n" \
  "v_mfma_f32_16x16x32_bf16 v[44:47], v[88:91], v[212:215], v[44:47]\n" \
  "s_waitcnt lgkmcnt(4)\n" \
  "ds_read_b128 v[88:91], v112 offset:24576\n" \
  "v_mfma_f32_16x16x32_bf16 v[52:55], v[92:95], v[212:215], v[52:55]\n" \
  "v_mfma_f32_16x16x32_bf16 v[56:59], v[92:95], v[216:219], v[56:59]\n" \
  "ds_read_b128 v[92:95], v112 offset:25600\n" \
  "v_mfma_f32_16x16x32_bf16 v[64:67], v[96:99], v[216:219], v[64:67]\n" \
  "buffer_load_dwordx4 v[196:199], v105, %[srd], %[so] offen offset:0 sc1\n" \
  "v_mfma_f32_16x16x32_bf16 v[60:63], v[96:99], v[212:215], v[60:63]\n" \
  "s_waitcnt lgkmcnt(4)\n" \
  "ds_read_b128 v[96:99], v112 offset:26624\n" \
  "v_mfma_f32_16x16x32_bf16 v[68:71], v[100:103], v[212:215], v[68:71]\n" \
  "v_mfma_f32_16x16x32_bf16 v[72:75], v[100:103], v[216:219], v[72:75]\n" \
  "s_waitcnt vmcnt(13)\n" \
  "ds_read_b128 v[100:103], v112 offset:27648\n" \
  "v_mfma_f32_16x16x32_bf16 v[20:23], v[76:79], v[220:223], v[20:23]\n" \
  "buffer_load_dwordx4 v[200:203], v105, %[srd], %[so] offen offset:256 sc1\n" \
  "v_mfma_f32_16x16x32_bf16 v[24:27], v[76:79], v[224:227], v[24:27]\n" \
  "s_waitcnt lgkmcnt(4)\n" \
  "ds_read_b128 v[76:79], v112 offset:28672\n" \
  "v_mfma_f32_16x16x32_bf16 v[32:35], v[80:83], v[224:227], v[32:35]\n" \
  "v_mfma_f32_16x16x32_bf16 v[28:31], v[80:83], v[220:223], v[28:31]\n" \
  "ds_read_b128 v[80:83], v112 offset:29696\n" \
  "v_mfma_f32_16x16x32_bf16 v[36:39], v[84:87], v[220:223], v[36:39]\n" \
  "v_mfma_f32_16x16x32_bf16 v[40:43], v[84:87], v[224:227], v[40:43]\n" \
  "s_waitcnt lgkmcnt(4)\n" \
  "ds_read_b128 v[84:87], v112 offset:30720\n" \
  "v_mfma_f32_16x16x32_bf16 v[48:51], v[88:91], v[224:227], v[48:51]\n" \
  "buffer_load_dwordx4 v[204:207], v105, %[srd], %[so] offen offset:2048 sc1\n" \
  "v_mfma_f32_16x16x32_bf16 v[44:47], v[88:91], v[220:223], v[44:47]\n" \
  "ds_read_b128 v[88:91], v112 offset:31744\n" \
  "v_mfma_f32_16x16x32_bf16 v[52:55], v[92:95], v[220:223], v[52:55]\n" \
  "v_mfma_f32_16x16x32_bf16 v[56:59], v[92:95], v[224:227], v[56:59]\n" \
  "s_waitcnt lgkmcnt(4)\n" \
  "ds_read_b128 v[92:95], v112 offset:32768\n" \
  "v_mfma_f32_16x16x32_bf16 v[64:67], v[96:99], v[224:227], v[64:67]\n" \
  "buffer_load_dwordx4 v[208:211], v105, %[srd], %[so] offen offset:2304 sc1\n" \
  "s_add_u32 %[so], %[so], 4096\n" \
  "v_mfma_f32_16x16x32_bf16 v[60:63], v[96:99], v[220:223], v[60:63]\n" \
  "ds_read_b128 v[96:99], v112 offset:33792\n" \
  "v_mfma_f32_16x16x32_bf16 v[68:71], v[100:103], v[220:223], v[68:71]\n" \
  "v_mfma_f32_16x16x32_bf16 v[72:75], v[100:103], v[224:227], v[72:75]\n" \
  "s_waitcnt vmcnt(14) lgkmcnt(4)\n" \
  "ds_read_b128 v[100:103], v112 offset:34816\n" \
  "v_mfma_f32_16x16x32_bf16 v[20:23], v[76:79], v[228:231], v[20:23]\n" \
  "v_mfma_f32_16x16x32_bf16 v[24:27], v[76:79], v[232:235], v[24:27]\n" \
  "ds_read_b128 v[76:79], v112 offset:35840\n" \
  "v_mfma_f32_16x16x32_bf16 v[32:35], v[80:83], v[232:235], v[32:35]\n" \
  "buffer_load_dwordx4 v[212:215], v105, %[srd], %[so] offen offset:0 sc1\n" \
  "v_mfma_f32_16x16x32_bf16 v[28:31], v[80:83], v[228:231], v[28:31]\n" \
  "s_waitcnt lgkmcnt(4)\n" \
  "ds_read_b128 v[80:83], v112 offset:36864\n" \
  "v_mfma_f32_16x16x32_bf16 v[36:39], v[84:87], v[228:231], v[36:39]\n" \
  "v_mfma_f32_16x16x32_bf16 v[40:43], v[84:87], v[232:235], v[40:43]\n" \
  "ds_read_b128 v[84:87], v112 offset:37888\n" \
  "v_mfma_f32_16x16x32_bf16 v[48:51], v[88:91], v[232:235], v[48:51]\n" \
  "buffer_load_dwordx4 v[216:219], v105, %[srd], %[so] offen offset:256 sc1\n" \
  "v_mfma_f32_16x16x32_bf16 v[44:47], v[88:91], v[228:231], v[44:47]\n" \
  "s_waitcnt lgkmcnt(4)\n" \
  "ds_read_b128 v[88:91], v112 offset:38912\n" \
  "v_mfma_f32_16x16x32_bf16 v[52:55], v[92:95], v[228:231], v[52:55]\n" \
  "v_mfma_f32_16x16x32_bf16 v[56:59], v[92:95], v[232:235], v[56:59]\n" \
  "ds_read_b128 v[92:95], v112 offset:39936\n" \
  "v_mfma_f32_16x16x32_bf16 v[64:67], v[96:99], v[232:235], v[64:67]\n" \
  "buffer_load_dwordx4 v[220:223], v105, %[srd], %[so] offen offset:2048 sc1\n" \
  "v_mfma_f32_16x16x32_bf16 v[60:63], v[96:99], v[228:231], v[60:63]\n" \
  "s_waitcnt lgkmcnt(4)\n" \
  "ds_read_b128 v[96:99], v112 offset:40960\n" \
  "v_mfma_f32_16x16x32_bf16 v[68:71], v[100:103], v[228:231], v[68:71]\n" \
  "v_mfma_f32_16x16x32_bf16 v[72:75], v[100:103], v[232:235], v[72:75]\n" \
  "s_waitcnt vmcnt(15)\n" \
  "ds_read_b128 v[100:103], v112 offset:41984\n" \
  "v_mfma_f32_16x16x32_bf16 v[20:23], v[76:79], v[236:239], v[20:23]\n" \
  "v_mfma_f32_16x16x32_bf16 v[24:27], v[76:79], v[240:243], v[24:27]\n" \
  "s_waitcnt lgkmcnt(4)\n" \
  "ds_read_b128 v[76:79], v112 offset:43008\n" \
  "v_mfma_f32_16x16x32_bf16 v[32:35], v[80:83], v[240:243], v[32:35]\n" \
  "buffer_load_dwordx4 v[224:227], v105, %[srd], %[so] offen offset:2304 sc1\n" \
  "s_add_u32 %[so], %[so], 4096\n" \
  "v_mfma_f32_16x16x32_bf16 v[28:31], v[80:83], v[236:239], v[28:31]\n" \
  "ds_read_b128 v[80:83], v112 offset:44032\n" \
  "v_mfma_f32_16x16x32_bf16 v[36:39], v[84:87], v[236:239], v[36:39]\n" \
  "v_mfma_f32_16x16x32_bf16 v[40:43], v[84:87], v[240:243], v[40:43]\n" \
  "s_waitcnt lgkmcnt(4)\n" \
  "ds_read_b128 v[84:87], v112 offset:45056\n" \
  "v_mfma_f32_16x16x32_bf16 v[48:51], v[88:91], v[240:243], v[48:51]\n" \
  "buffer_load_dwordx4 v[228:231], v105, %[srd], %[so] offen offset:0 sc1\n" \
  "v_mfma_f32_16x16x32_bf16 v[44:47], v[88:91], v[236:239], v[44:47]\n" \
  "ds_read_b128 v[88:91], v112 offset:46080\n" \
  "v_mfma_f32_16x16x32_bf16 v[52:55], v[92:95], v[236:239], v[52:55]\n" \
  "v_mfma_f32_16x16x32_bf16 v[56:59], v[92:95], v[240:243], v[56:59]\n" \
  "s_waitcnt lgkmcnt(4)\n" \
  "ds_read_b128 v[92:95], v112 offset:47104\n" \
  "v_mfma_f32_16x16x32_bf16 v[64:67], v[96:99], v[240:243], v[64:67]\n" \
  "buffer_load_dwordx4 v[232:235], v105, %[srd], %[so] offen offset:256 sc1\n" \
  "v_mfma_f32_16x16x32_bf16 v[60:63], v[96:99], v[236:239], v[60:63]\n" \
  "ds_read_b128 v[96:99], v112 offset:48128\n" \
  "v_mfma_f32_16x16x32_bf16 v[68:71], v[100:103], v[236:239], v[68:71]\n" \
  "v_mfma_f32_16x16x32_bf16 v[72:75], v[100:103], v[240:243], v[72:75]\n" \
  "s_waitcnt vmcnt(16) lgkmcnt(4)\n" \
  "ds_read_b128 v[100:103], v112 offset:49152\n" \
  "v_mfma_f32_16x16x32_bf16 v[20:23], v[76:79], v[164:167], v[20:23]\n" \
  "s_barrier\n" \
  "v_mfma_f32_16x16x32_bf16 v[24:27], v[76:79], v[168:171], v[24:27]\n" \
  "ds_read_b128 v[76:79], v112 offset:50176\n" \
  "v_mfma_f32_16x16x32_bf16 v[32:35], v[80:83], v[168:171], v[32:35]\n" \
  "buffer_load_dwordx4 v[236:239], v105, %[srd], %[so] offen offset:2048 sc1\n" \
  "ds_read_b128 v[124:127], v108 offset:0\n" \
  "v_mfma_f32_16x16x32_bf16 v[28:31], v[80:83], v[164:167], v[28:31]\n" \
  "s_waitcnt lgkmcnt(5)\n" \
  "ds_read_b128 v[80:83], v112 offset:51200\n" \
  "v_mfma_f32_16x16x32_bf16 v[36:39], v[84:87], v[164:167], v[36:39]\n" \
  "ds_read_b128 v[128:131], v108 offset:64\n" \
  "v_mfma_f32_16x16x32_bf16 v[40:43], v[84:87], v[168:171], v[40:43]\n" \
  "ds_read_b128 v[84:87], v112 offset:52224\n" \
  "v_mfma_f32_16x16x32_bf16 v[48:51], v[88:91], v[168:171], v[48:51]\n" \
  "buffer_load_dwordx4 v[240:243], v105, %[srd], %[so] offen offset:2304 sc1\n" \
  "s_add_u32 %[so], %[so], 4096\n" \
  "ds_read_b128 v[132:135], v108 offset:128\n" \
  "v_mfma_f32_16x16x32_bf16 v[44:47], v[88:91], v[164:167], v[44:47]\n" \
  "s_waitcnt lgkmcnt(7)\n" \
  "ds_read_b128 v[88:91], v112 offset:53248\n" \
  "v_mfma_f32_16x16x32_bf16 v[52:55], v[92:95], v[164:167], v[52:55]\n" \
  "ds_read_b128 v[136:139], v108 offset:192\n" \
  "v_mfma_f32_16x16x32_bf16 v[56:59], v[92:95], v[168:171], v[56:59]\n" \
  "ds_read_b128 v[92:95], v112 offset:54272\n" \
  "v_mfma_f32_16x16x32_bf16 v[64:67], v[96:99], v[168:171], v[64:67]\n" \
  "ds_read_b128 v[140:143], v108 offset:256\n" \
  "v_mfma_f32_16x16x32_bf16 v[60:63], v[96:99], v[164:167], v[60:63]\n" \
  "s_waitcnt lgkmcnt(9)\n" \
  "ds_read_b128 v[96:99], v112 offset:55296\n" \
  "v_mfma_f32_16x16x32_bf16 v[68:71], v[100:103], v[164:167], v[68:71]\n" \
  "ds_read_b128 v[144:147], v108 offset:320\n" \
  "v_mfma_f32_16x16x32_bf16 v[72:75], v[100:103], v[168:171], v[72:75]\n" \
  "s_waitcnt vmcnt(16)\n" \
  "ds_read_b128 v[100:103], v112 offset:56320\n" \
  "v_mfma_f32_16x16x32_bf16 v[20:23], v[76:79], v[172:175], v[20:23]\n" \
  ".Lk_pad3:\n" \
  "s_cbranch_execz .Lk_ret3\n" \
  "ds_read_b128 v[148:151], v248 offset:384\n" \
  "v_mfma_f32_16x16x32_bf16 v[24:27], v[76:79], v[176:179], v[24:27]\n" \
  "s_waitcnt lgkmcnt(9)\n" \
  "ds_read_b128 v[76:79], v113 offset:0\n" \
  "v_mfma_f32_16x16x32_bf16 v[32:35], v[80:83], v[176:179], v[32:35]\n" \
  "v_mfma_f32_16x16x32_bf16 v[28:31], v[80:83], v[172:175], v[28:31]\n" \
  "ds_read_b128 v[80:83], v113 offset:7168\n" \
  "v_mfma_f32_16x16x32_bf16 v[36:39], v[84:87], v[172:175], v[36:39]\n" \
  "v_mfma_f32_16x16x32_bf16 v[40:43], v[84:87], v[176:179], v[40:43]\n" \
  "s_waitcnt lgkmcnt(7)\n" \
  "ds_read_b128 v[84:87], v113 offset:14336\n" \
  "v_mfma_f32_16x16x32_bf16 v[48:51], v[88:91], v[176:179], v[48:51]\n" \
  "v_mfma_f32_16x16x32_bf16 v[44:47], v[88:91], v[172:175], v[44:47]\n" \
  "ds_read_b128 v[88:91], v113 offset:21504\n" \
  "v_mfma_f32_16x16x32_bf16 v[52:55], v[92:95], v[172:175], v[52:55]\n" \
  "v_mfma_f32_16x16x32_bf16 v[56:59], v[92:95], v[176:179], v[56:59]\n" \
  "s_waitcnt lgkmcnt(5)\n" \
  "ds_read_b128 v[92:95], v113 offset:28672\n" \
  "v_mfma_f32_16x16x32_bf16 v[64:67], v[96:99], v[176:179], v[64:67]\n" \
  "v_mfma_f32_16x16x32_bf16 v[60:63], v[96:99], v[172:175], v[60:63]\n" \
  "ds_read_b128 v[96:99], v113 offset:35840\n" \
  "v_mfma_f32_16x16x32_bf16 v[68:71], v[100:103], v[172:175], v[68:71]\n" \
  "v_mfma_f32_16x16x32_bf16 v[72:75], v[100:103], v[176:179], v[72:75]\n" \
  "s_waitcnt vmcnt(14) lgkmcnt(4)\n" \
  "ds_read_b128 v[100:103], v113 offset:43008\n" \
  "v_mfma_f32_16x16x32_bf16 v[20:23], v[76:79], v[180:183], v[20:23]\n" \
  "v_mfma_f32_16x16x32_bf16 v[24:27], v[76:79], v[184:187], v[24:27]\n" \
  "s_waitcnt vmcnt(12)\n" \
  "ds_read_b128 v[76:79], v113 offset:50176\n" \
  "v_mfma_f32_16x16x32_bf16 v[20:23], v[80:83], v[188:191], v[20:23]\n" \
  "s_barrier\n" \
  "v_mfma_f32_16x16x32_bf16 v[24:27], v[80:83], v[192:195], v[24:27]\n" \
  "s_waitcnt vmcnt(10) lgkmcnt(4)\n" \
  "ds_read_b128 v[80:83], v113 offset:1024\n" \
  "v_mfma_f32_16x16x32_bf16 v[20:23], v[84:87], v[196:199], v[20:23]\n" \
  "v_mfma_f32_16x16x32_bf16 v[24:27], v[84:87], v[200:203], v[24:27]\n" \
  "s_waitcnt vmcnt(8)\n" \
  "ds_read_b128 v[84:87], v113 offset:8192\n" \
  "v_mfma_f32_16x16x32_bf16 v[20:23], v[88:91], v[204:207], v[20:23]\n" \
  "v_mfma_f32_16x16x32_bf16 v[24:27], v[88:91], v[208:211], v[24:27]\n" \
  "s_waitcnt vmcnt(6) lgkmcnt(4)\n" \
  "ds_read_b128 v[88:91], v113 offset:15360\n" \
  "v_mfma_f32_16x16x32_bf16 v[20:23], v[92:95], v[212:215], v[20:23]\n" \
  "v_mfma_f32_16x16x32_bf16 v[24:27], v[92:95], v[216:219], v[24:27]\n" \
  "s_waitcnt vmcnt(4)\n" \
  "ds_read_b128 v[92:95], v113 offset:22528\n" \
  "v_mfma_f32_16x16x32_bf16 v[20:23], v[96:99], v[220:223], v[20:23]\n" \
  "v_mfma_f32_16x16x32_bf16 v[24:27], v[96:99], v[224:227], v[24:27]\n" \
  "s_waitcnt vmcnt(2) lgkmcnt(4)\n" \
  "ds_read_b128 v[96:99], v113 offset:29696\n" \
  "v_mfma_f32_16x16x32_bf16 v[20:23], v[100:103], v[228:231], v[20:23]\n" \
  "v_mfma_f32_16x16x32_bf16 v[24:27], v[100:103], v[232:235], v[24:27]\n" \
  "s_waitcnt vmcnt(0)\n" \
  "ds_read_b128 v[100:103], v113 offset:36864\n" \
  "v_mfma_f32_16x16x32_bf16 v[20:23], v[76:79], v[236:239], v[20:23]\n" \
  "v_mfma_f32_16x16x32_bf16 v[24:27], v[76:79], v[240:243], v[24:27]\n" \
  "s_waitcnt lgkmcnt(4)\n" \
  "ds_read_b128 v[76:79], v113 offset:44032\n" \
  "v_mfma_f32_16x16x32_bf16 v[28:31], v[80:83], v[180:183], v[28:31]\n" \
  "v_mfma_f32_16x16x32_bf16 v[32:35], v[80:83], v[184:187], v[32:35]\n" \
  "ds_read_b128 v[80:83], v113 offset:51200\n" \
  "v_mfma_f32_16x16x32_bf16 v[28:31], v[84:87], v[188:191], v[28:31]\n" \
  "v_mfma_f32_16x16x32_bf16 v[32:35], v[84:87], v[192:195], v[32:35]\n" \
  "s_waitcnt lgkmcnt(4)\n" \
  "ds_read_b128 v[84:87], v113 offset:2048\n" \
  "v_mfma_f32_16x16x32_bf16 v[28:31], v[88:91], v[196:199], v[28:31]\n" \
  "v_mfma_f32_16x16x32_bf16 v[32:35], v[88:91], v[200:203], v[32:35]\n" \
  "ds_read_b128 v[88:91], v113 offset:9216\n" \
  "v_fma_f32 v20, v124, v116, v20\n" \
  "v_mfma_f32_16x16x32_bf16 v[28:31], v[92:95], v[204:207], v[28:31]\n" \
  "v_fma_f32 v21, v125, v116, v21\n" \
  "v_fma_f32 v22, v126, v116, v22\n" \
  "v_mfma_f32_16x16x32_bf16 v[32:35], v[92:95], v[208:211], v[32:35]\n" \
  "s_waitcnt lgkmcnt(4)\n" \
  "ds_read_b128 v[92:95], v113 offset:16384\n" \
  "v_fma_f32 v23, v127, v116, v23\n" \
  "v_fma_f32 v24, v124, v120, v24\n" \
  "v_mfma_f32_16x16x32_bf16 v[28:31], v[96:99], v[212:215], v[28:31]\n" \
  "v_fma_f32 v25, v125, v120, v25\n" \
  "v_fma_f32 v26, v126, v120, v26\n" \
  "v_mfma_f32_16x16x32_bf16 v[32:35], v[96:99], v[216:219], v[32:35]\n" \
  "ds_read_b128 v[96:99], v113 offset:23552\n" \
  "v_fma_f32 v27, v127, v120, v27\n" \
  "v_mul_f32 v20, v117, v20\n" \
  "v_mfma_f32_16x16x32_bf16 v[28:31], v[100:103], v[220:223], v[28:31]\n" \
  "v_mul_f32 v21, v117, v21\n" \
  "v_mul_f32 v22, v117, v22\n" \
  "v_mfma_f32_16x16x32_bf16 v[32:35], v[100:103], v[224:227], v[32:35]\n" \
  "s_waitcnt lgkmcnt(4)\n" \
  "ds_read_b128 v[100:103], v113 offset:30720\n" \
  "v_mul_f32 v23, v117, v23\n" \
  "v_mul_f32 v24, v121, v24\n" \
  "v_mfma_f32_16x16x32_bf16 v[28:31], v[76:79], v[228:231], v[28:31]\n" \
  "v_mul_f32 v25, v121, v25\n" \
  "v_mul_f32 v26, v121, v26\n" \
  "v_mfma_f32_16x16x32_bf16 v[32:35], v[76:79], v[232:235], v[32:35]\n" \
  "ds_read_b128 v[76:79], v113 offset:37888\n" \
  "v_mul_f32 v27, v121, v27\n" \
  "v_cvt_pk_bf16_f32 v152, v20, v21\n" \
  "v_mfma_f32_16x16x32_bf16 v[28:31], v[80:83], v[236:239], v[28:31]\n" \
  "v_cvt_pk_bf16_f32 v153, v22, v23\n" \
  "v_cvt_pk_bf16_f32 v154, v24, v25\n" \
  "v_mfma_f32_16x16x32_bf16 v[32:35], v[80:83], v[240:243], v[32:35]\n" \
  "s_waitcnt lgkmcnt(4)\n" \
  "ds_read_b128 v[80:83], v113 offset:45056\n" \
  "v_cvt_pk_bf16_f32 v155, v26, v27\n" \
  "v_pk_max_i16 v152, v152, 0\n" \
  "v_mfma_f32_16x16x32_bf16 v[36:39], v[84:87], v[180:183], v[36:39]\n" \
  "v_pk_max_i16 v154, v154, 0\n" \
  "v_pk_max_i16 v153, v153, 0\n" \
  "v_mfma_f32_16x16x32_bf16 v[40:43], v[84:87], v[184:187], v[40:43]\n" \
  "ds_read_b128 v[84:87], v113 offset:52224\n" \
  "v_pk_max_i16 v155, v155, 0\n" \
  "s_nop 0\n" \
  "v_mfma_f32_16x16x32_bf16 v[36:39], v[88:91], v[188:191], v[36:39]\n" \
  "v_permlane16_swap_b32 v152, v154\n" \
  "v_permlane16_swap_b32 v153, v155\n" \
  "v_mfma_f32_16x16x32_bf16 v[40:43], v[88:91], v[192:195], v[40:43]\n" \
  "s_waitcnt lgkmcnt(4)\n" \
  "ds_read_b128 v[88:91], v113 offset:3072\n" \
  "buffer_store_dwordx4 v[152:155], v109, %[usrd], %[us0] offen offset:0 sc1\n" \
  "v_mfma_f32_16x16x32_bf16 v[36:39], v[92:95], v[196:199], v[36:39]\n" \
  "v_mfma_f32_16x16x32_bf16 v[40:43], v[92:95], v[200:203], v[40:43]\n" \
  "ds_read_b128 v[92:95], v113 offset:10240\n" \
  "v_fma_f32 v28, v128, v116, v28\n" \
  "v_mfma_f32_16x16x32_bf16 v[36:39], v[96:99], v[204:207], v[36:39]\n" \
  "v_fma_f32 v29, v129, v116, v29\n" \
  "v_fma_f32 v30, v130, v116, v30\n" \
  "v_mfma_f32_16x16x32_bf16 v[40:43], v[96:99], v[208:211], v[40:43]\n" \
  "s_waitcnt lgkmcnt(4)\n" \
  "ds_read_b128 v[96:99], v113 offset:17408\n" \
  "v_fma_f32 v31, v131, v116, v31\n" \
  "v_fma_f32 v32, v128, v120, v32\n" \
  "v_mfma_f32_16x16x32_bf16 v[36:39], v[100:103], v[212:215], v[36:39]\n" \
  "v_fma_f32 v33, v129, v120, v33\n" \
  "v_fma_f32 v34, v130, v120, v34\n" \
  "v_mfma_f32_16x16x32_bf16 v[40:43], v[100:103], v[216:219], v[40:43]\n" \
  "ds_read_b128 v[100:103], v113 offset:24576\n" \
  "v_fma_f32 v35, v131, v120, v35\n" \
  "v_mul_f32 v28, v117, v28\n" \
  "v_mfma_f32_16x16x32_bf16 v[36:39], v[76:79], v[220:223], v[36:39]\n" \
  "v_mul_f32 v29, v117, v29\n" \
  "v_mul_f32 v30, v117, v30\n" \
  "v_mfma_f32_16x16x32_bf16 v[40:43], v[76:79], v[224:227], v[40:43]\n" \
  "s_waitcnt lgkmcnt(4)\n" \
  "ds_read_b128 v[76:79], v113 offset:31744\n" \
  "v_mul_f32 v31, v117, v31\n" \
  "v_mul_f32 v32, v121, v32\n" \
  "v_mfma_f32_16x16x32_bf16 v[36:39], v[80:83], v[228:231], v[36:39]\n" \
  "v_mul_f32 v33, v121, v33\n" \
  "v_mul_f32 v34, v121, v34\n" \
  "v_mfma_f32_16x16x32_bf16 v[40:43], v[80:83], v[232:235], v[40:43]\n" \
  "ds_read_b128 v[80:83], v113 offset:38912\n" \
  "v_mul_f32 v35, v121, v35\n" \
  "v_cvt_pk_bf16_f32 v156, v28, v29\n" \
  "v_mfma_f32_16x16x32_bf16 v[36:39], v[84:87], v[236:239], v[36:39]\n" \
  "v_cvt_pk_bf16_f32 v157, v30, v31\n" \
  "v_cvt_pk_bf16_f32 v158, v32, v33\n" \
  "v_mfma_f32_16x16x32_bf16 v[40:43], v[84:87], v[240:243], v[40:43]\n" \
  "s_waitcnt lgkmcnt(4)\n" \
  "ds_read_b128 v[84:87], v113 offset:46080\n" \
  "v_cvt_pk_bf16_f32 v159, v34, v35\n" \
  "v_pk_max_i16 v156, v156, 0\n" \
  "v_mfma_f32_16x16x32_bf16 v[44:47], v[88:91], v[180:183], v[44:47]\n" \
  "v_pk_max_i16 v158, v158, 0\n" \
  "v_pk_max_i16 v157, v157, 0\n" \
  "v_mfma_f32_16x16x32_bf16 v[48:51], v[88:91], v[184:187], v[48:51]\n" \
  "ds_read_b128 v[88:91], v113 offset:53248\n" \
  "v_pk_max_i16 v159, v159, 0\n" \
  "s_nop 0\n" \
  "v_mfma_f32_16x16x32_bf16 v[44:47], v[92:95], v[188:191], v[44:47]\n" \
  "v_permlane16_swap_b32 v156, v158\n" \
  "v_permlane16_swap_b32 v157, v159\n" \
  "v_mfma_f32_16x16x32_bf16 v[48:51], v[92:95], v[192:195], v[48:51]\n" \
  "s_waitcnt lgkmcnt(4)\n" \
  "ds_read_b128 v[92:95], v113 offset:4096\n" \
  "buffer_store_dwordx4 v[156:159], v109, %[usrd], %[us0] offen offset:1024 sc1\n" \
  "v_mfma_f32_16x16x32_bf16 v[44:47], v[96:99], v[196:199], v[44:47]\n" \
  "v_mfma_f32_16x16x32_bf16 v[48:51], v[96:99], v[200:203], v[48:51]\n" \
  "ds_read_b128 v[96:99], v113 offset:11264\n" \
  "v_fma_f32 v36, v132, v116, v36\n" \
  "v_mfma_f32_16x16x32_bf16 v[44:47], v[100:103], v[204:207], v[44:47]\n" \
  "v_fma_f32 v37, v133, v116, v37\n" \
  "v_fma_f32 v38, v134, v116, v38\n" \
  "v_mfma_f32_16x16x32_bf16 v[48:51], v[100:103], v[208:211], v[48:51]\n" \
  "s_waitcnt lgkmcnt(4)\n" \
  "ds_read_b128 v[100:103], v113 offset:18432\n" \
  "v_fma_f32 v39, v135, v116, v39\n" \
  "v_fma_f32 v40, v132, v120, v40\n" \
  "v_mfma_f32_16x16x32_bf16 v[44:47], v[76:79], v[212:215], v[44:47]\n" \
  "v_fma_f32 v41, v133, v120, v41\n" \
  "v_fma_f32 v42, v134, v120, v42\n" \
  "v_mfma_f32_16x16x32_bf16 v[48:51], v[76:79], v[216:219], v[48:51]\n" \
  "ds_read_b128 v[76:79], v113 offset:25600\n" \
  "v_fma_f32 v43, v135, v120, v43\n" \
  "v_mul_f32 v36, v117, v36\n" \
  "v_mfma_f32_16x16x32_bf16 v[44:47], v[80:83], v[220:223], v[44:47]\n" \
  "v_mul_f32 v37, v117, v37\n" \
  "v_mul_f32 v38, v117, v38\n" \
  "v_mfma_f32_16x16x32_bf16 v[48:51], v[80:83], v[224:227], v[48:51]\n" \
  "s_waitcnt lgkmcnt(4)\n" \
  "ds_read_b128 v[80:83], v113 offset:32768\n" \
  "v_mul_f32 v39, v117, v39\n" \
  "v_mul_f32 v40, v121, v40\n" \
  "v_mfma_f32_16x16x32_bf16 v[44:47], v[84:87], v[228:231], v[44:47]\n" \
  "v_mul_f32 v41, v121, v41\n" \
  "v_mul_f32 v42, v121, v42\n" \
  "v_mfma_f32_16x16x32_bf16 v[48:51], v[84:87], v[232:235], v[48:51]\n" \
  "ds_read_b128 v[84:87], v113 offset:39936\n" \
  "v_mul_f32 v43, v121, v43\n" \
  "v_cvt_pk_bf16_f32 v152, v36, v37\n" \
  "v_mfma_f32_16x16x32_bf16 v[44:47], v[88:91], v[236:239], v[44:47]\n" \
  "v_cvt_pk_bf16_f32 v153, v38, v39\n" \
  "v_cvt_pk_bf16_f32 v154, v40, v41\n" \
  "v_mfma_f32_16x16x32_bf16 v[48:51], v[88:91], v[240:243], v[48:51]\n" \
  "s_waitcnt lgkmcnt(4)\n" \
  "ds_read_b128 v[88:91], v113 offset:47104\n" \
  "v_cvt_pk_bf16_f32 v155, v42, v43\n" \
  "v_pk_max_i16 v152, v152, 0\n" \
  "v_mfma_f32_16x16x32_bf16 v[52:55], v[92:95], v[180:183], v[52:55]\n" \
  "v_pk_max_i16 v154, v154, 0\n" \
  "v_pk_max_i16 v153, v153, 0\n" \
  "v_mfma_f32_16x16x32_bf16 v[56:59], v[92:95], v[184:187], v[56:59]\n" \
  "ds_read_b128 v[92:95], v113 offset:54272\n" \
  "v_pk_max_i16 v155, v155, 0\n" \
  "s_nop 0\n" \
  "v_mfma_f32_16x16x32_bf16 v[52:55], v[96:99], v[188:191], v[52:55]\n" \
  "v_permlane16_swap_b32 v152, v154\n" \
  "v_permlane16_swap_b32 v153, v155\n" \
  "v_mfma_f32_16x16x32_bf16 v[56:59], v[96:99], v[192:195], v[56:59]\n" \
  "s_waitcnt lgkmcnt(4)\n" \
  "ds_read_b128 v[96:99], v113 offset:5120\n" \
  "buffer_store_dwordx4 v[152:155], v109, %[usrd], %[us0] offen offset:2048 sc1\n" \
  "v_mfma_f32_16x16x32_bf16 v[52:55], v[100:103], v[196:199], v[52:55]\n" \
  "v_mfma_f32_16x16x32_bf16 v[56:59], v[100:103], v[200:203], v[56:59]\n" \
  "ds_read_b128 v[100:103], v113 offset:12288\n" \
  "v_fma_f32 v44, v136, v116, v44\n" \
  "v_mfma_f32_16x16x32_bf16 v[52:55], v[76:79], v[204:207], v[52:55]\n" \
  "v_fma_f32 v45, v137, v116, v45\n" \
  "v_fma_f32 v46, v138, v116, v46\n" \
  "v_mfma_f32_16x16x32_bf16 v[56:59], v[76:79], v[208:211], v[56:59]\n" \
  "s_waitcnt lgkmcnt(4)\n" \
  "ds_read_b128 v[76:79], v113 offset:19456\n" \
  "v_fma_f32 v47, v139, v116, v47\n" \
  "v_fma_f32 v48, v136, v120, v48\n" \
  "v_mfma_f32_16x16x32_bf16 v[52:55], v[80:83], v[212:215], v[52:55]\n" \
  "v_fma_f32 v49, v137, v120, v49\n" \
  "v_fma_f32 v50, v138, v120, v50\n" \
  "v_mfma_f32_16x16x32_bf16 v[56:59], v[80:83], v[216:219], v[56:59]\n" \
  "ds_read_b128 v[80:83], v113 offset:26624\n" \
  "v_fma_f32 v51, v139, v120, v51\n" \
  "v_mul_f32 v44, v117, v44\n" \
  "v_mfma_f32_16x16x32_bf16 v[52:55], v[84:87], v[220:223], v[52:55]\n" \
  "v_mul_f32 v45, v117, v45\n" \
  "v_mul_f32 v46, v117, v46\n" \
  "v_mfma_f32_16x16x32_bf16 v[56:59], v[84:87], v[224:227], v[56:59]\n" \
  "s_waitcnt lgkmcnt(4)\n" \
  "ds_read_b128 v[84:87], v113 offset:33792\n" \
  "v_mul_f32 v47, v117, v47\n" \
  "v_mul_f32 v48, v121, v48\n" \
  "v_mfma_f32_16x16x32_bf16 v[52:55], v[88:91], v[228:231], v[52:55]\n" \
  "v_mul_f32 v49, v121, v49\n" \
  "v_mul_f32 v50, v121, v50\n" \
  "v_mfma_f32_16x16x32_bf16 v[56:59], v[88:91], v[232:235], v[56:59]\n" \
  "ds_read_b128 v[88:91], v113 offset:40960\n" \
  "v_mul_f32 v51, v121, v51\n" \
  "v_cvt_pk_bf16_f32 v156, v44, v45\n" \
  "v_mfma_f32_16x16x32_bf16 v[52:55], v[92:95], v[236:239], v[52:55]\n" \
  "v_cvt_pk_bf16_f32 v157, v46, v47\n" \
  "v_cvt_pk_bf16_f32 v158, v48, v49\n" \
  "v_mfma_f32_16x16x32_bf16 v[56:59], v[92:95], v[240:243], v[56:59]\n" \
  "s_waitcnt lgkmcnt(4)\n" \
  "ds_read_b128 v[92:95], v113 offset:48128\n" \
  "v_cvt_pk_bf16_f32 v159, v50, v51\n" \
  "v_pk_max_i16 v156, v156, 0\n" \
  "v_mfma_f32_16x16x32_bf16 v[60:63], v[96:99], v[180:183], v[60:63]\n" \
  "v_pk_max_i16 v158, v158, 0\n" \
  "v_pk_max_i16 v157, v157, 0\n" \
  "v_mfma_f32_16x16x32_bf16 v[64:67], v[96:99], v[184:187], v[64:67]\n" \
  "ds_read_b128 v[96:99], v113 offset:55296\n" \
  "v_pk_max_i16 v159, v159, 0\n" \
  "s_nop 0\n" \
  "v_mfma_f32_16x16x32_bf16 v[60:63], v[100:103], v[188:191], v[60:63]\n" \
  "v_permlane16_swap_b32 v156, v158\n" \
  "v_permlane16_swap_b32 v157, v159\n" \
  "v_mfma_f32_16x16x32_bf16 v[64:67], v[100:103], v[192:195], v[64:67]\n" \
  "s_waitcnt lgkmcnt(4)\n" \
  "ds_read_b128 v[100:103], v113 offset:6144\n" \
  "buffer_store_dwordx4 v[156:159], v109, %[usrd], %[us0] offen offset:3072 sc1\n" \
  "v_mfma_f32_16x16x32_bf16 v[60:63], v[76:79], v[196:199], v[60:63]\n" \
  "v_mfma_f32_16x16x32_bf16 v[64:67], v[76:79], v[200:203], v[64:67]\n" \
  "ds_read_b128 v[76:79], v113 offset:13312\n" \
  "v_fma_f32 v52, v140, v116, v52\n" \
  "v_mfma_f32_16x16x32_bf16 v[60:63], v[80:83], v[204:207], v[60:63]\n" \
  "v_fma_f32 v53, v141, v116, v53\n" \
  "v_fma_f32 v54, v142, v116, v54\n" \
  "v_mfma_f32_16x16x32_bf16 v[64:67], v[80:83], v[208:211], v[64:67]\n" \
  "s_waitcnt lgkmcnt(4)\n" \
  "ds_read_b128 v[80:83], v113 offset:20480\n" \
  "v_fma_f32 v55, v143, v116, v55\n" \
  "v_fma_f32 v56, v140, v120, v56\n" \
  "v_mfma_f32_16x16x32_bf16 v[60:63], v[84:87], v[212:215], v[60:63]\n" \
  "v_fma_f32 v57, v141, v120, v57\n" \
  "v_fma_f32 v58, v142, v120, v58\n" \
  "v_mfma_f32_16x16x32_bf16 v[64:67], v[84:87], v[216:219], v[64:67]\n" \
  "ds_read_b128 v[84:87], v113 offset:27648\n" \
  "v_fma_f32 v59, v143, v120, v59\n" \
  "v_mul_f32 v52, v117, v52\n" \
  "v_mfma_f32_16x16x32_bf16 v[60:63], v[88:91], v[220:223], v[60:63]\n" \
  "v_mul_f32 v53, v117, v53\n" \
  "v_mul_f32 v54, v117, v54\n" \
  "v_mfma_f32_16x16x32_bf16 v[64:67], v[88:91], v[224:227], v[64:67]\n" \
  "s_waitcnt lgkmcnt(4)\n" \
  "ds_read_b128 v[88:91], v113 offset:34816\n" \
  "v_mul_f32 v55, v117, v55\n" \
  "v_mul_f32 v56, v121, v56\n" \
  "v_mfma_f32_16x16x32_bf16 v[60:63], v[92:95], v[228:231], v[60:63]\n" \
  "v_mul_f32 v57, v121, v57\n" \
  "v_mul_f32 v58, v121, v58\n" \
  "v_mfma_f32_16x16x32_bf16 v[64:67], v[92:95], v[232:235], v[64:67]\n" \
  "ds_read_b128 v[92:95], v113 offset:41984\n" \
  "v_mul_f32 v59, v121, v59\n" \
  "v_cvt_pk_bf16_f32 v152, v52, v53\n" \
  "v_mfma_f32_16x16x32_bf16 v[60:63], v[96:99], v[236:239], v[60:63]\n" \
  "v_cvt_pk_bf16_f32 v153, v54, v55\n" \
  "v_cvt_pk_bf16_f32 v154, v56, v57\n" \
  "v_mfma_f32_16x16x32_bf16 v[64:67], v[96:99], v[240:243], v[64:67]\n" \
  "s_waitcnt lgkmcnt(4)\n" \
  "ds_read_b128 v[96:99], v113 offset:49152\n" \
  "v_cvt_pk_bf16_f32 v155, v58, v59\n" \
  "v_pk_max_i16 v152, v152, 0\n" \
  "v_mfma_f32_16x16x32_bf16 v[68:71], v[100:103], v[180:183], v[68:71]\n" \
  "v_pk_max_i16 v154, v154, 0\n" \
  "v_pk_max_i16 v153, v153, 0\n" \
  "v_mfma_f32_16x16x32_bf16 v[72:75], v[100:103], v[184:187], v[72:75]\n" \
  "ds_read_b128 v[100:103], v113 offset:56320\n" \
  "v_pk_max_i16 v155, v155, 0\n" \
  "s_nop 0\n" \
  "v_mfma_f32_16x16x32_bf16 v[68:71], v[76:79], v[188:191], v[68:71]\n" \
  "v_permlane16_swap_b32 v152, v154\n" \
  "v_permlane16_swap_b32 v153, v155\n" \
  "v_mfma_f32_16x16x32_bf16 v[72:75], v[76:79], v[192:195], v[72:75]\n" \
  "s_waitcnt lgkmcnt(4)\n" \
  "buffer_store_dwordx4 v[152:155], v109, %[usrd], %[us1] offen offset:0 sc1\n" \
  "v_mfma_f32_16x16x32_bf16 v[68:71], v[80:83], v[196:199], v[68:71]\n" \
  "v_mfma_f32_16x16x32_bf16 v[72:75], v[80:83], v[200:203], v[72:75]\n" \
  "v_fma_f32 v60, v144, v116, v60\n" \
  "v_mfma_f32_16x16x32_bf16 v[68:71], v[84:87], v[204:207], v[68:71]\n" \
  "v_fma_f32 v61, v145, v116, v61\n" \
  "v_fma_f32 v62, v146, v116, v62\n" \
  "v_mfma_f32_16x16x32_bf16 v[72:75], v[84:87], v[208:211], v[72:75]\n" \
  "s_waitcnt lgkmcnt(2)\n" \
  "v_fma_f32 v63, v147, v116, v63\n" \
  "v_fma_f32 v64, v144, v120, v64\n" \
  "v_mfma_f32_16x16x32_bf16 v[68:71], v[88:91], v[212:215], v[68:71]\n" \
  "v_fma_f32 v65, v145, v120, v65\n" \
  "v_fma_f32 v66, v146, v120, v66\n" \
  "v_mfma_f32_16x16x32_bf16 v[72:75], v[88:91], v[216:219], v[72:75]\n" \
  "v_fma_f32 v67, v147, v120, v67\n" \
  "v_mul_f32 v60, v117, v60\n" \
  "v_mfma_f32_16x16x32_bf16 v[68:71], v[92:95], v[220:223], v[68:71]\n" \
  "v_mul_f32 v61, v117, v61\n" \
  "v_mul_f32 v62, v117, v62\n" \
  "v_mfma_f32_16x16x32_bf16 v[72:75], v[92:95], v[224:227], v[72:75]\n" \
  "s_waitcnt lgkmcnt(0)\n" \
  "v_mul_f32 v63, v117, v63\n" \
  "v_mul_f32 v64, v121, v64\n" \
  "v_mfma_f32_16x16x32_bf16 v[68:71], v[96:99], v[228:231], v[68:71]\n" \
  "v_mul_f32 v65, v121, v65\n" \
  "v_mul_f32 v66, v121, v66\n" \
  "v_mfma_f32_16x16x32_bf16 v[72:75], v[96:99], v[232:235], v[72:75]\n" \
  "v_mul_f32 v67, v121, v67\n" \
  "v_cvt_pk_bf16_f32 v156, v60, v61\n" \
  "v_mfma_f32_16x16x32_bf16 v[68:71], v[100:103], v[236:239], v[68:71]\n" \
  "v_cvt_pk_bf16_f32 v157, v62, v63\n" \
  "v_cvt_pk_bf16_f32 v158, v64, v65\n" \
  "v_mfma_f32_16x16x32_bf16 v[72:75], v[100:103], v[240:243], v[72:75]\n" \
  "v_cvt_pk_bf16_f32 v159, v66, v67\n" \
  "v_pk_max_i16 v156, v156, 0\n" \
  "s_barrier\n" \
  "v_pk_max_i16 v158, v158, 0\n" \
  "v_pk_max_i16 v157, v157, 0\n" \
  "v_pk_max_i16 v159, v159, 0\n" \
  "s_nop 0\n" \
  "v_permlane16_swap_b32 v156, v158\n" \
  "v_permlane16_swap_b32 v157, v159\n" \
  "buffer_store_dwordx4 v[156:159], v109, %[usrd], %[us1] offen offset:1024 sc1\n" \
  "s_nop 6\n" \
  "v_pk_fma_f32 v[68:69], v[148:149], v[116:117], v[68:69] op_sel_hi:[1,0,1]\n" \
  "v_pk_fma_f32 v[70:71], v[150:151], v[116:117], v[70:71] op_sel_hi:[1,0,1]\n" \
  "v_pk_fma_f32 v[72:73], v[148:149], v[120:121], v[72:73] op_sel_hi:[1,0,1]\n" \
  "v_pk_fma_f32 v[74:75], v[150:151], v[120:121], v[74:75] op_sel_hi:[1,0,1]\n" \
  "v_pk_mul_f32 v[68:69], v[68:69], v[116:117] op_sel:[0,1] op_sel_hi:[1,1]\n" \
  "v_pk_mul_f32 v[70:71], v[70:71], v[116:117] op_sel:[0,1] op_sel_hi:[1,1]\n" \
  "v_pk_mul_f32 v[72:73], v[72:73], v[120:121] op_sel:[0,1] op_sel_hi:[1,1]\n" \
  "v_pk_mul_f32 v[74:75], v[74:75], v[120:121] op_sel:[0,1] op_sel_hi:[1,1]\n" \
  "v_cvt_pk_bf16_f32 v152, v68, v69\n" \
  "v_cvt_pk_bf16_f32 v153, v70, v71\n" \
  "v_cvt_pk_bf16_f32 v154, v72, v73\n" \
  "v_cvt_pk_bf16_f32 v155, v74, v75\n" \
  "v_pk_max_i16 v152, v152, 0\n" \
  "v_pk_max_i16 v154, v154, 0\n" \
  "v_pk_max_i16 v153, v153, 0\n" \
  "v_pk_max_i16 v155, v155, 0\n" \
  "s_nop 0\n" \
  "v_permlane16_swap_b32 v152, v154\n" \
  "v_permlane16_swap_b32 v153, v155\n" \
  "buffer_store_dwordx4 v[152:155], v109, %[usrd], %[us2] offen offset:0 sc1\n" \
  "s_nop 1\n"
#define P4_CLOB "v20", "v21", "v22", "v23", "v24", "v25", "v26", "v27", "v28", "v29", "v30", "v31", "v32", "v33", "v34", "v35", "v36", "v37", "v38", "v39", "v40", "v41", "v42", "v43", "v44", "v45", "v46", "v47", "v48", "v49", "v50", "v51", "v52", "v53", "v54", "v55", "v56", "v57", "v58", "v59", "v60", "v61", "v62", "v63", "v64", "v65", "v66", "v67", "v68", "v69", "v70", "v71", "v72", "v73", "v74", "v75", "v76", "v77", "v78", "v79", "v80", "v81", "v82", "v83", "v84", "v85", "v86", "v87", "v88", "v89", "v90", "v91", "v92", "v93", "v94", "v95", "v96", "v97", "v98", "v99", "v100", "v101", "v102", "v103", "v112", "v113", "v114", "v115", "v116", "v117", "v118", "v119", "v120", "v121", "v122", "v123", "v124", "v125", "v126", "v127", "v128", "v129", "v130", "v131", "v132", "v133", "v134", "v135", "v136", "v137", "v138", "v139", "v140", "v141", "v142", "v143", "v144", "v145", "v146", "v147", "v148", "v149", "v150", "v151", "v152", "v153", "v154", "v155", "v156", "v157", "v158", "v159", "v160", "v161", "v162", "v163"
#define HELPER1_ASM \
  "s_mov_b32 %[sm0], m0\n" \
  "s_cmp_ge_u32 %[nb], 15\n" \
  "s_cbranch_scc1 6f\n" \
  "s_add_u32 m0, %[l0], 16384\n" \
  "s_nop 0\n" \
  "global_load_lds_dwordx4 v4, s[38:39] offset:0\n" \
  "global_load_lds_dwordx4 v5, s[38:39] offset:1024\n" \
  "global_load_lds_dwordx4 v6, s[38:39] offset:2048\n" \
  "global_load_lds_dwordx4 v7, s[38:39] offset:3072\n" \
  "s_add_u32 s38, s38, 245760\n" \
  "s_addc_u32 s39, s39, 0\n" \
  "s_add_u32 m0, %[l0], 0\n" \
  "s_nop 0\n" \
  "global_load_lds_dwordx4 v0, s[36:37] offset:0\n" \
  "global_load_lds_dwordx4 v1, s[36:37] offset:1024\n" \
  "global_load_lds_dwordx4 v2, s[36:37] offset:2048\n" \
  "global_load_lds_dwordx4 v3, s[36:37] offset:3072\n" \
  "s_add_u32 s36, s36, 128\n" \
  "s_addc_u32 s37, s37, 0\n" \
  "s_mov_b64 exec, 0xfffff\n" \
  "global_load_dword v20, v18, %[wq]\n" \
  "s_mov_b64 exec, -1\n" \
  "s_add_u32 m0, %[l0], 49152\n" \
  "s_nop 0\n" \
  "global_load_lds_dwordx4 v4, s[38:39] offset:0\n" \
  "global_load_lds_dwordx4 v5, s[38:39] offset:1024\n" \
  "global_load_lds_dwordx4 v6, s[38:39] offset:2048\n" \
  "global_load_lds_dwordx4 v7, s[38:39] offset:3072\n" \
  "s_add_u32 s38, s38, 245760\n" \
  "s_addc_u32 s39, s39, 0\n" \
  "s_add_u32 m0, %[l0], 32768\n" \
  "s_nop 0\n" \
  "global_load_lds_dwordx4 v0, s[36:37] offset:0\n" \
  "global_load_lds_dwordx4 v1, s[36:37] offset:1024\n" \
  "global_load_lds_dwordx4 v2, s[36:37] offset:2048\n" \
  "global_load_lds_dwordx4 v3, s[36:37] offset:3072\n" \
  "s_add_u32 s36, s36, 128\n" \
  "s_addc_u32 s37, s37, 0\n" \
  "s_mov_b64 exec, 0xfffff\n" \
  "global_load_dword v20, v19, %[x]\n" \
  "s_mov_b64 exec, -1\n" \
  "s_add_u32 m0, %[l0], 81920\n" \
  "s_nop 0\n" \
  "global_load_lds_dwordx4 v4, s[38:39] offset:0\n" \
  "global_load_lds_dwordx4 v5, s[38:39] offset:1024\n" \
  "global_load_lds_dwordx4 v6, s[38:39] offset:2048\n" \
  "global_load_lds_dwordx4 v7, s[38:39] offset:3072\n" \
  "s_add_u32 s38, s38, 245760\n" \
  "s_addc_u32 s39, s39, 0\n" \
  "s_waitcnt vmcnt(14)\n" \
  "s_barrier\n" \
  "s_add_u32 m0, %[l0], 65536\n" \
  "s_nop 0\n" \
  "global_load_lds_dwordx4 v0, s[36:37] offset:0\n" \
  "global_load_lds_dwordx4 v1, s[36:37] offset:1024\n" \
  "global_load_lds_dwordx4 v2, s[36:37] offset:2048\n" \
  "global_load_lds_dwordx4 v3, s[36:37] offset:3072\n" \
  "s_add_u32 s36, s36, 128\n" \
  "s_addc_u32 s37, s37, 0\n" \
  "s_add_u32 m0, %[l0], 114688\n" \
  "s_nop 0\n" \
  "global_load_lds_dwordx4 v4, s[38:39] offset:0\n" \
  "global_load_lds_dwordx4 v5, s[38:39] offset:1024\n" \
  "global_load_lds_dwordx4 v6, s[38:39] offset:2048\n" \
  "global_load_lds_dwordx4 v7, s[38:39] offset:3072\n" \
  "s_add_u32 s38, s38, 245760\n" \
  "s_addc_u32 s39, s39, 0\n" \
  "s_waitcnt vmcnt(13)\n" \
  "s_barrier\n" \
  "s_add_u32 m0, %[l0], 98304\n" \
  "s_nop 0\n" \
  "global_load_lds_dwordx4 v0, s[36:37] offset:0\n" \
  "global_load_lds_dwordx4 v1, s[36:37] offset:1024\n" \
  "global_load_lds_dwordx4 v2, s[36:37] offset:2048\n" \
  "global_load_lds_dwordx4 v3, s[36:37] offset:3072\n" \
  "s_add_u32 s36, s36, 128\n" \
  "s_addc_u32 s37, s37, 0\n" \
  "s_add_u32 m0, %[l0], 16384\n" \
  "s_nop 0\n" \
  "global_load_lds_dwordx4 v4, s[38:39] offset:0\n" \
  "global_load_lds_dwordx4 v5, s[38:39] offset:1024\n" \
  "global_load_lds_dwordx4 v6, s[38:39] offset:2048\n" \
  "global_load_lds_dwordx4 v7, s[38:39] offset:3072\n" \
  "s_add_u32 s38, s38, 245760\n" \
  "s_addc_u32 s39, s39, 0\n" \
  "s_waitcnt vmcnt(12)\n" \
  "s_barrier\n" \
  "s_add_u32 m0, %[l0], 0\n" \
  "s_nop 0\n" \
  "global_load_lds_dwordx4 v0, s[36:37] offset:0\n" \
  "global_load_lds_dwordx4 v1, s[36:37] offset:1024\n" \
  "global_load_lds_dwordx4 v2, s[36:37] offset:2048\n" \
  "global_load_lds_dwordx4 v3, s[36:37] offset:3072\n" \
  "s_add_u32 s36, s36, 128\n" \
  "s_addc_u32 s37, s37, 0\n" \
  "s_add_u32 m0, %[l0], 49152\n" \
  "s_nop 0\n" \
  "global_load_lds_dwordx4 v4, s[38:39] offset:0\n" \
  "global_load_lds_dwordx4 v5, s[38:39] offset:1024\n" \
  "global_load_lds_dwordx4 v6, s[38:39] offset:2048\n" \
  "global_load_lds_dwordx4 v7, s[38:39] offset:3072\n" \
  "s_add_u32 s38, s38, 245760\n" \
  "s_addc_u32 s39, s39, 0\n" \
  "s_waitcnt vmcnt(12)\n" \
  "s_barrier\n" \
  "s_add_u32 m0, %[l0], 32768\n" \
  "s_nop 0\n" \
  "global_load_lds_dwordx4 v0, s[36:37] offset:0\n" \
  "global_load_lds_dwordx4 v1, s[36:37] offset:1024\n" \
  "global_load_lds_dwordx4 v2, s[36:37] offset:2048\n" \
  "global_load_lds_dwordx4 v3, s[36:37] offset:3072\n" \
  "s_add_u32 s36, s36, 128\n" \
  "s_addc_u32 s37, s37, 0\n" \
  "s_add_u32 m0, %[l0], 81920\n" \
  "s_nop 0\n" \
  "global_load_lds_dwordx4 v4, s[38:39] offset:0\n" \
  "global_load_lds_dwordx4 v5, s[38:39] offset:1024\n" \
  "global_load_lds_dwordx4 v6, s[38:39] offset:2048\n" \
  "global_load_lds_dwordx4 v7, s[38:39] offset:3072\n" \
  "s_add_u32 s38, s38, 245760\n" \
  "s_addc_u32 s39, s39, 0\n" \
  "s_waitcnt vmcnt(12)\n" \
  "s_barrier\n" \
  "s_add_u32 m0, %[l0], 65536\n" \
  "s_nop 0\n" \
  "global_load_lds_dwordx4 v0, s[36:37] offset:0\n" \
  "global_load_lds_dwordx4 v1, s[36:37] offset:1024\n" \
  "global_load_lds_dwordx4 v2, s[36:37] offset:2048\n" \
  "global_load_lds_dwordx4 v3, s[36:37] offset:3072\n" \
  "s_add_u32 s36, s36, 128\n" \
  "s_addc_u32 s37, s37, 0\n" \
  "s_add_u32 m0, %[l0], 114688\n" \
  "s_nop 0\n" \
  "global_load_lds_dwordx4 v4, s[38:39] offset:0\n" \
  "global_load_lds_dwordx4 v5, s[38:39] offset:1024\n" \
  "global_load_lds_dwordx4 v6, s[38:39] offset:2048\n" \
  "global_load_lds_dwordx4 v7, s[38:39] offset:3072\n" \
  "s_add_u32 s38, s38, 245760\n" \
  "s_addc_u32 s39, s39, 0\n" \
  "s_waitcnt vmcnt(12)\n" \
  "s_barrier\n" \
  "s_add_u32 m0, %[l0], 98304\n" \
  "s_nop 0\n" \
  "global_load_lds_dwordx4 v0, s[36:37] offset:0\n" \
  "global_load_lds_dwordx4 v1, s[36:37] offset:1024\n" \
  "global_load_lds_dwordx4 v2, s[36:37] offset:2048\n" \
  "global_load_lds_dwordx4 v3, s[36:37] offset:3072\n" \
  "s_add_u32 s36, s36, 128\n" \
  "s_addc_u32 s37, s37, 0\n" \
  "s_add_u32 m0, %[l0], 16384\n" \
  "s_nop 0\n" \
  "global_load_lds_dwordx4 v4, s[38:39] offset:0\n" \
  "global_load_lds_dwordx4 v5, s[38:39] offset:1024\n" \
  "global_load_lds_dwordx4 v6, s[38:39] offset:2048\n" \
  "global_load_lds_dwordx4 v7, s[38:39] offset:3072\n" \
  "s_add_u32 s38, s38, 245760\n" \
  "s_addc_u32 s39, s39, 0\n" \
  "s_waitcnt vmcnt(12)\n" \
  "s_barrier\n" \
  "s_add_u32 m0, %[l0], 0\n" \
  "s_nop 0\n" \
  "global_load_lds_dwordx4 v0, s[36:37] offset:0\n" \
  "global_load_lds_dwordx4 v1, s[36:37] offset:1024\n" \
  "global_load_lds_dwordx4 v2, s[36:37] offset:2048\n" \
  "global_load_lds_dwordx4 v3, s[36:37] offset:3072\n" \
  "s_add_u32 s36, s36, 128\n" \
  "s_addc_u32 s37, s37, 0\n" \
  "s_add_u32 m0, %[l0], 49152\n" \
  "s_nop 0\n" \
  "global_load_lds_dwordx4 v4, s[38:39] offset:0\n" \
  "global_load_lds_dwordx4 v5, s[38:39] offset:1024\n" \
  "global_load_lds_dwordx4 v6, s[38:39] offset:2048\n" \
  "global_load_lds_dwordx4 v7, s[38:39] offset:3072\n" \
  "s_add_u32 s38, s38, 245760\n" \
  "s_addc_u32 s39, s39, 0\n" \
  "s_waitcnt vmcnt(12)\n" \
  "s_barrier\n" \
  "s_add_u32 m0, %[l0], 32768\n" \
  "s_nop 0\n" \
  "global_load_lds_dwordx4 v0, s[36:37] offset:0\n" \
  "global_load_lds_dwordx4 v1, s[36:37] offset:1024\n" \
  "global_load_lds_dwordx4 v2, s[36:37] offset:2048\n" \
  "global_load_lds_dwordx4 v3, s[36:37] offset:3072\n" \
  "s_add_u32 s36, s36, 128\n" \
  "s_addc_u32 s37, s37, 0\n" \
  "s_waitcnt vmcnt(8)\n" \
  "s_barrier\n" \
  "global_load_dword v16, v10, %[ws] sc1\n" \
  "global_load_dword v14, v9, %[ws] sc1\n" \
  "global_load_dwordx2 v[12:13], v8, %[ws] sc1\n" \
  "s_waitcnt vmcnt(3)\n" \
  "s_barrier\n" \
  "s_waitcnt vmcnt(0)\n" \
  "s_branch 7f\n" \
  "6:\n" \
  "s_mov_b64 exec, 0xfffff\n" \
  "global_load_dword v20, v18, %[wq]\n" \
  "s_mov_b64 exec, -1\n" \
  "s_mov_b64 exec, 0xfffff\n" \
  "global_load_dword v20, v19, %[x]\n" \
  "s_mov_b64 exec, -1\n" \
  "global_load_dword v16, v10, %[ws] sc1\n" \
  "s_waitcnt vmcnt(0)\n" \
  "v_add_u32 v12, 1, v16\n" \
  "v_lshlrev_b32 v13, 8, v12\n" \
  "s_mov_b64 exec, 0xff\n" \
  "global_store_dword v11, v13, %[ws] sc1\n" \
  "s_mov_b64 exec, 1\n" \
  "s_waitcnt vmcnt(0)\n" \
  "global_store_dword v9, v12, %[ws] sc1\n" \
  "s_mov_b64 exec, -1\n" \
  "7:\n" \
  "s_mov_b32 m0, %[sm0]\n" \
  "s_waitcnt lgkmcnt(0)\n"
#define HELPER_ASM \
  "s_mov_b32 %[sm0], m0\n" \
  "v_add_u32 v22, 4096, v8\n" \
  "v_mov_b32 v192, 0x3f803f80\n" \
  "v_mov_b32 v193, 0x3f803f80\n" \
  "v_mov_b32 v194, 0x3f803f80\n" \
  "v_mov_b32 v195, 0x3f803f80\n" \
  "v_mov_b32 v25, 0\n" \
  "s_mov_b32 %[t0], 0x40000\n" \
  "s_mov_b64 exec, %[pm]\n" \
  "global_atomic_add v26, v16, v25, s[48:49] sc0 sc1\n" \
  "s_sleep 6\n" \
  "global_atomic_add v27, v16, v25, s[48:49] sc0 sc1\n" \
  "9:\n" \
  "s_waitcnt vmcnt(1)\n" \
  "v_cmp_ne_u32 vcc, v20, v26\n" \
  "s_cbranch_vccz 8f\n" \
  "global_atomic_add v26, v16, v25, s[48:49] sc0 sc1\n" \
  "s_waitcnt vmcnt(1)\n" \
  "v_cmp_ne_u32 vcc, v20, v27\n" \
  "s_cbranch_vccz 8f\n" \
  "global_atomic_add v27, v16, v25, s[48:49] sc0 sc1\n" \
  "s_sub_u32 %[t0], %[t0], 1\n" \
  "s_cbranch_scc0 9b\n" \
  "8:\n" \
  "s_mov_b64 exec, -1\n" \
  "s_cmp_lt_u32 %[hw], 2\n" \
  "s_cbranch_scc1 2f\n" \
  "s_mul_i32 %[t0], %[hw], 6144\n" \
  "s_add_u32 m0, %[t0], 86016\n" \
  "s_nop 0\n" \
  "global_load_lds_dwordx4 v0, s[58:59] sc1\n" \
  "global_load_lds_dwordx4 v14, s[58:59] offset:1024 sc1\n" \
  "s_add_u32 s58, s58, 1024\n" \
  "s_addc_u32 s59, s59, 0\n" \
  "global_load_lds_dwordx4 v0, s[58:59] offset:2048 sc1\n" \
  "s_add_u32 s58, s58, 4096\n" \
  "s_addc_u32 s59, s59, 0\n" \
  "s_mul_i32 %[t0], %[hw], 6144\n" \
  "s_add_u32 m0, %[t0], 89088\n" \
  "s_nop 0\n" \
  "global_load_lds_dwordx4 v0, s[58:59] sc1\n" \
  "global_load_lds_dwordx4 v14, s[58:59] offset:1024 sc1\n" \
  "s_add_u32 s58, s58, 1024\n" \
  "s_addc_u32 s59, s59, 0\n" \
  "global_load_lds_dwordx4 v0, s[58:59] offset:2048 sc1\n" \
  "s_add_u32 s58, s58, 4096\n" \
  "s_addc_u32 s59, s59, 0\n" \
  "s_add_u32 s36, s36, 81920\n" \
  "s_addc_u32 s37, s37, 0\n" \
  "s_branch 3f\n" \
  "2:\n" \
  "s_mul_i32 %[t0], %[hw], 2880\n" \
  "s_add_u32 m0, %[t0], 0\n" \
  "s_nop 0\n" \
  "global_load_lds_dwordx4 v0, s[36:37] sc1\n" \
  "global_load_lds_dwordx4 v1, s[36:37] offset:1024 sc1\n" \
  "s_mov_b32 exec_lo, 0xfffffcff\n" \
  "s_mov_b32 exec_hi, 0x3ffff\n" \
  "global_load_lds_dwordx4 v2, s[36:37] offset:2048 sc1\n" \
  "s_mov_b64 exec, -1\n" \
  "s_add_u32 s36, s36, 40960\n" \
  "s_addc_u32 s37, s37, 0\n" \
  "s_mul_i32 %[t0], %[hw], 2880\n" \
  "s_add_u32 m0, %[t0], 5760\n" \
  "s_nop 0\n" \
  "global_load_lds_dwordx4 v0, s[36:37] sc1\n" \
  "global_load_lds_dwordx4 v1, s[36:37] offset:1024 sc1\n" \
  "s_mov_b32 exec_lo, 0xfffffcff\n" \
  "s_mov_b32 exec_hi, 0x3ffff\n" \
  "global_load_lds_dwordx4 v2, s[36:37] offset:2048 sc1\n" \
  "s_mov_b64 exec, -1\n" \
  "s_add_u32 s36, s36, 40960\n" \
  "s_addc_u32 s37, s37, 0\n" \
  "3:\n" \
  "s_waitcnt vmcnt(0) lgkmcnt(0)\n" \
  "s_barrier\n" \
  "s_mul_i32 %[t0], %[hw], 2880\n" \
  "s_add_u32 m0, %[t0], 11520\n" \
  "s_nop 0\n" \
  "global_load_lds_dwordx4 v0, s[36:37] sc1\n" \
  "global_load_lds_dwordx4 v1, s[36:37] offset:1024 sc1\n" \
  "s_mov_b32 exec_lo, 0xfffffcff\n" \
  "s_mov_b32 exec_hi, 0x3ffff\n" \
  "global_load_lds_dwordx4 v2, s[36:37] offset:2048 sc1\n" \
  "s_mov_b64 exec, -1\n" \
  "s_add_u32 s36, s36, 81920\n" \
  "s_addc_u32 s37, s37, 0\n" \
  "s_mul_i32 %[t0], %[hw], 2880\n" \
  "s_add_u32 m0, %[t0], 23040\n" \
  "s_nop 0\n" \
  "global_load_lds_dwordx4 v0, s[36:37] sc1\n" \
  "global_load_lds_dwordx4 v1, s[36:37] offset:1024 sc1\n" \
  "s_mov_b32 exec_lo, 0xfffffcff\n" \
  "s_mov_b32 exec_hi, 0x3ffff\n" \
  "global_load_lds_dwordx4 v2, s[36:37] offset:2048 sc1\n" \
  "s_mov_b64 exec, -1\n" \
  "s_add_u32 s36, s36, 81920\n" \
  "s_addc_u32 s37, s37, 0\n" \
  "s_mul_i32 %[t0], %[hw], 2880\n" \
  "s_add_u32 m0, %[t0], 34560\n" \
  "s_nop 0\n" \
  "global_load_lds_dwordx4 v0, s[36:37] sc1\n" \
  "global_load_lds_dwordx4 v1, s[36:37] offset:1024 sc1\n" \
  "s_mov_b32 exec_lo, 0xfffffcff\n" \
  "s_mov_b32 exec_hi, 0x3ffff\n" \
  "global_load_lds_dwordx4 v2, s[36:37] offset:2048 sc1\n" \
  "s_mov_b64 exec, -1\n" \
  "s_sub_u32 s36, s36, 240640\n" \
  "s_subb_u32 s37, s37, 0\n" \
  "s_waitcnt vmcnt(6)\n" \
  "s_barrier\n" \
  "s_cmp_lg_u32 %[hw], 1\n" \
  "s_cbranch_scc1 8f\n" \
  "s_add_u32 s58, s48, 32768\n" \
  "s_addc_u32 s59, s49, 0\n" \
  "s_mov_b32 %[t0], 0x40000\n" \
  "s_mov_b64 exec, %[pm]\n" \
  "9:\n" \
  "global_atomic_add v24, v16, v30, s[58:59] sc0 sc1\n" \
  "s_waitcnt vmcnt(0)\n" \
  "v_cmp_ne_u32 vcc, v20, v24\n" \
  "s_cbranch_vccz 8f\n" \
  "s_sub_u32 %[t0], %[t0], 1\n" \
  "s_cbranch_scc0 9b\n" \
  "8:\n" \
  "s_mov_b64 exec, -1\n" \
  "s_mul_i32 %[t0], %[hw], 4096\n" \
  "s_add_u32 m0, %[t0], 49152\n" \
  "s_nop 0\n" \
  "global_load_lds_dwordx4 v9, s[46:47] offset:0\n" \
  "s_add_u32 s46, s46, %[rinc]\n" \
  "s_addc_u32 s47, s47, 0\n" \
  "s_mul_i32 %[t0], %[hw], 4096\n" \
  "s_add_u32 m0, %[t0], 49152\n" \
  "s_nop 0\n" \
  "global_load_lds_dwordx4 v9, s[46:47] offset:1024\n" \
  "s_add_u32 s46, s46, %[rinc]\n" \
  "s_addc_u32 s47, s47, 0\n" \
  "s_cmp_lg_u32 %[hw], 3\n" \
  "s_cbranch_scc1 2f\n" \
  "s_mov_b64 exec, 0\n" \
  "s_branch .Lk_pad0\n" \
  ".Lk_ret0:\n" \
  "s_mov_b64 exec, -1\n" \
  "s_mov_b64 exec, 0\n" \
  "s_branch .Lk_pad1\n" \
  ".Lk_ret1:\n" \
  "s_mov_b64 exec, -1\n" \
  "2:\n" \
  "s_waitcnt vmcnt(5)\n" \
  "s_barrier\n" \
  "s_mul_i32 %[t0], %[hw], 2880\n" \
  "s_add_u32 m0, %[t0], 0\n" \
  "s_nop 0\n" \
  "global_load_lds_dwordx4 v0, s[36:37] sc1\n" \
  "global_load_lds_dwordx4 v1, s[36:37] offset:1024 sc1\n" \
  "s_mov_b32 exec_lo, 0xfffffcff\n" \
  "s_mov_b32 exec_hi, 0x3ffff\n" \
  "global_load_lds_dwordx4 v2, s[36:37] offset:2048 sc1\n" \
  "s_mov_b64 exec, -1\n" \
  "s_add_u32 s36, s36, 81920\n" \
  "s_addc_u32 s37, s37, 0\n" \
  "s_mul_i32 %[t0], %[hw], 4096\n" \
  "s_add_u32 m0, %[t0], 49152\n" \
  "s_nop 0\n" \
  "global_load_lds_dwordx4 v9, s[46:47] offset:2048\n" \
  "s_add_u32 s46, s46, %[rinc]\n" \
  "s_addc_u32 s47, s47, 0\n" \
  "s_mul_i32 %[t0], %[hw], 4096\n" \
  "s_add_u32 m0, %[t0], 49152\n" \
  "s_nop 0\n" \
  "global_load_lds_dwordx4 v9, s[46:47] offset:3072\n" \
  "s_cmp_ge_u32 %[hw], 2\n" \
  "s_cbranch_scc1 3f\n" \
  "s_cmp_eq_u32 %[hw], 1\n" \
  "s_cbranch_scc1 2f\n" \
  "s_mov_b32 m0, 65536\n" \
  "s_nop 0\n" \
  "global_load_lds_dwordx4 v10, s[52:53] offset:0\n" \
  "s_add_u32 s52, s52, 9216\n" \
  "s_addc_u32 s53, s53, 0\n" \
  "global_load_lds_dwordx4 v10, s[52:53] offset:1024\n" \
  "s_add_u32 s52, s52, 9216\n" \
  "s_addc_u32 s53, s53, 0\n" \
  "global_load_lds_dwordx4 v10, s[52:53] offset:2048\n" \
  "s_add_u32 s52, s52, 9216\n" \
  "s_addc_u32 s53, s53, 0\n" \
  "global_load_lds_dwordx4 v10, s[52:53] offset:3072\n" \
  "s_mov_b64 exec, 1\n" \
  "global_load_dword v15, v8, s[40:41]\n" \
  "s_mov_b64 exec, -1\n" \
  "s_branch 4f\n" \
  "2:\n" \
  "s_mov_b64 exec, 1\n" \
  "global_load_dword v15, v8, s[40:41]\n" \
  "global_load_dword v15, v8, s[40:41]\n" \
  "global_load_dword v15, v8, s[40:41]\n" \
  "global_load_dword v15, v8, s[40:41]\n" \
  "global_load_dword v15, v8, s[40:41]\n" \
  "s_mov_b64 exec, -1\n" \
  "s_branch 4f\n" \
  "3:\n" \
  "s_mul_i32 %[t0], %[hw], 5120\n" \
  "s_add_u32 m0, %[t0], 141312\n" \
  "s_nop 0\n" \
  "global_load_lds_dwordx4 v3, s[40:41] offset:0\n" \
  "global_load_lds_dwordx4 v4, s[40:41] offset:1024\n" \
  "global_load_lds_dwordx4 v5, s[40:41] offset:2048\n" \
  "global_load_lds_dwordx4 v6, s[40:41] offset:3072\n" \
  "s_add_u32 m0, m0, 4096\n" \
  "s_nop 0\n" \
  "global_load_lds_dwordx4 v7, s[40:41]\n" \
  "4:\n" \
  "s_waitcnt vmcnt(12)\n" \
  "s_barrier\n" \
  "s_mul_i32 %[t0], %[hw], 2880\n" \
  "s_add_u32 m0, %[t0], 11520\n" \
  "s_nop 0\n" \
  "global_load_lds_dwordx4 v0, s[36:37] sc1\n" \
  "global_load_lds_dwordx4 v1, s[36:37] offset:1024 sc1\n" \
  "s_mov_b32 exec_lo, 0xfffffcff\n" \
  "s_mov_b32 exec_hi, 0x3ffff\n" \
  "global_load_lds_dwordx4 v2, s[36:37] offset:2048 sc1\n" \
  "s_mov_b64 exec, -1\n" \
  "s_add_u32 s36, s36, 81920\n" \
  "s_addc_u32 s37, s37, 0\n" \
  "s_waitcnt vmcnt(10)\n" \
  "s_barrier\n" \
  "s_mul_i32 %[t0], %[hw], 2880\n" \
  "s_add_u32 m0, %[t0], 23040\n" \
  "s_nop 0\n" \
  "global_load_lds_dwordx4 v0, s[36:37] sc1\n" \
  "global_load_lds_dwordx4 v1, s[36:37] offset:1024 sc1\n" \
  "s_mov_b32 exec_lo, 0xfffffcff\n" \
  "s_mov_b32 exec_hi, 0x3ffff\n" \
  "global_load_lds_dwordx4 v2, s[36:37] offset:2048 sc1\n" \
  "s_mov_b64 exec, -1\n" \
  "s_add_u32 s36, s36, 81920\n" \
  "s_addc_u32 s37, s37, 0\n" \
  "s_waitcnt vmcnt(6)\n" \
  "ds_read_b64_tr_b16 v[212:213], v11 offset:0\n" \
  "ds_read_b64_tr_b16 v[214:215], v11 offset:512\n" \
  "ds_read_b64_tr_b16 v[216:217], v11 offset:32\n" \
  "ds_read_b64_tr_b16 v[218:219], v11 offset:544\n" \
  "ds_read_b64_tr_b16 v[220:221], v11 offset:64\n" \
  "ds_read_b64_tr_b16 v[222:223], v11 offset:576\n" \
  "ds_read_b64_tr_b16 v[224:225], v11 offset:96\n" \
  "ds_read_b64_tr_b16 v[226:227], v11 offset:608\n" \
  "s_waitcnt lgkmcnt(0)\n" \
  "global_store_dwordx4 v8, v[212:215], s[54:55] offset:0 sc1\n" \
  "global_store_dwordx4 v8, v[216:219], s[54:55] offset:1024 sc1\n" \
  "global_store_dwordx4 v8, v[220:223], s[54:55] offset:2048 sc1\n" \
  "global_store_dwordx4 v8, v[224:227], s[54:55] offset:3072 sc1\n" \
  "s_cmp_lg_u32 %[hw], 0\n" \
  "s_cbranch_scc1 2f\n" \
  "ds_read_b64_tr_b16 v[228:229], v11 offset:16384\n" \
  "ds_read_b64_tr_b16 v[230:231], v11 offset:16896\n" \
  "ds_read_b64_tr_b16 v[232:233], v11 offset:16416\n" \
  "ds_read_b64_tr_b16 v[234:235], v11 offset:16928\n" \
  "ds_read_b64_tr_b16 v[236:237], v11 offset:16448\n" \
  "ds_read_b64_tr_b16 v[238:239], v11 offset:16960\n" \
  "ds_read_b64_tr_b16 v[240:241], v11 offset:16480\n" \
  "ds_read_b64_tr_b16 v[242:243], v11 offset:16992\n" \
  "s_waitcnt lgkmcnt(0)\n" \
  "global_store_dwordx4 v8, v[228:231], s[56:57] offset:0 sc1\n" \
  "global_store_dwordx4 v8, v[232:235], s[56:57] offset:1024 sc1\n" \
  "global_store_dwordx4 v8, v[236:239], s[56:57] offset:2048 sc1\n" \
  "global_store_dwordx4 v8, v[240:243], s[56:57] offset:3072 sc1\n" \
  "2:\n" \
  "s_cmp_eq_u32 %[rinc], 9216\n" \
  "s_cbranch_scc1 2f\n" \
  "v_mfma_f32_16x16x32_bf16 v[196:199], v[212:215], v[192:195], 0\n" \
  "v_mfma_f32_16x16x32_bf16 v[200:203], v[216:219], v[192:195], 0\n" \
  "v_mfma_f32_16x16x32_bf16 v[204:207], v[220:223], v[192:195], 0\n" \
  "v_mfma_f32_16x16x32_bf16 v[208:211], v[224:227], v[192:195], 0\n" \
  "s_mov_b32 exec_lo, 0x10001\n" \
  "s_mov_b32 exec_hi, 0x10001\n" \
  "s_nop 5\n" \
  "global_store_dwordx4 v29, v[196:199], s[48:49] offset:0 sc1\n" \
  "global_store_dwordx4 v29, v[200:203], s[48:49] offset:64 sc1\n" \
  "global_store_dwordx4 v29, v[204:207], s[48:49] offset:128 sc1\n" \
  "global_store_dwordx4 v29, v[208:211], s[48:49] offset:192 sc1\n" \
  "s_mov_b64 exec, -1\n" \
  "2:\n" \
  "s_mov_b64 exec, 7\n" \
  "global_store_dword v21, v30, s[48:49] sc1\n" \
  "s_mov_b64 exec, -1\n" \
  "s_cmp_lg_u32 %[hw], 3\n" \
  "s_cbranch_scc1 2f\n" \
  "s_mov_b64 exec, 0\n" \
  "s_branch .Lk_pad2\n" \
  ".Lk_ret2:\n" \
  "s_mov_b64 exec, -1\n" \
  "s_mov_b64 exec, 0\n" \
  "s_branch .Lk_pad3\n" \
  ".Lk_ret3:\n" \
  "s_mov_b64 exec, -1\n" \
  "2:\n" \
  "s_waitcnt vmcnt(8)\n" \
  "s_barrier\n" \
  "s_waitcnt vmcnt(0)\n" \
  "s_mov_b64 exec, 1\n" \
  "global_store_dword v17, v13, s[48:49] sc1\n" \
  "s_mov_b64 exec, -1\n" \
  "s_mul_i32 %[t0], %[hw], 2880\n" \
  "s_add_u32 m0, %[t0], 34560\n" \
  "s_nop 0\n" \
  "global_load_lds_dwordx4 v0, s[36:37] sc1\n" \
  "global_load_lds_dwordx4 v1, s[36:37] offset:1024 sc1\n" \
  "s_mov_b32 exec_lo, 0xfffffcff\n" \
  "s_mov_b32 exec_hi, 0x3ffff\n" \
  "global_load_lds_dwordx4 v2, s[36:37] offset:2048 sc1\n" \
  "s_mov_b64 exec, -1\n" \
  "s_sub_u32 s36, s36, 240640\n" \
  "s_subb_u32 s37, s37, 0\n" \
  "s_barrier\n" \
  "s_mul_i32 %[t0], %[hw], 2880\n" \
  "s_add_u32 m0, %[t0], 0\n" \
  "s_nop 0\n" \
  "global_load_lds_dwordx4 v0, s[36:37] sc1\n" \
  "global_load_lds_dwordx4 v1, s[36:37] offset:1024 sc1\n" \
  "s_mov_b32 exec_lo, 0xfffffcff\n" \
  "s_mov_b32 exec_hi, 0x3ffff\n" \
  "global_load_lds_dwordx4 v2, s[36:37] offset:2048 sc1\n" \
  "s_mov_b64 exec, -1\n" \
  "s_add_u32 s36, s36, 81920\n" \
  "s_addc_u32 s37, s37, 0\n" \
  "s_waitcnt vmcnt(3)\n" \
  "s_barrier\n" \
  "s_mov_b32 %[t0], 0x40000\n" \
  "9:\n" \
  "global_load_dwordx4 v[24:27], v18, s[48:49] sc1\n" \
  "s_sub_u32 %[t0], %[t0], 1\n" \
  "s_cbranch_scc1 8f\n" \
  "s_waitcnt vmcnt(0)\n" \
  "v_cmp_ne_u64 vcc, v[24:25], v[12:13]\n" \
  "v_cmp_ne_u64 s[46:47], v[26:27], v[12:13]\n" \
  "s_nop 0\n" \
  "s_or_b64 vcc, vcc, s[46:47]\n" \
  "s_cbranch_scc1 9b\n" \
  "8:\n" \
  "s_mul_i32 %[t0], %[hw], 2880\n" \
  "s_add_u32 m0, %[t0], 11520\n" \
  "s_nop 0\n" \
  "global_load_lds_dwordx4 v0, s[36:37] sc1\n" \
  "global_load_lds_dwordx4 v1, s[36:37] offset:1024 sc1\n" \
  "s_mov_b32 exec_lo, 0xfffffcff\n" \
  "s_mov_b32 exec_hi, 0x3ffff\n" \
  "global_load_lds_dwordx4 v2, s[36:37] offset:2048 sc1\n" \
  "s_mov_b64 exec, -1\n" \
  "s_add_u32 s36, s36, 81920\n" \
  "s_addc_u32 s37, s37, 0\n" \
  "s_barrier\n" \
  "s_mul_i32 %[t0], %[hw], 2880\n" \
  "s_add_u32 m0, %[t0], 23040\n" \
  "s_nop 0\n" \
  "global_load_lds_dwordx4 v0, s[36:37] sc1\n" \
  "global_load_lds_dwordx4 v1, s[36:37] offset:1024 sc1\n" \
  "s_mov_b32 exec_lo, 0xfffffcff\n" \
  "s_mov_b32 exec_hi, 0x3ffff\n" \
  "global_load_lds_dwordx4 v2, s[36:37] offset:2048 sc1\n" \
  "s_mov_b64 exec, -1\n" \
  "s_add_u32 s36, s36, 81920\n" \
  "s_addc_u32 s37, s37, 0\n" \
  "s_cmp_lg_u32 %[u1], 1023\n" \
  "s_cbranch_scc1 2f\n" \
  "s_mov_b64 exec, 1\n" \
  "global_store_dword v30, v13, s[48:49] sc1\n" \
  "s_mov_b64 exec, -1\n" \
  "2:\n" \
  "s_mov_b64 s[46:47], s[48:49]\n" \
  "global_load_dwordx4 v[4:7], v31, s[46:47] sc1\n" \
  "s_add_u32 s46, s46, 55296\n" \
  "s_addc_u32 s47, s47, 0\n" \
  "global_load_dwordx4 v[24:27], v31, s[46:47] sc1\n" \
  "s_add_u32 s46, s46, 55296\n" \
  "s_addc_u32 s47, s47, 0\n" \
  "s_mov_b32 exec_hi, 0\n" \
  "global_load_dwordx4 v[28:31], v31, s[46:47] sc1\n" \
  "s_add_u32 s46, s46, 55296\n" \
  "s_addc_u32 s47, s47, 0\n" \
  "s_mov_b64 exec, -1\n" \
  "s_mul_i32 %[t0], %[hw], 5120\n" \
  "s_add_u32 m0, %[t0], 49152\n" \
  "s_nop 0\n" \
  "global_load_lds_dwordx4 v8, s[38:39] offset:0 sc1\n" \
  "global_load_lds_dwordx4 v8, s[38:39] offset:1024 sc1\n" \
  "global_load_lds_dwordx4 v8, s[38:39] offset:2048 sc1\n" \
  "global_load_lds_dwordx4 v8, s[38:39] offset:3072 sc1\n" \
  "s_add_u32 m0, m0, 4096\n" \
  "s_nop 0\n" \
  "global_load_lds_dwordx4 v22, s[38:39] sc1\n" \
  "s_add_u32 s38, s38, 163840\n" \
  "s_addc_u32 s39, s39, 0\n" \
  "s_mul_i32 %[t0], %[hw], 5120\n" \
  "s_add_u32 m0, %[t0], 69632\n" \
  "s_nop 0\n" \
  "global_load_lds_dwordx4 v8, s[38:39] offset:0 sc1\n" \
  "global_load_lds_dwordx4 v8, s[38:39] offset:1024 sc1\n" \
  "global_load_lds_dwordx4 v8, s[38:39] offset:2048 sc1\n" \
  "global_load_lds_dwordx4 v8, s[38:39] offset:3072 sc1\n" \
  "s_add_u32 m0, m0, 4096\n" \
  "s_nop 0\n" \
  "global_load_lds_dwordx4 v22, s[38:39] sc1\n" \
  "s_add_u32 s38, s38, 163840\n" \
  "s_addc_u32 s39, s39, 0\n" \
  "s_mul_i32 %[t0], %[hw], 5120\n" \
  "s_add_u32 m0, %[t0], 90112\n" \
  "s_nop 0\n" \
  "global_load_lds_dwordx4 v8, s[38:39] offset:0 sc1\n" \
  "global_load_lds_dwordx4 v8, s[38:39] offset:1024 sc1\n" \
  "global_load_lds_dwordx4 v8, s[38:39] offset:2048 sc1\n" \
  "global_load_lds_dwordx4 v8, s[38:39] offset:3072 sc1\n" \
  "s_add_u32 m0, m0, 4096\n" \
  "s_nop 0\n" \
  "global_load_lds_dwordx4 v22, s[38:39] sc1\n" \
  "s_add_u32 s38, s38, 163840\n" \
  "s_addc_u32 s39, s39, 0\n" \
  "s_waitcnt vmcnt(21)\n" \
  "s_barrier\n" \
  "s_mul_i32 %[t0], %[hw], 2880\n" \
  "s_add_u32 m0, %[t0], 34560\n" \
  "s_nop 0\n" \
  "global_load_lds_dwordx4 v0, s[36:37] sc1\n" \
  "global_load_lds_dwordx4 v1, s[36:37] offset:1024 sc1\n" \
  "s_mov_b32 exec_lo, 0xfffffcff\n" \
  "s_mov_b32 exec_hi, 0x3ffff\n" \
  "global_load_lds_dwordx4 v2, s[36:37] offset:2048 sc1\n" \
  "s_mov_b64 exec, -1\n" \
  "s_sub_u32 s36, s36, 240640\n" \
  "s_subb_u32 s37, s37, 0\n" \
  "s_mul_i32 %[t0], %[hw], 5120\n" \
  "s_add_u32 m0, %[t0], 110592\n" \
  "s_nop 0\n" \
  "global_load_lds_dwordx4 v8, s[38:39] offset:0 sc1\n" \
  "global_load_lds_dwordx4 v8, s[38:39] offset:1024 sc1\n" \
  "global_load_lds_dwordx4 v8, s[38:39] offset:2048 sc1\n" \
  "global_load_lds_dwordx4 v8, s[38:39] offset:3072 sc1\n" \
  "s_add_u32 m0, m0, 4096\n" \
  "s_nop 0\n" \
  "global_load_lds_dwordx4 v22, s[38:39] sc1\n" \
  "s_add_u32 s38, s38, 163840\n" \
  "s_addc_u32 s39, s39, 0\n" \
  "s_mul_i32 %[t0], %[hw], 5120\n" \
  "s_add_u32 m0, %[t0], 131072\n" \
  "s_nop 0\n" \
  "global_load_lds_dwordx4 v8, s[38:39] offset:0 sc1\n" \
  "global_load_lds_dwordx4 v8, s[38:39] offset:1024 sc1\n" \
  "global_load_lds_dwordx4 v8, s[38:39] offset:2048 sc1\n" \
  "global_load_lds_dwordx4 v8, s[38:39] offset:3072 sc1\n" \
  "s_add_u32 m0, m0, 4096\n" \
  "s_nop 0\n" \
  "global_load_lds_dwordx4 v22, s[38:39] sc1\n" \
  "s_add_u32 s38, s38, 163840\n" \
  "s_addc_u32 s39, s39, 0\n" \
  "global_load_dwordx4 v[32:35], v8, s[42:43] offset:0 sc1\n" \
  "global_load_dwordx4 v[36:39], v8, s[42:43] offset:1024 sc1\n" \
  "global_load_dwordx4 v[40:43], v8, s[42:43] offset:2048 sc1\n" \
  "global_load_dwordx4 v[44:47], v8, s[42:43] offset:3072 sc1\n" \
  "global_load_dwordx4 v[48:51], v22, s[42:43] offset:0 sc1\n" \
  "global_load_dwordx4 v[52:55], v22, s[42:43] offset:1024 sc1\n" \
  "global_load_dwordx4 v[56:59], v22, s[42:43] offset:2048 sc1\n" \
  "s_add_u32 s42, s42, 442368\n" \
  "s_addc_u32 s43, s43, 0\n" \
  "s_waitcnt vmcnt(38)\n" \
  "s_barrier\n" \
  "s_mul_i32 %[t0], %[hw], 2880\n" \
  "s_add_u32 m0, %[t0], 0\n" \
  "s_nop 0\n" \
  "global_load_lds_dwordx4 v0, s[36:37] sc1\n" \
  "global_load_lds_dwordx4 v1, s[36:37] offset:1024 sc1\n" \
  "s_mov_b32 exec_lo, 0xfffffcff\n" \
  "s_mov_b32 exec_hi, 0x3ffff\n" \
  "global_load_lds_dwordx4 v2, s[36:37] offset:2048 sc1\n" \
  "s_mov_b64 exec, -1\n" \
  "s_add_u32 s36, s36, 81920\n" \
  "s_addc_u32 s37, s37, 0\n" \
  "global_load_dwordx4 v[60:63], v8, s[42:43] offset:0 sc1\n" \
  "global_load_dwordx4 v[64:67], v8, s[42:43] offset:1024 sc1\n" \
  "global_load_dwordx4 v[68:71], v8, s[42:43] offset:2048 sc1\n" \
  "global_load_dwordx4 v[72:75], v8, s[42:43] offset:3072 sc1\n" \
  "global_load_dwordx4 v[76:79], v22, s[42:43] offset:0 sc1\n" \
  "global_load_dwordx4 v[80:83], v22, s[42:43] offset:1024 sc1\n" \
  "global_load_dwordx4 v[84:87], v22, s[42:43] offset:2048 sc1\n" \
  "s_add_u32 s42, s42, 442368\n" \
  "s_addc_u32 s43, s43, 0\n" \
  "global_load_dwordx4 v[88:91], v8, s[44:45] offset:0 sc1\n" \
  "global_load_dwordx4 v[92:95], v8, s[44:45] offset:1024 sc1\n" \
  "global_load_dwordx4 v[96:99], v8, s[44:45] offset:2048 sc1\n" \
  "global_load_dwordx4 v[100:103], v8, s[44:45] offset:3072 sc1\n" \
  "global_load_dwordx4 v[104:107], v22, s[44:45] offset:0 sc1\n" \
  "s_add_u32 s44, s44, 163840\n" \
  "s_addc_u32 s45, s45, 0\n" \
  "global_load_dwordx4 v[108:111], v8, s[44:45] offset:0 sc1\n" \
  "global_load_dwordx4 v[112:115], v8, s[44:45] offset:1024 sc1\n" \
  "global_load_dwordx4 v[116:119], v8, s[44:45] offset:2048 sc1\n" \
  "global_load_dwordx4 v[120:123], v8, s[44:45] offset:3072 sc1\n" \
  "global_load_dwordx4 v[124:127], v22, s[44:45] offset:0 sc1\n" \
  "s_add_u32 s44, s44, 163840\n" \
  "s_addc_u32 s45, s45, 0\n" \
  "s_waitcnt vmcnt(37)\n" \
  "s_barrier\n" \
  "s_mul_i32 %[t0], %[hw], 2880\n" \
  "s_add_u32 m0, %[t0], 11520\n" \
  "s_nop 0\n" \
  "global_load_lds_dwordx4 v0, s[36:37] sc1\n" \
  "global_load_lds_dwordx4 v1, s[36:37] offset:1024 sc1\n" \
  "s_mov_b32 exec_lo, 0xfffffcff\n" \
  "s_mov_b32 exec_hi, 0x3ffff\n" \
  "global_load_lds_dwordx4 v2, s[36:37] offset:2048 sc1\n" \
  "s_mov_b64 exec, -1\n" \
  "s_add_u32 s36, s36, 81920\n" \
  "s_addc_u32 s37, s37, 0\n" \
  "global_load_dwordx4 v[128:131], v8, s[44:45] offset:0 sc1\n" \
  "global_load_dwordx4 v[132:135], v8, s[44:45] offset:1024 sc1\n" \
  "global_load_dwordx4 v[136:139], v8, s[44:45] offset:2048 sc1\n" \
  "global_load_dwordx4 v[140:143], v8, s[44:45] offset:3072 sc1\n" \
  "global_load_dwordx4 v[144:147], v22, s[44:45] offset:0 sc1\n" \
  "s_add_u32 s44, s44, 163840\n" \
  "s_addc_u32 s45, s45, 0\n" \
  "global_load_dwordx4 v[148:151], v8, s[44:45] offset:0 sc1\n" \
  "global_load_dwordx4 v[152:155], v8, s[44:45] offset:1024 sc1\n" \
  "global_load_dwordx4 v[156:159], v8, s[44:45] offset:2048 sc1\n" \
  "global_load_dwordx4 v[160:163], v8, s[44:45] offset:3072 sc1\n" \
  "global_load_dwordx4 v[164:167], v22, s[44:45] offset:0 sc1\n" \
  "s_add_u32 s44, s44, 163840\n" \
  "s_addc_u32 s45, s45, 0\n" \
  "global_load_dwordx4 v[168:171], v8, s[44:45] offset:0 sc1\n" \
  "global_load_dwordx4 v[172:175], v8, s[44:45] offset:1024 sc1\n" \
  "global_load_dwordx4 v[176:179], v8, s[44:45] offset:2048 sc1\n" \
  "global_load_dwordx4 v[180:183], v8, s[44:45] offset:3072 sc1\n" \
  "global_load_dwordx4 v[184:187], v22, s[44:45] offset:0 sc1\n" \
  "s_add_u32 s44, s44, 163840\n" \
  "s_addc_u32 s45, s45, 0\n" \
  "s_waitcnt vmcnt(35)\n" \
  "s_barrier\n" \
  "s_mul_i32 %[t0], %[hw], 2880\n" \
  "s_add_u32 m0, %[t0], 23040\n" \
  "s_nop 0\n" \
  "global_load_lds_dwordx4 v0, s[36:37] sc1\n" \
  "global_load_lds_dwordx4 v1, s[36:37] offset:1024 sc1\n" \
  "s_mov_b32 exec_lo, 0xfffffcff\n" \
  "s_mov_b32 exec_hi, 0x3ffff\n" \
  "global_load_lds_dwordx4 v2, s[36:37] offset:2048 sc1\n" \
  "s_mov_b64 exec, -1\n" \
  "s_add_u32 s36, s36, 81920\n" \
  "s_addc_u32 s37, s37, 0\n" \
  "global_load_dwordx4 v[188:191], v8, s[44:45] offset:0 sc1\n" \
  "global_load_dwordx4 v[192:195], v8, s[44:45] offset:1024 sc1\n" \
  "global_load_dwordx4 v[196:199], v8, s[44:45] offset:2048 sc1\n" \
  "global_load_dwordx4 v[200:203], v8, s[44:45] offset:3072 sc1\n" \
  "global_load_dwordx4 v[204:207], v22, s[44:45] offset:0 sc1\n" \
  "s_add_u32 s44, s44, 163840\n" \
  "s_addc_u32 s45, s45, 0\n" \
  "global_load_dwordx4 v[208:211], v8, s[44:45] offset:0 sc1\n" \
  "global_load_dwordx4 v[212:215], v8, s[44:45] offset:1024 sc1\n" \
  "global_load_dwordx4 v[216:219], v8, s[44:45] offset:2048 sc1\n" \
  "global_load_dwordx4 v[220:223], v8, s[44:45] offset:3072 sc1\n" \
  "global_load_dwordx4 v[224:227], v22, s[44:45] offset:0 sc1\n" \
  "s_add_u32 s44, s44, 163840\n" \
  "s_addc_u32 s45, s45, 0\n" \
  "global_load_dwordx4 v[228:231], v8, s[44:45] offset:0 sc1\n" \
  "global_load_dwordx4 v[232:235], v8, s[44:45] offset:1024 sc1\n" \
  "global_load_dwordx4 v[236:239], v8, s[44:45] offset:2048 sc1\n" \
  "global_load_dwordx4 v[240:243], v8, s[44:45] offset:3072 sc1\n" \
  "global_load_dwordx4 v[244:247], v22, s[44:45] offset:0 sc1\n" \
  "s_add_u32 s44, s44, 163840\n" \
  "s_addc_u32 s45, s45, 0\n" \
  "s_waitcnt vmcnt(33)\n" \
  "s_barrier\n" \
  "s_mul_i32 %[t0], %[hw], 2880\n" \
  "s_add_u32 m0, %[t0], 34560\n" \
  "s_nop 0\n" \
  "global_load_lds_dwordx4 v0, s[36:37] sc1\n" \
  "global_load_lds_dwordx4 v1, s[36:37] offset:1024 sc1\n" \
  "s_mov_b32 exec_lo, 0xfffffcff\n" \
  "s_mov_b32 exec_hi, 0x3ffff\n" \
  "global_load_lds_dwordx4 v2, s[36:37] offset:2048 sc1\n" \
  "s_mov_b64 exec, -1\n" \
  "s_sub_u32 s36, s36, 240640\n" \
  "s_subb_u32 s37, s37, 0\n" \
  "s_cmp_lg_u32 %[hw], 3\n" \
  "s_cbranch_scc1 2f\n" \
  "s_mov_b64 exec, 0\n" \
  "s_branch .Lk_pad4\n" \
  ".Lk_ret4:\n" \
  "s_mov_b64 exec, -1\n" \
  "s_mov_b64 exec, 0\n" \
  "s_branch .Lk_pad5\n" \
  ".Lk_ret5:\n" \
  "s_mov_b64 exec, -1\n" \
  "2:\n" \
  "global_load_dword v9, v19, s[48:49] sc1\n" \
  "s_waitcnt vmcnt(19)\n" \
  "s_barrier\n" \
  "s_cmp_lg_u32 %[hw], 3\n" \
  "s_cbranch_scc1 2f\n" \
  "s_mov_b64 exec, 0\n" \
  "s_nop 0\n" \
  ".Lk_ret6:\n" \
  "s_mov_b64 exec, -1\n" \
  "s_mov_b64 exec, 0\n" \
  "s_nop 0\n" \
  ".Lk_ret7:\n" \
  "s_mov_b64 exec, -1\n" \
  "2:\n" \
  "s_waitcnt vmcnt(1)\n" \
  "s_barrier\n" \
  "s_barrier\n" \
  "s_setprio 0\n" \
  "s_sleep 8\n" \
  "s_sub_u32 s38, s44, 1884160\n" \
  "s_subb_u32 s39, s45, 0\n" \
  "s_mul_i32 %[t0], %[hw], 7168\n" \
  "s_add_u32 %[t0], %[t0], %[o6]\n" \
  "s_add_u32 %[t0], %[t0], 0\n" \
  "v_add_u32 v23, %[t0], v8\n" \
  "ds_write_b128 v23, v[32:35]\n" \
  "s_mul_i32 %[t0], %[hw], 7168\n" \
  "s_sub_u32 %[t0], %[t0], %[o1]\n" \
  "s_add_u32 %[t0], %[t0], 1024\n" \
  "v_add_u32 v23, %[t0], v8\n" \
  "ds_write_b128 v23, v[36:39] offset:0\n" \
  "ds_write_b128 v23, v[40:43] offset:1024\n" \
  "ds_write_b128 v23, v[44:47] offset:2048\n" \
  "ds_write_b128 v23, v[48:51] offset:3072\n" \
  "ds_write_b128 v23, v[52:55] offset:4096\n" \
  "ds_write_b128 v23, v[56:59] offset:5120\n" \
  "s_waitcnt lgkmcnt(0)\n" \
  "global_load_dwordx4 v[32:35], v8, s[44:45] offset:0 sc1\n" \
  "global_load_dwordx4 v[36:39], v8, s[44:45] offset:1024 sc1\n" \
  "global_load_dwordx4 v[40:43], v8, s[44:45] offset:2048 sc1\n" \
  "global_load_dwordx4 v[44:47], v8, s[44:45] offset:3072 sc1\n" \
  "global_load_dwordx4 v[48:51], v22, s[44:45] offset:0 sc1\n" \
  "s_add_u32 s44, s44, 163840\n" \
  "s_addc_u32 s45, s45, 0\n" \
  "v_add_f32 v4, v4, v24\n" \
  "v_add_f32 v5, v5, v25\n" \
  "v_add_f32 v6, v6, v26\n" \
  "v_add_f32 v7, v7, v27\n" \
  "s_mov_b32 exec_hi, 0\n" \
  "v_add_f32 v4, v4, v28\n" \
  "v_add_f32 v5, v5, v29\n" \
  "v_add_f32 v6, v6, v30\n" \
  "v_add_f32 v7, v7, v31\n" \
  "s_mov_b64 exec, -1\n" \
  "v_add_f32_dpp v4, v4, v4 row_ror:8 row_mask:0xf bank_mask:0xf\n" \
  "v_add_f32_dpp v5, v5, v5 row_ror:8 row_mask:0xf bank_mask:0xf\n" \
  "v_add_f32_dpp v6, v6, v6 row_ror:8 row_mask:0xf bank_mask:0xf\n" \
  "v_add_f32_dpp v7, v7, v7 row_ror:8 row_mask:0xf bank_mask:0xf\n" \
  "v_mov_b32 v24, v4\n" \
  "v_mov_b32 v25, v5\n" \
  "v_mov_b32 v26, v6\n" \
  "v_mov_b32 v27, v7\n" \
  "v_permlane16_swap_b32 v4, v24\n" \
  "v_permlane16_swap_b32 v5, v25\n" \
  "v_permlane16_swap_b32 v6, v26\n" \
  "v_permlane16_swap_b32 v7, v27\n" \
  "v_add_f32 v4, v4, v24\n" \
  "v_add_f32 v5, v5, v25\n" \
  "v_add_f32 v6, v6, v26\n" \
  "v_add_f32 v7, v7, v27\n" \
  "v_mov_b32 v24, v4\n" \
  "v_mov_b32 v25, v5\n" \
  "v_mov_b32 v26, v6\n" \
  "v_mov_b32 v27, v7\n" \
  "v_permlane32_swap_b32 v4, v24\n" \
  "v_permlane32_swap_b32 v5, v25\n" \
  "v_permlane32_swap_b32 v6, v26\n" \
  "v_permlane32_swap_b32 v7, v27\n" \
  "v_add_f32 v4, v4, v24\n" \
  "v_add_f32 v5, v5, v25\n" \
  "v_add_f32 v6, v6, v26\n" \
  "v_add_f32 v7, v7, v27\n" \
  "s_barrier\n" \
  "s_sleep 2\n" \
  "s_mul_i32 %[t0], %[hw], 7168\n" \
  "s_add_u32 %[t0], %[t0], %[o6]\n" \
  "s_add_u32 %[t0], %[t0], 49152\n" \
  "v_add_u32 v23, %[t0], v8\n" \
  "ds_write_b128 v23, v[60:63]\n" \
  "s_mul_i32 %[t0], %[hw], 7168\n" \
  "s_sub_u32 %[t0], %[t0], %[o1]\n" \
  "s_add_u32 %[t0], %[t0], 50176\n" \
  "v_add_u32 v23, %[t0], v8\n" \
  "ds_write_b128 v23, v[64:67] offset:0\n" \
  "ds_write_b128 v23, v[68:71] offset:1024\n" \
  "ds_write_b128 v23, v[72:75] offset:2048\n" \
  "ds_write_b128 v23, v[76:79] offset:3072\n" \
  "ds_write_b128 v23, v[80:83] offset:4096\n" \
  "ds_write_b128 v23, v[84:87] offset:5120\n" \
  "s_waitcnt lgkmcnt(0)\n" \
  "s_sleep 16\n" \
  "global_load_dwordx4 v[52:55], v8, s[44:45] offset:0 sc1\n" \
  "global_load_dwordx4 v[56:59], v8, s[44:45] offset:1024 sc1\n" \
  "global_load_dwordx4 v[60:63], v8, s[44:45] offset:2048 sc1\n" \
  "global_load_dwordx4 v[64:67], v8, s[44:45] offset:3072 sc1\n" \
  "global_load_dwordx4 v[68:71], v22, s[44:45] offset:0 sc1\n" \
  "s_add_u32 s44, s44, 163840\n" \
  "s_addc_u32 s45, s45, 0\n" \
  "s_barrier\n" \
  "s_mov_b64 exec, 0x7f\n" \
  "s_mul_i32 %[t0], %[hw], 112\n" \
  "s_add_u32 %[t0], %[t0], 28672\n" \
  "v_add_u32 v23, %[t0], v8\n" \
  "ds_write_b128 v23, v[4:7] offset:0\n" \
  "s_mov_b64 exec, -1\n" \
  "s_waitcnt lgkmcnt(0)\n" \
  "s_barrier\n" \
  "s_barrier\n" \
  "s_mul_i32 %[t0], %[hw], 5120\n" \
  "s_add_u32 m0, %[t0], 0\n" \
  "s_nop 0\n" \
  "global_load_lds_dwordx4 v8, s[38:39] offset:0 sc1\n" \
  "global_load_lds_dwordx4 v8, s[38:39] offset:1024 sc1\n" \
  "global_load_lds_dwordx4 v8, s[38:39] offset:2048 sc1\n" \
  "global_load_lds_dwordx4 v8, s[38:39] offset:3072 sc1\n" \
  "s_add_u32 m0, m0, 4096\n" \
  "s_nop 0\n" \
  "global_load_lds_dwordx4 v22, s[38:39] sc1\n" \
  "s_add_u32 s38, s38, 163840\n" \
  "s_addc_u32 s39, s39, 0\n" \
  "s_mul_i32 %[t0], %[hw], 5120\n" \
  "s_add_u32 m0, %[t0], 20480\n" \
  "s_nop 0\n" \
  "global_load_lds_dwordx4 v8, s[38:39] offset:0 sc1\n" \
  "global_load_lds_dwordx4 v8, s[38:39] offset:1024 sc1\n" \
  "global_load_lds_dwordx4 v8, s[38:39] offset:2048 sc1\n" \
  "global_load_lds_dwordx4 v8, s[38:39] offset:3072 sc1\n" \
  "s_add_u32 m0, m0, 4096\n" \
  "s_nop 0\n" \
  "global_load_lds_dwordx4 v22, s[38:39] sc1\n" \
  "s_add_u32 s38, s38, 163840\n" \
  "s_addc_u32 s39, s39, 0\n" \
  "s_mul_i32 %[t0], %[hw], 5120\n" \
  "s_add_u32 m0, %[t0], 40960\n" \
  "s_nop 0\n" \
  "global_load_lds_dwordx4 v8, s[38:39] offset:0 sc1\n" \
  "global_load_lds_dwordx4 v8, s[38:39] offset:1024 sc1\n" \
  "global_load_lds_dwordx4 v8, s[38:39] offset:2048 sc1\n" \
  "global_load_lds_dwordx4 v8, s[38:39] offset:3072 sc1\n" \
  "s_add_u32 m0, m0, 4096\n" \
  "s_nop 0\n" \
  "global_load_lds_dwordx4 v22, s[38:39] sc1\n" \
  "s_add_u32 s38, s38, 163840\n" \
  "s_addc_u32 s39, s39, 0\n" \
  "s_cmp_ge_u32 %[hw], 2\n" \
  "s_cbranch_scc1 5f\n" \
  "s_mul_i32 %[t0], %[hw], 5120\n" \
  "s_add_u32 m0, %[t0], 61440\n" \
  "s_nop 0\n" \
  "global_load_lds_dwordx4 v8, s[38:39] offset:0 sc1\n" \
  "global_load_lds_dwordx4 v8, s[38:39] offset:1024 sc1\n" \
  "global_load_lds_dwordx4 v8, s[38:39] offset:2048 sc1\n" \
  "global_load_lds_dwordx4 v8, s[38:39] offset:3072 sc1\n" \
  "s_add_u32 m0, m0, 4096\n" \
  "s_nop 0\n" \
  "global_load_lds_dwordx4 v22, s[38:39] sc1\n" \
  "s_add_u32 s38, s38, 163840\n" \
  "s_addc_u32 s39, s39, 0\n" \
  "5:\n" \
  "s_cmp_ge_u32 %[hw], 2\n" \
  "s_cbranch_scc1 5f\n" \
  "s_waitcnt vmcnt(5)\n" \
  "s_branch 6f\n" \
  "5:\n" \
  "s_waitcnt vmcnt(0)\n" \
  "6:\n" \
  "s_barrier\n" \
  "s_mul_i32 %[t0], %[hw], 5120\n" \
  "s_add_u32 %[t0], %[t0], 71680\n" \
  "v_add_u32 v23, %[t0], v8\n" \
  "ds_write_b128 v23, v[88:91] offset:0\n" \
  "ds_write_b128 v23, v[92:95] offset:1024\n" \
  "ds_write_b128 v23, v[96:99] offset:2048\n" \
  "ds_write_b128 v23, v[100:103] offset:3072\n" \
  "ds_write_b128 v23, v[104:107] offset:4096\n" \
  "s_mul_i32 %[t0], %[hw], 5120\n" \
  "s_add_u32 %[t0], %[t0], 92160\n" \
  "v_add_u32 v23, %[t0], v8\n" \
  "ds_write_b128 v23, v[108:111] offset:0\n" \
  "ds_write_b128 v23, v[112:115] offset:1024\n" \
  "ds_write_b128 v23, v[116:119] offset:2048\n" \
  "ds_write_b128 v23, v[120:123] offset:3072\n" \
  "ds_write_b128 v23, v[124:127] offset:4096\n" \
  "s_mul_i32 %[t0], %[hw], 5120\n" \
  "s_add_u32 %[t0], %[t0], 112640\n" \
  "v_add_u32 v23, %[t0], v8\n" \
  "ds_write_b128 v23, v[128:131] offset:0\n" \
  "ds_write_b128 v23, v[132:135] offset:1024\n" \
  "ds_write_b128 v23, v[136:139] offset:2048\n" \
  "ds_write_b128 v23, v[140:143] offset:3072\n" \
  "ds_write_b128 v23, v[144:147] offset:4096\n" \
  "s_waitcnt vmcnt(0) lgkmcnt(0)\n" \
  "s_barrier\n" \
  "s_mul_i32 %[t0], %[hw], 5120\n" \
  "s_add_u32 %[t0], %[t0], 0\n" \
  "v_add_u32 v23, %[t0], v8\n" \
  "ds_write_b128 v23, v[168:171] offset:0\n" \
  "ds_write_b128 v23, v[172:175] offset:1024\n" \
  "ds_write_b128 v23, v[176:179] offset:2048\n" \
  "ds_write_b128 v23, v[180:183] offset:3072\n" \
  "ds_write_b128 v23, v[184:187] offset:4096\n" \
  "s_mul_i32 %[t0], %[hw], 5120\n" \
  "s_add_u32 %[t0], %[t0], 20480\n" \
  "v_add_u32 v23, %[t0], v8\n" \
  "ds_write_b128 v23, v[188:191] offset:0\n" \
  "ds_write_b128 v23, v[192:195] offset:1024\n" \
  "ds_write_b128 v23, v[196:199] offset:2048\n" \
  "ds_write_b128 v23, v[200:203] offset:3072\n" \
  "ds_write_b128 v23, v[204:207] offset:4096\n" \
  "s_mul_i32 %[t0], %[hw], 5120\n" \
  "s_add_u32 %[t0], %[t0], 40960\n" \
  "v_add_u32 v23, %[t0], v8\n" \
  "ds_write_b128 v23, v[208:211] offset:0\n" \
  "ds_write_b128 v23, v[212:215] offset:1024\n" \
  "ds_write_b128 v23, v[216:219] offset:2048\n" \
  "ds_write_b128 v23, v[220:223] offset:3072\n" \
  "ds_write_b128 v23, v[224:227] offset:4096\n" \
  "s_mul_i32 %[t0], %[hw], 5120\n" \
  "s_add_u32 %[t0], %[t0], 133120\n" \
  "v_add_u32 v23, %[t0], v8\n" \
  "ds_write_b128 v23, v[148:151] offset:0\n" \
  "ds_write_b128 v23, v[152:155] offset:1024\n" \
  "ds_write_b128 v23, v[156:159] offset:2048\n" \
  "ds_write_b128 v23, v[160:163] offset:3072\n" \
  "ds_write_b128 v23, v[164:167] offset:4096\n" \
  "s_waitcnt lgkmcnt(0)\n" \
  "s_barrier\n" \
  "s_mul_i32 %[t0], %[hw], 5120\n" \
  "s_add_u32 %[t0], %[t0], 61440\n" \
  "v_add_u32 v23, %[t0], v8\n" \
  "ds_write_b128 v23, v[228:231] offset:0\n" \
  "ds_write_b128 v23, v[232:235] offset:1024\n" \
  "ds_write_b128 v23, v[236:239] offset:2048\n" \
  "ds_write_b128 v23, v[240:243] offset:3072\n" \
  "ds_write_b128 v23, v[244:247] offset:4096\n" \
  "s_mul_i32 %[t0], %[hw], 5120\n" \
  "s_add_u32 %[t0], %[t0], 81920\n" \
  "v_add_u32 v23, %[t0], v8\n" \
  "ds_write_b128 v23, v[32:35] offset:0\n" \
  "ds_write_b128 v23, v[36:39] offset:1024\n" \
  "ds_write_b128 v23, v[40:43] offset:2048\n" \
  "ds_write_b128 v23, v[44:47] offset:3072\n" \
  "ds_write_b128 v23, v[48:51] offset:4096\n" \
  "s_mul_i32 %[t0], %[hw], 5120\n" \
  "s_add_u32 %[t0], %[t0], 102400\n" \
  "v_add_u32 v23, %[t0], v8\n" \
  "ds_write_b128 v23, v[52:55] offset:0\n" \
  "ds_write_b128 v23, v[56:59] offset:1024\n" \
  "ds_write_b128 v23, v[60:63] offset:2048\n" \
  "ds_write_b128 v23, v[64:67] offset:3072\n" \
  "ds_write_b128 v23, v[68:71] offset:4096\n" \
  "s_waitcnt lgkmcnt(0)\n" \
  "s_barrier\n" \
  "s_mov_b32 m0, %[sm0]\n" \
  ".Lk_code_end:\n"
#define HELPER_CLOB "v32", "v33", "v34", "v35", "v36", "v37", "v38", "v39", "v40", "v41", "v42", "v43", "v44", "v45", "v46", "v47", "v48", "v49", "v50", "v51", "v52", "v53", "v54", "v55", "v56", "v57", "v58", "v59", "v60", "v61", "v62", "v63", "v64", "v65", "v66", "v67", "v68", "v69", "v70", "v71", "v72", "v73", "v74", "v75", "v76", "v77", "v78", "v79", "v80", "v81", "v82", "v83", "v84", "v85", "v86", "v87", "v88", "v89", "v90", "v91", "v92", "v93", "v94", "v95", "v96", "v97", "v98", "v99", "v100", "v101", "v102", "v103", "v104", "v105", "v106", "v107", "v108", "v109", "v110", "v111", "v112", "v113", "v114", "v115", "v116", "v117", "v118", "v119", "v120", "v121", "v122", "v123", "v124", "v125", "v126", "v127", "v128", "v129", "v130", "v131", "v132", "v133", "v134", "v135", "v136", "v137", "v138", "v139", "v140", "v141", "v142", "v143", "v144", "v145", "v146", "v147", "v148", "v149", "v150", "v151", "v152", "v153", "v154", "v155", "v156", "v157", "v158", "v159", "v160", "v161", "v162", "v163", "v164", "v165", "v166", "v167", "v168", "v169", "v170", "v171", "v172", "v173", "v174", "v175", "v176", "v177", "v178", "v179", "v180", "v181", "v182", "v183", "v184", "v185", "v186", "v187", "v188", "v189", "v190", "v191", "v192", "v193", "v194", "v195", "v196", "v197", "v198", "v199", "v200", "v201", "v202", "v203", "v204", "v205", "v206", "v207", "v208", "v209", "v210", "v211", "v212", "v213", "v214", "v215", "v216", "v217", "v218", "v219", "v220", "v221", "v222", "v223", "v224", "v225", "v226", "v227", "v228", "v229", "v230", "v231", "v232", "v233", "v234", "v235", "v236", "v237", "v238", "v239", "v240", "v241", "v242", "v243", "v244", "v245", "v246", "v247", "v248", "v249", "v250", "v251"

constexpr int WS_EPOCH = 0;
constexpr int WS_FL = 4096;
constexpr int WS_ST1 = WS_FL + 4 * 256 * 512;
constexpr int WS_ST2 = WS_ST1 + 2048 * 16;
constexpr int WS_CS = WS_ST2 + 8 * 2048 * 8;
constexpr int WS_RF = WS_CS + 4096;
constexpr int WS_C2 = WS_RF + 4096;
constexpr int WS_Q = WS_C2 + 139264;
constexpr int WS_KV = WS_Q + 16 * 64 * 2560;
constexpr int WS_O = WS_KV + 16 * 64 * 5120;
constexpr int WS_H = WS_O + 64 * 80 * 512;
constexpr int WS_U = WS_H + 64 * 80 * 512;
constexpr int WS_WT = WS_U + 64 * 216 * 512;
constexpr int WS_END = WS_WT + 5120 * 1024;
constexpr int L_RED3 = 28672, L_WO = 49152, L_XR = 151552, L_SM = 161792, L_RED5 = 4 * 30720, L_TOTAL = 163840;
constexpr int TB = 2880;
constexpr int SPIN = 1 << 18;

#define STAMP(i)
#define PROF_DECL
#define PROF_OUT
#define PROF_CLOB
#define PROF_PUT()
#define BAR() asm volatile("s_barrier" ::: "memory")
#define LBAR() asm volatile("s_waitcnt lgkmcnt(0)\n\ts_barrier" ::: "memory")
#define RLX __ATOMIC_RELAXED, __HIP_MEMORY_SCOPE_AGENT

__device__ __forceinline__ unsigned cvtpk(f32x2 v) { return __builtin_bit_cast(unsigned, __builtin_convertvector(v, bf16x2)); }
__device__ __forceinline__ float bflo(unsigned w) { return __uint_as_float(w << 16); }
__device__ __forceinline__ float bfhi(unsigned w) { return __uint_as_float(w & 0xffff0000u); }
__device__ __forceinline__ unsigned rfl(unsigned v) { return __builtin_amdgcn_readfirstlane(v); }
__device__ __forceinline__ int lane_id() {
  int l; asm volatile("v_mbcnt_lo_u32_b32 %0, -1, 0\n\tv_mbcnt_hi_u32_b32 %0, -1, %0" : "=v"(l)); __builtin_assume(l >= 0 && l < 64); return l;
}
template <int N> __device__ __forceinline__ void wait_vm() { asm volatile("s_waitcnt vmcnt(%0)" ::"i"(N) : "memory"); }
__device__ __forceinline__ u32x4 mk_srd(const char* p, unsigned bytes) {
  const uint64_t a = (uint64_t)p;
  return (u32x4){rfl((unsigned)a), rfl((unsigned)(a >> 32)) & 0xffffu, bytes, 0x00020000u};
}
__device__ __forceinline__ void st128(u32x4 v, u32x4 srd, unsigned voff, int soff) {
  asm volatile("buffer_store_dwordx4 %0, %1, %2, %3 offen sc1\n\ts_nop 1" ::"v"(v), "v"(voff), "s"(srd), "s"(soff) : "memory");
}
__device__ __forceinline__ f32x16 mfma32(u32x4 a, u32x4 b, f32x16 c) {
  return __builtin_amdgcn_mfma_f32_32x32x16_bf16(__builtin_bit_cast(bf16x8, a), __builtin_bit_cast(bf16x8, b), c, 0, 0, 0);
}
__device__ __forceinline__ f32x16 mfma32k8(u32x2 a, u32x2 b, f32x16 c) {
  return __builtin_amdgcn_mfma_f32_32x32x8bf16_1k(__builtin_bit_cast(s16x4, a), __builtin_bit_cast(s16x4, b), c, 0, 0, 0);
}
__device__ __forceinline__ f32x16 smfmac(u32x4 a, f32x8 b, f32x16 c) {
  return __builtin_amdgcn_smfmac_f32_32x32x32_bf16(__builtin_bit_cast(bf16x8, a), __builtin_bit_cast(bf16x16, b), c, 0xDDDD, 0, 0);
}
__device__ __forceinline__ float max16(const f32x16& s) {
  float a = fmaxf(fmaxf(s[0], s[1]), s[2]), b = fmaxf(fmaxf(s[3], s[4]), s[5]);
  a = fmaxf(fmaxf(a, s[6]), s[7]); b = fmaxf(fmaxf(b, s[8]), s[9]);
  a = fmaxf(fmaxf(a, s[10]), s[11]); b = fmaxf(fmaxf(b, s[12]), s[13]);
  a = fmaxf(fmaxf(a, s[14]), s[15]);
  return fmaxf(a, b);
}
__device__ __forceinline__ float x32_max(float m) {
  auto rr = __builtin_amdgcn_permlane32_swap(__float_as_uint(m), __float_as_uint(m), false, false);
  return fmaxf(__uint_as_float(rr[0]), __uint_as_float(rr[1]));
}
__device__ __forceinline__ void touch(const void* base, unsigned off, unsigned long long lanes) {
  unsigned keep;
  asm volatile("s_mov_b32 %0, m0\n\ts_mov_b32 m0, %4\n\ts_mov_b64 exec, %3\n\tglobal_load_lds_dword %1, %2\n\ts_mov_b64 exec, -1\n\ts_mov_b32 m0, %0"
               : "=&s"(keep) : "v"(off), "s"(base), "s"(lanes), "i"(L_SM + 1664));
}
__device__ __forceinline__ int w2pos(int ks) {
  const int pp = (ks * 9363) >> 16, q = ks - pp * 7;
  const int a = 4 * pp + q - (q >> 2) * 2, b = q == 6 ? 46 + pp : 28 + 2 * pp + q;
  return !CFG_EP || ks == 53 ? ks : (q & 2) ? b : a;
}
__device__ __forceinline__ unsigned flo(int seam, int grp, int rep) { return WS_FL + seam * 131072 + (grp * 16 + rep) * 128; }

__global__ void __launch_bounds__(512, 2) __attribute__((aligned(4096))) k_block(const char* __restrict__ x, const char* __restrict__ wqkv, const char* __restrict__ wo,
                                                  const char* __restrict__ w1, const char* __restrict__ w2, char* ws, char* out) {
  __shared__ __attribute__((aligned(16))) char smem[L_TOTAL];
  LDS_AS char* lds = (LDS_AS char*)smem;
  unsigned long long pc0; int clen;
  asm volatile("s_getpc_b64 %0\n.Lk_code_top:\n\ts_mov_b32 %1, .Lk_code_end-.Lk_code_top"
               : "=&s"(pc0), "=&s"(clen) : "s"(lds) : "memory");
  __builtin_amdgcn_sched_barrier(0);
  const int w = rfl(threadIdx.x >> 6);
  const int xcd = blockIdx.x & 7, bi = blockIdx.x >> 3;
  const int rb1 = 4 * (xcd >> 1) + (bi & 3), nb1 = CFG_LS ? 2 * (bi >> 2) + (xcd & 1) : 8 * (xcd & 1) + (bi >> 2);
  const int head = xcd * 4 + (bi >> 3), qb = bi & 7, odd = head & 1;
  const int rb3 = 8 * (xcd >> 1) + (bi & 7), nb3 = 4 * (xcd & 1) + (bi >> 3);
  const int rb4 = rb1, nb4 = 8 * (xcd & 1) + (bi >> 2);
  const int rot4 = CFG_EP ? nb4 & 1 : 0;
  LDS_AS float* f_sm = (LDS_AS float*)(lds + L_SM);
  PROF_DECL
  STAMP(0);
  touch((const void*)pc0, max(min((int)(((bi * 8 + w) * 2 + (threadIdx.x & 1)) * 128), clen - 4), 0), 3);

  if (CFG_PRE & 16) {
    const int l = threadIdx.x & 63, wi = bi * 8 + w;
    touch(wqkv, ((xcd >> 1) * 160 + min(wi, 159)) * 3840 + min((l & 14) * 256 + (xcd & 1) * 256 + (l & 1) * 128, 3712), wi < 160 ? 0xffffull : 1ull);
    touch(x, (rb1 * 128 + nb1 * 8 + w) * 1280 + min(l, 9) * 128, 0x3ffull);
  }
  if ((CFG_PRE & 32) && w < 4) {
    const int l = threadIdx.x & 63;
    touch(wqkv, min(((xcd >> 1) * 160 + (bi * 4 + w) * 2 + (l >> 4)) * 3840 + min((l & 14) * 256 + (xcd & 1) * 256 + (l & 1) * 128, 3712), 2457596), bi < 20 ? 0xffffffffull : 1ull);
    touch(x, (rb1 * 128 + nb1 * 8 + w * 2) * 1280 + min(l, 19) * 128, 0xfffffull);
  }
  if ((CFG_PRE & 64) && w < 4) {
    const int l = threadIdx.x & 63, id = w * 20 + min(l, 19);
    touch(wqkv, ((bi * 5 + (id >> 4)) * 4 + (xcd >> 1)) * 3840 + min((id & 14) * 256 + (xcd & 1) * 256 + (id & 1) * 128, 3712), 0xfffffull);
    touch(x, (rb1 * 128 + nb1 * 8 + w * 2) * 1280 + min(l, 19) * 128, 0xfffffull);
  }
  auto p1_offsets = [&](int lane) {
    u32x8 iv;
#pragma unroll
    for (int i = 0; i < 4; ++i) {
      const int row = 8 * i + (lane >> 3), kr = 4 * i + (lane >> 4);
      iv[i] = row * 1280 + (((lane & 7) ^ ((row >> 1) & 7)) << 4) - 1024 * i;
      iv[4 + i] = kr * 3840 + (((lane & 15) ^ ((kr & 3) << 2)) << 4) - 1024 * i;
    }
    return iv;
  };
  if (w >= 4) {
    const int hw = w - 4, lane = lane_id();
    unsigned tag;
    {
      const u32x8 iv = p1_offsets(lane);
      const int h = lane >> 5, r = lane & 31;
      const unsigned go = WS_ST1 + ((rb1 * 128 + hw * 32 + r) * 2 + h) * 8;
      const unsigned gc = WS_CS + (nb1 < 15 ? nb1 * 128 + (lane & 3) * 4 : rb1 * 128 + hw * 4);
      const u32x4 iw = {go, gc, (unsigned)WS_EPOCH, flo(0, rb1 + (lane & 4) * 4, hw * 4 + (lane & 3))};
      const char* xs = x + (rb1 * 128 + hw * 32) * 1280;
      const char* wsrc = wqkv + hw * 16 * 3840 + nb1 * 256;
      const int u = blockIdx.x * 4 + hw;
      const char* pw = (u < 160 ? wo : u < 592 ? w1 - 160 * 5120 : w2 - 592 * 5120) + u * 5120;
      const int id = hw * 20 + min(lane, 19);
      const u32x2 tv = {(unsigned)(((bi * 5 + (id >> 4)) * 4 + (xcd >> 1)) * 3840 + min((id & 14) * 256 + (xcd & 1) * 256 + (id & 1) * 128, 3712)),
                        (unsigned)((rb1 * 128 + nb1 * 8) * 1280 + id * 128)};
      unsigned long long gv; unsigned gw, ep, sm;
      asm volatile(HELPER1_ASM : "={v[12:13]}"(gv), "={v14}"(gw), "={v16}"(ep), "+{s[36:37]}"(xs), "+{s[38:39]}"(wsrc), [sm0] "=&s"(sm) PROF_OUT
                   : "{v[0:7]}"(iv), "{v[8:11]}"(iw), [l0] "s"(hw * 4096), [ws] "s"(ws), [nb] "s"(nb1), [pw] "s"(pw), "{v[18:19]}"(tv), [wq] "s"(wqkv), [x] "s"(x) : "s64", "s65", "v20", "scc", "memory" PROF_CLOB);
      tag = rfl(ep) + 1u;
      *(LDS_AS unsigned*)(lds + L_SM + 1536 + hw * 4) = tag;
      if (nb1 < 15) {
#pragma unroll 1
        for (int s = 0; s < SPIN && !__all((unsigned)(gv >> 32) == tag && gw == tag); ++s) {
          gv = __hip_atomic_load((GLB_AS unsigned long long*)(ws + go), RLX); gw = __hip_atomic_load((GLB_AS unsigned*)(ws + gc), RLX);
        }
        f_sm[h * 128 + hw * 32 + r] = __uint_as_float((unsigned)gv) * (nb1 < 5 && !h ? 0.22360679774997896f * 1.4426950408889634f : 1.0f);
        __builtin_amdgcn_s_waitcnt(0x0F70);
        STAMP(51);
        LBAR();
      }
    }
  }
  int w_ = w; asm volatile("" : "+s"(w_));
  if (w_ >= 4) {
    const int hw = w - 4, lane = lane_id();
    const unsigned tag = rfl(*(LDS_AS unsigned*)(lds + L_SM + 1536 + hw * 4));
    if (hw == 3) { const unsigned c = (lane & 1) ? 0u : 0x3f803f80u; *(LDS_AS u32x4*)(lds + (lane >> 2) * TB + 1536 + 640 + ((lane >> 1) & 1) * 672 + (lane & 1) * 16) = (u32x4){c, c, c, c}; }
    u32x16 ia, ib;
    const int u1 = blockIdx.x * 4 + hw, u2 = 284 + blockIdx.x, rstr = (u1 >= 200 && u1 < 740) ? 3456 : 1280;
    const bool isw1 = rstr != 1280, isw2 = u1 >= 740;
    const int v1 = u1 - (isw2 ? 740 : isw1 ? 200 : 0), ks1 = (v1 * (isw1 ? 2428 : 6554)) >> 16, cb1 = v1 - ks1 * (isw1 ? 27 : 10);
    const int ks2 = (u2 * 6554) >> 16, cb2 = u2 - ks2 * 10;
    const char* pr1 = (isw2 ? w2 : isw1 ? w1 : wo) + (ks1 * 32 * rstr + cb1 * 128);
    const char* pr2 = w2 + (ks2 * 40960 + cb2 * 128);
    const int d1 = isw2 ? 740 + w2pos(ks1) * 10 + cb1 : u1, d2 = 740 + w2pos(ks2) * 10 + cb2;
    {
      const unsigned kb = odd * 1024, vb = 2560 + odd * 1280;
      ia[0] = kb + lane * 16; ia[1] = lane < 32 ? ia[0] : vb + (lane - 32) * 16 - 1024; ia[2] = vb + (lane < 8 ? 32 + lane : lane < 50 ? 30 + lane : 0) * 16 - 2048;
#pragma unroll
      for (int i = 0; i < 5; ++i) { const int id = 64 * i + lane, p = id / 10; ia[3 + i] = p * 1280 + (id - p * 10) * 16 - 1024 * (i & 3); }
      ia[8] = lane * 16;
      ia[9] = (lane >> 3) * rstr + (lane & 7) * 16;
      ia[10] = (lane >> 3) * 1280 + (lane & 7) * 16;
      ia[11] = L_WO + hw * 4096 + (8 * (lane >> 4) + ((lane & 15) >> 2)) * 128 + (lane & 3) * 8;
      ia[12] = tag;
      ia[13] = tag;
      ia[14] = ia[0] + (lane < 32 ? 0 : 1024);
      ib[1] = WS_RF + u1 * 4;
      ib[2] = WS_RF + hw * 1024 + lane * 16;
      const int id = min((bi * 4 + hw) * 28 + min(lane, 27), 3519), ks = id / 160;
      ib[3] = WS_WT + (2960 + (ks < 14 ? ks : ks + 32) * 40 + (xcd & 1) * 20) * 1024 + (id - ks * 160) * 128;
#pragma unroll
      for (int i = 6; i < 16; ++i) ib[i] = lane;
      ib[5] = flo(1 + min(lane, 2), 0, u1);
      ib[13] = WS_C2 + max(u1 - 200, 0) * 256 + lane; ib[14] = 0;
      ib[15] = WS_C2 + (lane >> 3) * 6912 + nb4 * 448 + hw * 112 + min(lane & 7, 6) * 16;
    }
    unsigned long long pm;
    {
      ia[15] = lane;
      const int gI = min(lane, 5), ty = gI >> 1, lo = ty == 2 ? 20 * head : 20 * head - 4 * odd, hi = ty == 2 ? lo + 19 : lo + 23;
      ib[0] = flo(0, ty * 5 + (((gI & 1) ? hi : lo) >> 7), ty ? (qb + 8 * odd) % CFG_R0 : qb + 8 * odd);
      pm = __ballot(lane < 6 && (!(lane & 1) || (hi >> 7) != (lo >> 7))) & (CFG_QE ? (hw < 2 ? 0x3c : 3) : 0x3f);
      ib[4] = (tag << 8) + (ty ? 16 : 2);
    }
    const char* pkv = ws + WS_KV + ((head >> 1) * 64 + (hw << CFG_LS)) * 5120;
    const char* pwo = ws + WS_WT + (hw * 40 + nb3 * 5) * 1024;
    const char* pxr = x + (rb3 * 64 + (hw & 1) * 32) * 1280 + nb3 * 160;
    const char* pw1 = ws + WS_WT + (800 + hw * 108 + nb4 * 7) * 1024;
    const char* pw2 = ws + WS_WT + (2960 + (14 + hw) * 40 + nb3 * 5) * 1024;
    const char* pq = ws + WS_Q + ((head >> 1) * 64 + qb * 8 + (hw & 1) * 4) * 2560;
    unsigned t0, sm0;
    asm volatile(HELPER_ASM
                 : "+{v[0:15]}"(ia), "+{v[16:31]}"(ib), "+{s[36:37]}"(pkv), "+{s[38:39]}"(pwo), "+{s[42:43]}"(pw1), "+{s[44:45]}"(pw2), "+{s[46:47]}"(pr1),
                   "+{s[52:53]}"(pr2), "+{s[58:59]}"(pq), [t0] "=&s"(t0), [sm0] "=&s"(sm0) PROF_OUT
                 : "{s[40:41]}"(pxr), "{s[48:49]}"(ws), "{s[54:55]}"(ws + WS_WT + d1 * 4096), "{s[56:57]}"(ws + WS_WT + d2 * 4096), [hw] "s"(hw),
                   [pm] "s"(pm), [o1] "s"(rot4 * 1024), [o6] "s"(rot4 * 6144), [u1] "s"(u1), [rinc] "s"(8 * rstr - 1024)
                 : HELPER_CLOB, "scc", "vcc", "memory" PROF_CLOB);
    PROF_PUT();
  } else {
    const u32x4 wsrd = mk_srd(ws, WS_END);
    unsigned cnt, t0;
    u32x16 k0, k1, k2, k3, k4; u32x4 k5;
    {
      const int lane = lane_id(), r = lane & 31, h = lane >> 5, wm = w & 1, wn = w >> 1;
      if (CFG_PRE & 1) touch(wqkv, min(((xcd >> 1) * 160 + (bi * 4 + w) * 2 + (lane >> 4)) * 3840 + (CFG_LS ? min((lane & 14) * 256 + (xcd & 1) * 256 + (lane & 1) * 128, 3712) : (xcd & 1) * 1920 + min(lane & 15, 14) * 128), 2457596), bi < 20 ? 0xffffffffull : 1ull);
      if (CFG_PRE & 2) touch(wqkv, (192 + min(bi * 4 + w, 111) * 4 + (xcd >> 1)) * 3840 + min((lane & 14) * 256 + (xcd & 1) * 256 + (lane & 1) * 128, 3712), bi < 28 ? 0xffffull : 1ull);
      if (CFG_PRE & 8) touch(x, (rb1 * 128 + nb1 * 8 + w * 2) * 1280 + min(lane, 19) * 128, 0xfffffull);
      const bool vori = nb1 >= 10, isq = nb1 < 5;
      const unsigned x0 = (wm * 64 + r) * 128 + ((h ^ ((r >> 1) & 7)) << 4);
      const int q = (lane & 15) >> 2, p = lane & 3, blk = (lane >> 4) & 1, P = vori ? p : ((p >> 1) | ((p & 1) << 1));
      const unsigned wo0 = 16384 + (8 * h + q) * 256 + (((8 * w + 4 * blk + P) * 8) ^ (q << 6));
      const unsigned a4 = L_SM + (vori ? wm * 256 : 1024 + w * 128) + h * 16, a1 = L_SM + (vori ? 1024 + w * 128 : wm * 256) + r * 4;
      const u32x4 in2 = {(vori ? a1 : a4) ^ 128, (unsigned)(L_SM + 1024 + w * 128 + lane * 4), (((lane >> 4) ^ lane) & 1) ? 0u : 0x3f803f80u,
                         flo(0, nb1, isq ? (rb1 >> 1) + 8 * (lane & 1) : lane & 15)};
      u32x8 in = {x0, x0 ^ 32, x0 ^ 64, x0 ^ 96, wo0, wo0 ^ 64, a4, a1};
      u32x4 sto;
      const int tstr = isq ? 2560 : 5120;
      if (vori) {
#pragma unroll
        for (int i = 0; i < 2; ++i) {
          const int n = (nb1 - 10) * 128 + (w ^ i) * 32 + r, hd = n / 20, d = n - hd * 20;
          sto[2 * i] = (hd >> 1) * (64 * 5120) + 2560 + (hd & 1) * 1280 + (h * 20 + d) * 16; sto[2 * i + 1] = sto[2 * i] + 640;
        }
      } else {
        const int pos = isq ? r : ((r & 19) | ((r & 4) << 1) | ((r & 8) >> 1));
#pragma unroll
        for (int i = 0; i < 4; ++i) { const int cg = (isq ? nb1 : nb1 - 5) * 16 + (w * 4 ^ 2 * i) + h, pr = cg / 5; sto[i] = pr * 64 * tstr + (cg - pr * 5) * 512 + pos * 16; }
      }
      const int so0 = (isq ? WS_Q : WS_KV) + (rb1 * 4 + 2 * wm) * tstr, so1 = so0 + tstr;
      const u32x4 csrd = mk_srd(x + (rb1 * 128 + w * 32) * 1280, 32 * 1280);
      if (nb1 == 15) { in[0] = (lane >> 4) * 1280 + (lane & 15) * 16; in[1] = WS_ST1 + (rb1 * 128 + w * 32 + (lane >> 4)) * 16 + (lane & 1) * 8; in[2] = (rb1 * 4 + w) * 32768 + lane * 128; }
      if (CFG_PRE & 4) { asm volatile("" : "+{v[96:103]}"(in), "+{v[104:107]}"(sto)); __builtin_amdgcn_sched_barrier(0); touch(wqkv, (192 + min(bi * 4 + w, 111) * 4 + (xcd >> 1)) * 3840 + min((lane & 14) * 256 + (xcd & 1) * 256 + (lane & 1) * 128, 3712), bi < 28 ? 0xffffull : 1ull); }
      const int u = blockIdx.x * 4 + w;
      const char* pw = (u < 160 ? wo : u < 592 ? w1 - 160 * 5120 : w2 - 592 * 5120) + u * 5120;
      asm volatile(P1_ASM : "+{v[96:103]}"(in), [cnt] "=&s"(cnt) PROF_OUT
                   : "{v[104:107]}"(sto), "{v[108:111]}"(in2), [srd] "s"(wsrd), [so0] "s"(so0), [so1] "s"(so1), [nb] "s"(nb1), [lw] "s"(isq ? 0 : wm), [act] "s"(isq ? 3ull : (1ull << CFG_R0) - 1), [csrd] "s"(csrd), [pw] "s"(pw), [ps] "s"(w2) : P1_CLOB, "scc", "vcc", "memory" PROF_CLOB);
    }
    {
      const int ln = lane_id();
      const int r = ln & 31, h = ln >> 5;
      const unsigned o0 = odd * 1536 + ln * 16, o1 = 1024 + r * 16 + odd * 8;
      const int vd = r < 16 ? ((r & 3) | ((r & 4) << 1) | ((r & 8) >> 1)) + 4 * odd : (odd ? r - 16 : r);
      const unsigned ak0 = odd * 512 + ln * 16, ak1 = h ? 1536 + 640 : (odd ? 8 : 1024) + r * 16;
      const unsigned av = 1536 + (r < 20 ? h * 20 + vd : r == 20 ? 40 : 41) * 16;
      const u32x8 in = {ak0, ak1, av, o0, o1, (unsigned)ln * 16, flo(0, 16 + nb1, ln & 15), o1};
      const int qs = WS_Q + ((head >> 1) * 64 + qb * 8 + w * 2) * 2560;
      const int os0 = WS_O + (qb * 8 + w * 2) * (80 * 512) + (head >> 1) * 2560, os1 = os0 + 80 * 512;
      asm volatile(P2_ASM : "={v[164:179]}"(k0), "={v[180:195]}"(k1), "={v[196:211]}"(k2), "={v[212:227]}"(k3), "={v[228:243]}"(k4), "={v[244:247]}"(k5), [cnt] "=&s"(cnt), [t0] "=&s"(t0) PROF_OUT
                   : "{v[136:143]}"(in), [lact] "s"(nb1 >= 5 && nb1 < 15 ? (1ull << CFG_R0) - 1 : 0ull), [srd] "s"(wsrd), [qs] "s"(qs), [ql] "s"(98304 + w * 3072), [os0] "s"(os0), [os1] "s"(os1), [w1s] "s"(WS_WT + (800 + (8 + w) * 108 + nb4 * 7) * 1024) : P2_CLOB, "scc", "vcc", "memory" PROF_CLOB);
      if (__builtin_expect(t0 != 0, 0)) {
        const auto rs = __builtin_amdgcn_make_buffer_rsrc(ws, 0, WS_END, 0x00020000);
        const f32x16 zero = {};
        const int kvs = WS_KV + (head >> 1) * 64 * 5120;
        const unsigned gv = 2560 + odd * 1280 + (h * 20 + (r < 20 ? vd : 0)) * 16, cv = r == 20 ? 0x3f803f80u : 0u;
        wait_vm<0>();
#pragma unroll 1
        for (int i = 0; i < 2; ++i) {
          const u32x4 q0 = __builtin_amdgcn_raw_buffer_load_b128(rs, o0, qs + i * 2560, 16);
          u32x2 q1 = __builtin_amdgcn_raw_buffer_load_b64(rs, o1, qs + i * 2560, 16);
          if (h) q1 = (u32x2){0, 0};
          float tm = -INFINITY;
          f32x16 o = zero;
#pragma unroll 1
          for (int t = 0; t < 128; ++t) {
            const int so = kvs + (t & 63) * 5120;
            const u32x4 a = __builtin_amdgcn_raw_buffer_load_b128(rs, o0, so, 16);
            const u32x2 b = __builtin_amdgcn_raw_buffer_load_b64(rs, o1, so, 16);
            f32x16 s = mfma32k8((u32x2){h ? 0u : b[0], h ? 0u : b[1]}, q1, mfma32(a, q0, zero));
            if (t < 64) {
              tm = fmaxf(tm, max16(s));
              if (t == 63) tm = x32_max(tm);
            } else {
              u32x4 v0 = __builtin_amdgcn_raw_buffer_load_b128(rs, gv, so, 16), v1 = __builtin_amdgcn_raw_buffer_load_b128(rs, gv, so + 640, 16);
              if (r >= 20) { v0 = (u32x4){cv, cv, cv, cv}; v1 = v0; }
#pragma unroll
              for (int e = 0; e < 16; ++e) s[e] = __builtin_amdgcn_exp2f(s[e] - tm);
              o = smfmac(v0, __builtin_shufflevector(s, s, 0, 1, 2, 3, 4, 5, 6, 7), o);
              o = smfmac(v1, __builtin_shufflevector(s, s, 8, 9, 10, 11, 12, 13, 14, 15), o);
            }
          }
          auto lr = __builtin_amdgcn_permlane32_swap(__float_as_uint(o[8]), __float_as_uint(o[8]), false, false);
          const float inv = __builtin_amdgcn_rcpf(__uint_as_float(lr[1]));
          unsigned pk[6];
#pragma unroll
          for (int e = 0; e < 6; ++e) pk[e] = cvtpk((f32x2){o[2 * e] * inv, o[2 * e + 1] * inv});
          st128((u32x4){pk[0], pk[1], pk[2], pk[3]}, wsrd, o0, os0 + i * (80 * 512));
          if (h == 0) __builtin_amdgcn_raw_buffer_store_b64((u32x2){pk[4], pk[5]}, rs, o1, os0 + i * (80 * 512), 16);
        }
      }
    }
    f32x16 hk0; f32x4 hk1;
    const int wm = w & 1, kp = w >> 1;
    {
      const int ln = lane_id();
      const int r16 = ln & 15, g = ln >> 4, tl = wm * 32 + kp * 16 + r16;
      const unsigned va = g * 512 + r16 * 16 + kp * 256;
      const u32x8 in = {(unsigned)(L_WO + ln * 16 + (CFG_NS3 ? 0 : kp * 5120)), va, va ^ 256, flo(1, rb3, (nb3 * 2 + wm) % CFG_R1), (unsigned)(L_RED3 + w * 5120 + ln * 16),
                        (unsigned)(L_RED3 + (w ^ 2) * 5120 + ln * 16), (unsigned)((2 * (g & 1) + (g >> 1)) * 512 + r16 * 16), (unsigned)(L_XR + tl * 160 + g * 8)};
      const u32x4 in2 = {(unsigned)(WS_ST2 + nb3 * 16384 + (rb3 * 64 + tl) * 8),
                         flo(1, qb * 4 + w, r16), 1u, 1u};
      const int ab = WS_O + (rb3 * 2 + wm) * (80 * 512) + (CFG_NS3 ? 0 : kp * 2048), hs = WS_H + ((rb3 * 2 + wm) * 80 + nb3 * 10) * 512 + kp * 256;
      asm volatile(P3_ASM : "={v[0:15]}"(hk0), "={v[16:19]}"(hk1), "+{v[164:179]}"(k0), "+{v[180:195]}"(k1), "+{v[196:211]}"(k2), "+{v[212:227]}"(k3), "+{v[228:243]}"(k4), "+{v[244:247]}"(k5), [cnt] "=&s"(cnt), [so] "=&s"(t0) PROF_OUT
                   : "{v[116:123]}"(in), "{v[124:127]}"(in2), [srd] "s"(wsrd), [ab] "s"(ab), [hs] "s"(hs), [fl] "s"(ws)
                   : P3_CLOB, "scc", "vcc", "memory" PROF_CLOB);
    }
    {
      const int ln = lane_id();
      const int r16 = ln & 15, g = ln >> 4, tt = rb4 * 4 + w;
      const u32x8 in = {(unsigned)ln * 16, (unsigned)(g * 512 + r16 * 16), flo(2, tt, nb4 % CFG_R2), (unsigned)(g * 32768 + r16 * 8),
                        (unsigned)(L_RED3 + g * 16 + rot4 * 64), (unsigned)ln * 16, flo(2, rb3 * 2 + wm, r16), 1u};
      const u32x4 usrd = mk_srd(ws + WS_U + (tt * 216 + nb4 * 14) * 512, nb4 == 15 ? 3072 : 7168);
      const int ab = WS_H + tt * (80 * 512), st0 = WS_ST2 + tt * 256, st1 = st0 + 16384;
      asm volatile(P4_ASM : "+{v[0:15]}"(hk0), "+{v[16:19]}"(hk1), "+{v[164:179]}"(k0), "+{v[180:195]}"(k1), "+{v[196:211]}"(k2), "+{v[212:227]}"(k3), "+{v[228:243]}"(k4), "+{v[244:247]}"(k5), [cnt] "=&s"(cnt), [so] "=&s"(t0) PROF_OUT
                   : "{v[104:111]}"(in), "{v[248:249]}"((u32x2){(unsigned)(L_RED3 + g * 16 - rot4 * 384), flo(3, tt, r16)}), [da] "s"(L_WO + (4 + w) * 7168 - rot4 * 1024), [da0] "s"(L_WO + (4 + w) * 7168 + rot4 * 6144), [srd] "s"(wsrd), [usrd] "s"(usrd), [ab] "s"(ab), [st0] "s"(st0), [st1] "s"(st1), [us0] "s"(rot4 * 1024), [us1] "s"(4096 + rot4 * 1024), [us2] "s"(6144 - rot4 * 6144),
                     [fl] "s"(ws)
                   : P4_CLOB, "scc", "vcc", "memory" PROF_CLOB);
    }
    {
      const int ln = lane_id();
      const int r16 = ln & 15, g = ln >> 4, tl = wm * 32 + kp * 16 + r16;
      const unsigned va = g * 512 + r16 * 16 + kp * 256;
      const u32x8 in = {(unsigned)(ln * 16 + kp * 5120), va, va ^ 256, flo(3, rb3 * 2 + wm, (nb3 * 2 + kp) % CFG_R3), (unsigned)(L_RED5 + w * 5120 + ln * 16),
                        (unsigned)(L_RED5 + (w ^ 2) * 5120 + ln * 16), (unsigned)((rb3 * 64 + tl) * 1280 + (nb3 * 10 + 2 * (g & 1) + (g >> 1)) * 16), (unsigned)ln};
      const u32x4 in2 = {1u, flo(3, rb4 * 4 + w, r16), 1u, 1u};
      const int ab = WS_U + (rb3 * 2 + wm) * (216 * 512) + kp * 2048;
      asm volatile(P5_ASM : "+{v[0:15]}"(hk0), "+{v[16:19]}"(hk1), [cnt] "=&s"(cnt), [so] "=&s"(t0) PROF_OUT
                   : "{v[116:123]}"(in), "{v[124:127]}"(in2), [srd] "s"(wsrd), [ab] "s"(ab), [ab7] "s"(ab + kp * 12288), [ab5] "s"(ab + kp * 8192), [out] "s"(out), [fl] "s"(ws)
                   : P5_CLOB, "scc", "vcc", "memory" PROF_CLOB);
    }
    PROF_PUT();
  }
}

extern "C" void kernel_launch(void* const* d_in, const int* in_sizes, int n_in, void* d_out, int out_size, void* d_ws, size_t ws_size,
                              hipStream_t stream) {
  k_block<<<256, 512, 0, stream>>>((const char*)d_in[0], (const char*)d_in[1], (const char*)d_in[2], (const char*)d_in[4], (const char*)d_in[5],
                                   (char*)d_ws + (CFG_WOFF ? (ws_size - WS_END) & ~(size_t)65535 : 0), (char*)d_out);
}
```
